```python
import torch

def _softmax(x, axis=-1):
    return torch.softmax(x, dim=axis)
def _gelu(x):
    return torch.nn.functional.gelu(x, approximate="tanh")
def _silu(x):
    return torch.nn.functional.silu(x)
def _sigmoid(x):
    return torch.sigmoid(x)
def _layernorm(x, axis=-1, eps=1e-5):
    m = x.mean(dim=axis, keepdim=True)
    v = x.var(dim=axis, keepdim=True, unbiased=False)
    return (x - m) / torch.sqrt(v + eps)
def _scatter_add(tgt, idx, src, axis=0):
    return tgt.clone().index_add_(axis, idx, src)
def _segment_sum(src, seg, n):
    out = torch.zeros((n,) + src.shape[1:], dtype=src.dtype)
    return out.index_add_(0, seg, src)
def _pad(x, pw):
    flat = tuple(v for pair in reversed(pw) for v in pair)
    return torch.nn.functional.pad(x, flat)

kernel_name = 'recurrent_scan__gqa_block_cached__swiglu_ffn__decode'

def setup_inputs():

    _w = (lambda a: (lambda t: t.to(torch.bfloat16) if t.is_floating_point() else t)(torch.as_tensor(a)))
    def _A_setup():
        B, Sq, D = 1, 1, 576
        Hq, Hkv, Sk, Dh = 32, 16, 65536, 128
        assert Hq % Hkv == 0, (Hq, Hkv)
        return {
            "x": _w((lambda *s: torch.randn(*s, dtype=torch.bfloat16) * 0.05)(B, Sq, D)),
            "k_cache": _w((lambda *s: torch.randn(*s, dtype=torch.bfloat16) * 1.6)(B, Hkv, Sk, Dh)),
            "v_cache": _w((lambda *s: torch.randn(*s, dtype=torch.bfloat16) * 0.2)(B, Hkv, Sk, Dh)),
            "wq": _w((lambda *s: torch.randn(*s, dtype=torch.bfloat16) * 1.6)(D, Hq * Dh) * (D ** -0.5)),
            "wo": _w((lambda *s: torch.randn(*s, dtype=torch.bfloat16) * 0.2)(Hq * Dh, D) * ((Hq * Dh) ** -0.5)),
        }
    _A_in = _A_setup()

    def _B_setup():
        B, S, D, F = 1, 1, 576, 4608
        return {
            "x": _w((lambda *s: torch.randn(*s, dtype=torch.bfloat16) * 0.05)(B, S, D)),
            "w_gate": _w((lambda *s: torch.randn(*s, dtype=torch.bfloat16) * 0.1)(D, F)),
            "w_up": _w((lambda *s: torch.randn(*s, dtype=torch.bfloat16) * 0.1)(D, F)),
            "w_down": _w((lambda *s: torch.randn(*s, dtype=torch.bfloat16) * 0.1)(F, D)),
        }
    _B_in = _B_setup()

    return {
        "A_x": _A_in["x"],
        "A_k_cache": _A_in["k_cache"],
        "A_v_cache": _A_in["v_cache"],
        "A_wq": _A_in["wq"],
        "A_wo": _A_in["wo"],
        "B_x": _B_in["x"],
        "B_w_gate": _B_in["w_gate"],
        "B_w_up": _B_in["w_up"],
        "B_w_down": _B_in["w_down"],
    }

def reference(A_x, A_k_cache, A_v_cache, A_wq, A_wo, B_x, B_w_gate, B_w_up, B_w_down):
    def _A_ref(x, k_cache, v_cache, wq, wo):
        B, Sq, D = x.shape
        Hq, Dh = 32, 128
        Hkv = k_cache.shape[1]
        rep = Hq // Hkv
        q = (lambda x, *d: x.permute(*d))((x @ wq).reshape(B, Sq, Hq, Dh), 0, 2, 1, 3)
        k = (lambda x, n, axis: torch.repeat_interleave(x, n, dim=axis))(k_cache, rep, axis=1)
        v = (lambda x, n, axis: torch.repeat_interleave(x, n, dim=axis))(v_cache, rep, axis=1)
        scores = torch.einsum("bhqd,bhkd->bhqk", q, k) * (Dh ** -0.5)
        p = _softmax(scores, axis=-1)
        o = torch.einsum("bhqk,bhkd->bhqd", p, v)
        return (lambda x, *d: x.permute(*d))(o, 0, 2, 1, 3).reshape(B, Sq, Hq * Dh) @ wo

    def _B_ref(x, w_gate, w_up, w_down):
        g = _silu(x @ w_gate)
        u = x @ w_up
        return (g * u) @ w_down

    x = A_x
    xn = _layernorm(x, axis=-1)
    s = _A_ref(xn, A_k_cache, A_v_cache, A_wq, A_wo)
    g = _silu(_B_ref(xn, B_w_gate, B_w_up, B_w_down))
    return x + s * g
```

```cpp
#include <hip/hip_runtime.h>
#include <hip/hip_bf16.h>
#include <stdint.h>

namespace {

typedef unsigned v4u __attribute__((ext_vector_type(4)));
typedef unsigned v2u __attribute__((ext_vector_type(2)));
typedef float f32x4 __attribute__((ext_vector_type(4)));
typedef float f32x2 __attribute__((ext_vector_type(2)));
typedef __bf16 bf16x8 __attribute__((ext_vector_type(8)));
typedef __bf16 bf16x2 __attribute__((ext_vector_type(2)));
typedef unsigned long long u64;
#define LAS __attribute__((address_space(3)))
#define GAS __attribute__((address_space(1)))

constexpr int D = 576, HKV = 16, SK = 65536, DH = 128, FF = 4608, QN = 4096;
constexpr int NWG = 256, NTHR = 512;
constexpr int NT = SK / 32;
#define NS_TILES 24
constexpr int NS = NS_TILES;
constexpr int DYN0 = NS * 64;
#define PRE2 1
constexpr unsigned HEAD_BYTES = (unsigned)SK * DH * 2;
constexpr int NCG = D / 4;
constexpr int NFT = FF / 32;

constexpr int E_OFF = 0;
constexpr int C_OFF = 256;
constexpr int RDY_OFF = 8192;
constexpr int QG_OFF = 16384;
constexpr int ML_OFF = 49152;
constexpr int P1_OFF = 65536;
constexpr int FP_OFF = 589824;
constexpr int P2_OFF = 1048576;
constexpr int WS_BYTES = P2_OFF + NCG * NWG * 32;
constexpr int ST_OFF = 4 << 20;

constexpr int AUX_NT = 2, AUX_SC1 = 16;
constexpr unsigned RSRC3 = 0x00020000u;
constexpr unsigned SPIN_CAP = 1u << 17;

constexpr int L_QRDY = 0, L_TRDY = 1, L_CNTA = 2, L_CNTD = 3, L_CNTS = 4, L_CNTF = 5, L_GO = 6, L_FFR = 7, L_SDONE = 8, L_YD = 9;
constexpr int L_NCTL = 16;
constexpr int L_TAG = 16;
constexpr int L_Q = 32;
constexpr int L_RED = 192;
constexpr int L_G = 384;
constexpr int L_FIN = 400;
constexpr int L_ML = 432;
constexpr int L_ACC = 512;
constexpr int L_TOTAL = 24 * 1024;

constexpr float SCALE = 0.08838834764831845f;
constexpr float LOG2E = 1.4426950408889634f;

__device__ __forceinline__ unsigned cvtpk(float lo, float hi) {
  f32x2 v = {lo, hi};
  return __builtin_bit_cast(unsigned, __builtin_convertvector(v, bf16x2));
}
__device__ __forceinline__ float bflo(unsigned u) { return __builtin_bit_cast(float, u << 16); }
__device__ __forceinline__ float bfhi(unsigned u) { return __builtin_bit_cast(float, u & 0xffff0000u); }
__device__ __forceinline__ float rbf(float x) { return bflo(cvtpk(x, x)); }
__device__ __forceinline__ float asf(unsigned u) { return __builtin_bit_cast(float, u); }
__device__ __forceinline__ unsigned asu(float f) { return __builtin_bit_cast(unsigned, f); }

template <int CTRL> __device__ __forceinline__ float dpp(float x) {
  return __builtin_bit_cast(float, __builtin_amdgcn_mov_dpp(__builtin_bit_cast(int, x), CTRL, 0xf, 0xf, true));
}
constexpr int XOR1 = 0xB1, XOR2 = 0x4E, HMIR = 0x141, ROR8 = 0x128, ROR4 = 0x124, SHL4 = 0x104, SHR4 = 0x114, QB0 = 0x00, QB2 = 0xAA;

__device__ __forceinline__ float xrow_max(float x) {
  auto s = __builtin_amdgcn_permlane16_swap(asu(x), asu(x), false, false);
  x = fmaxf(asf(s[0]), asf(s[1]));
  auto t = __builtin_amdgcn_permlane32_swap(asu(x), asu(x), false, false);
  return fmaxf(asf(t[0]), asf(t[1]));
}
__device__ __forceinline__ float xrow_sum(float x) {
  auto s = __builtin_amdgcn_permlane16_swap(asu(x), asu(x), false, false);
  x = asf(s[0]) + asf(s[1]);
  auto t = __builtin_amdgcn_permlane32_swap(asu(x), asu(x), false, false);
  return asf(t[0]) + asf(t[1]);
}
__device__ __forceinline__ float wave_sum(float x) {
  x += dpp<XOR1>(x); x += dpp<XOR2>(x); x += dpp<HMIR>(x); x += dpp<ROR8>(x);
  return xrow_sum(x);
}
__device__ __forceinline__ float swap32_add(float a, float b) {
  auto s = __builtin_amdgcn_permlane32_swap(asu(a), asu(b), false, false);
  return asf(s[0]) + asf(s[1]);
}
__device__ __forceinline__ float swap16_add(float a, float b) {
  auto s = __builtin_amdgcn_permlane16_swap(asu(a), asu(b), false, false);
  return asf(s[0]) + asf(s[1]);
}
__device__ __forceinline__ f32x4 mfma(v4u a, v4u b, f32x4 c) {
  return __builtin_amdgcn_mfma_f32_16x16x32_bf16(__builtin_bit_cast(bf16x8, a), __builtin_bit_cast(bf16x8, b), c, 0, 0, 0);
}
__device__ __forceinline__ float silu_f(float x) { return x / (1.0f + __expf(-x)); }

#define CLOBBER() asm volatile("" ::: "memory")
#define LGKM0() asm volatile("s_waitcnt lgkmcnt(0)" ::: "memory")
#define DRAIN() asm volatile("s_waitcnt vmcnt(0)" ::: "memory")

__device__ __forceinline__ unsigned lds_ld(volatile LAS unsigned* L, int i) { return __builtin_amdgcn_readfirstlane(L[i]); }
template <int SLEEP> __device__ __forceinline__ void lds_wait(volatile LAS unsigned* L, int i, unsigned want) {
#pragma nounroll
  for (unsigned s = 0; s < (SPIN_CAP << 3); ++s) {
    if (lds_ld(L, i) >= want) break;
    __builtin_amdgcn_s_sleep(SLEEP);
  }
  CLOBBER();
}
__device__ __forceinline__ unsigned lds_inc(volatile LAS unsigned* L, int i, int lane) {
  unsigned r = 0;
  if (lane == 0) r = __hip_atomic_fetch_add((LAS unsigned*)(L + i), 1u, __ATOMIC_RELAXED, __HIP_MEMORY_SCOPE_WORKGROUP);
  r = __builtin_amdgcn_readfirstlane(r);
  CLOBBER();
  return r;
}

struct Args {
  const unsigned short* x;
  const unsigned char* k;
  const unsigned char* v;
  const unsigned char* wq;
  const unsigned char* wo;
  const unsigned char* wg;
  const unsigned char* wu;
  const unsigned char* wd;
  unsigned short* out;
  unsigned char* ws;
};

#define STAMP(i) do { } while (0)

struct Tile { v4u k[8]; v4u v[8]; };

struct SState {
  f32x4 acc;
  float m, l;
  v4u fa, fb;
};

template <int QD> __device__ __forceinline__ void build_frag(v4u& f, unsigned pb) {
  constexpr int r0 = (0 - QD) & 3, r1 = (1 - QD) & 3, r2 = (2 - QD) & 3, r3 = (3 - QD) & 3;
  f.x = __builtin_amdgcn_update_dpp(f.x, pb, r0 ? 0x120 + 4 * r0 : 0xE4, 0xf, 0x1, false);
  f.y = __builtin_amdgcn_update_dpp(f.y, pb, r1 ? 0x120 + 4 * r1 : 0xE4, 0xf, 0x2, false);
  f.z = __builtin_amdgcn_update_dpp(f.z, pb, r2 ? 0x120 + 4 * r2 : 0xE4, 0xf, 0x4, false);
  f.w = __builtin_amdgcn_update_dpp(f.w, pb, r3 ? 0x120 + 4 * r3 : 0xE4, 0xf, 0x8, false);
}

__device__ __forceinline__ float pick_bank(f32x4 d) {
  unsigned p = asu(d.x);
  p = __builtin_amdgcn_update_dpp(p, asu(d.y), 0xE4, 0xf, 0x2, false);
  p = __builtin_amdgcn_update_dpp(p, asu(d.z), 0xE4, 0xf, 0x4, false);
  p = __builtin_amdgcn_update_dpp(p, asu(d.w), 0xE4, 0xf, 0x8, false);
  return asf(p);
}

__device__ __forceinline__ v4u ldkv(__amdgpu_buffer_rsrc_t r, int voff, unsigned soff) {
  return __builtin_amdgcn_raw_buffer_load_b128(r, voff, soff, AUX_NT);
}

template <bool REFILL>
__device__ __forceinline__ void process_tile(Tile& t, SState& s, const v4u (&qf)[4], __amdgpu_buffer_rsrc_t rk, __amdgpu_buffer_rsrc_t rv,
                                             int voffk, int voffv, unsigned offn, unsigned halfmask) {
  f32x4 d0 = {0.f, 0.f, 0.f, 0.f}, d1 = {0.f, 0.f, 0.f, 0.f};
#pragma unroll
  for (int i = 0; i < 4; ++i) {
    d0 = mfma(t.k[i], qf[i], d0);
    if (REFILL) { t.k[i] = ldkv(rk, voffk + i * 64, offn); __builtin_amdgcn_sched_barrier(0); }
  }
#pragma unroll
  for (int i = 0; i < 4; ++i) {
    d1 = mfma(t.k[4 + i], qf[i], d1);
    if (REFILL) { t.k[4 + i] = ldkv(rk, voffk + 4096 + i * 64, offn); __builtin_amdgcn_sched_barrier(0); }
  }
  const float p0 = pick_bank(d0), p1 = pick_bank(d1);
  unsigned pk = cvtpk(p0, p1);
  pk = cvtpk(bflo(pk) * SCALE, bfhi(pk) * SCALE);
  const float s0 = bflo(pk), s1 = bfhi(pk);
  float tm = fmaxf(s0, s1);
  tm = fmaxf(tm, dpp<ROR4>(tm));
  tm = fmaxf(tm, dpp<ROR8>(tm));
  tm = xrow_max(tm);
  const float mn = fmaxf(s.m, tm);
  const float alpha = __builtin_amdgcn_exp2f((s.m - mn) * LOG2E);
  const float e0 = __builtin_amdgcn_exp2f((s0 - mn) * LOG2E);
  const float e1 = __builtin_amdgcn_exp2f((s1 - mn) * LOG2E);
  s.m = mn;
  s.l = fmaf(s.l, alpha, e0 + e1);
  const float a0 = dpp<QB0>(alpha), a1 = dpp<QB2>(alpha);
  s.acc.x *= a0; s.acc.y *= a0; s.acc.z *= a1; s.acc.w *= a1;
  const unsigned pb0 = cvtpk(e0, e0) & halfmask, pb1 = cvtpk(e1, e1) & halfmask;
#define PV_STEP(IDX, F, PB, QD) \
  build_frag<QD>(F, PB); \
  s.acc = mfma(F, t.v[IDX], s.acc); \
  if (REFILL) { t.v[IDX] = ldkv(rv, voffv + (IDX) * 1024, offn); __builtin_amdgcn_sched_barrier(0); }
  PV_STEP(0, s.fa, pb0, 0) PV_STEP(1, s.fb, pb0, 1) PV_STEP(2, s.fa, pb0, 2) PV_STEP(3, s.fb, pb0, 3)
  PV_STEP(4, s.fa, pb1, 0) PV_STEP(5, s.fb, pb1, 1) PV_STEP(6, s.fa, pb1, 2) PV_STEP(7, s.fb, pb1, 3)
#undef PV_STEP
}

__device__ __forceinline__ void issue_tile(Tile& t, __amdgpu_buffer_rsrc_t rk, __amdgpu_buffer_rsrc_t rv, int voffk, int voffv, unsigned off) {
#pragma unroll
  for (int i = 0; i < 8; ++i) t.k[i] = ldkv(rk, voffk + (i >> 2) * 4096 + (i & 3) * 64, off);
#pragma unroll
  for (int i = 0; i < 8; ++i) t.v[i] = ldkv(rv, voffv + i * 1024, off);
  __builtin_amdgcn_sched_barrier(0);
}

__global__ void __launch_bounds__(NTHR, 2) fused_decode_kernel(Args a) {
  __shared__ __attribute__((aligned(16))) unsigned lds_raw[L_TOTAL];
  volatile LAS unsigned* L = (volatile LAS unsigned*)lds_raw;
  LAS float* LF = (LAS float*)lds_raw;
  const int tid = threadIdx.x, lane = tid & 63, w = __builtin_amdgcn_readfirstlane(tid >> 6);
  const int b = blockIdx.x, g = b >> 4, j = b & 15;
  const __amdgpu_buffer_rsrc_t rws = __builtin_amdgcn_make_buffer_rsrc((void*)a.ws, 0, WS_BYTES, RSRC3);
  const int r4 = lane >> 4, c16 = lane & 15;
  const int p1_off0 = P1_OFF + ((g * 16 + (c16 >> 1)) * 16 + j) * 128 + (8 * (c16 & 1) + 2 * r4) * 8;
  const int p1_off1 = p1_off0 + 8 * 16 * 128;
  STAMP(0);

  if (w < 4) {
    const __amdgpu_buffer_rsrc_t rk = __builtin_amdgcn_make_buffer_rsrc((void*)(a.k + (size_t)g * HEAD_BYTES), 0, HEAD_BYTES, RSRC3);
    const __amdgpu_buffer_rsrc_t rv = __builtin_amdgcn_make_buffer_rsrc((void*)(a.v + (size_t)g * HEAD_BYTES), 0, HEAD_BYTES, RSRC3);
    const __amdgpu_buffer_rsrc_t rc = __builtin_amdgcn_make_buffer_rsrc((void*)(a.ws + C_OFF + g * 256), 0, 4, RSRC3);
    const int voffk = (4 * (c16 & 3) + (c16 >> 2)) * 256 + r4 * 16, voffv = lane * 16;
    const unsigned sb = (unsigned)(j * NS * 4 + w) * 8192u;
    Tile tb;
    Tile ta;
    issue_tile(ta, rk, rv, voffk, voffv, sb);
    issue_tile(tb, rk, rv, voffk, voffv, sb + 32768u);
    __builtin_amdgcn_s_barrier();
    lds_wait<1>(L, L_QRDY, 1u);
    STAMP(1);
    v4u qf[4];
#pragma unroll
    for (int i = 0; i < 4; ++i) qf[i] = *(LAS v4u*)(lds_raw + L_Q + ((c16 >> 1) & 1) * 64 + 16 * i + 4 * r4);
    const unsigned T32 = lds_ld(L, L_TAG), c0 = lds_ld(L, L_TAG + 1);
    const unsigned halfmask = (lane & 1) ? 0xffff0000u : 0x0000ffffu;
    SState s;
    s.acc = f32x4{0.f, 0.f, 0.f, 0.f};
    s.m = -INFINITY; s.l = 0.f;
    s.fa = v4u{0u, 0u, 0u, 0u}; s.fb = v4u{0u, 0u, 0u, 0u};
    process_tile<false>(ta, s, qf, rk, rv, voffk, voffv, 0u, halfmask);
    for (int n = PRE2; n < NS - 2; ++n) process_tile<true>(tb, s, qf, rk, rv, voffk, voffv, sb + (unsigned)(n + 1) * 32768u, halfmask);
    STAMP(2);
    lds_wait<1>(L, L_GO, 1u);
    unsigned offn = sb + (unsigned)(NS - 1) * 32768u;
    for (;;) {
      const unsigned tk = __builtin_amdgcn_raw_ptr_buffer_atomic_add_i32(1, rc, lane * 4, 0, 0);
      process_tile<true>(tb, s, qf, rk, rv, voffk, voffv, offn, halfmask);
      if (offn >= HEAD_BYTES) break;
      const unsigned idx = __builtin_amdgcn_readfirstlane(tk) - c0;
      offn = idx < (unsigned)(NT - DYN0) ? (DYN0 + idx) * 8192u : 0x7f000000u;
    }
    STAMP(3);
    float lw = s.l;
    lw += dpp<ROR4>(lw); lw += dpp<ROR8>(lw); lw = xrow_sum(lw);
    *(LAS f32x4*)(LF + L_ACC + (w * 64 + lane) * 4) = s.acc;
    if (lane == 0 || lane == 2) { LF[L_ML + w * 4 + lane] = s.m; LF[L_ML + w * 4 + lane + 1] = lw; }
    if (lane == 0) L[L_SDONE] = 1u;
    LGKM0();
    if (lds_inc(L, L_CNTS, lane) == 3u) {
      f32x4 ml[4], ac[4];
#pragma unroll
      for (int i = 0; i < 4; ++i) { ml[i] = *(LAS f32x4*)(LF + L_ML + i * 4); ac[i] = *(LAS f32x4*)(LF + L_ACC + (i * 64 + lane) * 4); }
      const float M0 = fmaxf(fmaxf(ml[0].x, ml[1].x), fmaxf(ml[2].x, ml[3].x)), M1 = fmaxf(fmaxf(ml[0].z, ml[1].z), fmaxf(ml[2].z, ml[3].z));
      float L0 = 0.f, L1 = 0.f;
      f32x4 o = {0.f, 0.f, 0.f, 0.f};
#pragma unroll
      for (int i = 0; i < 4; ++i) {
        const float w0 = __builtin_amdgcn_exp2f((ml[i].x - M0) * LOG2E), w1 = __builtin_amdgcn_exp2f((ml[i].z - M1) * LOG2E);
        L0 = fmaf(w0, ml[i].y, L0); L1 = fmaf(w1, ml[i].w, L1);
        o.x = fmaf(w0, ac[i].x, o.x); o.y = fmaf(w0, ac[i].y, o.y); o.z = fmaf(w1, ac[i].z, o.z); o.w = fmaf(w1, ac[i].w, o.w);
      }
      __builtin_amdgcn_raw_buffer_store_b128(v4u{asu(o.x), T32, asu(o.y), T32}, rws, p1_off0, 0, AUX_SC1);
      __builtin_amdgcn_raw_buffer_store_b128(v4u{asu(o.z), T32, asu(o.w), T32}, rws, p1_off1, 0, AUX_SC1);
      if (lane < 2)
        __builtin_amdgcn_raw_buffer_store_b128(v4u{asu(lane ? M1 : M0), T32, asu(lane ? L1 : L0), T32}, rws, ML_OFF + ((g * 2 + lane) * 16 + j) * 16, 0, AUX_SC1);
      STAMP(4);
    }
    if (b < NCG) {
      lds_wait<1>(L, L_YD, 3u);
      const int off = P2_OFF + (b * NWG + 64 * w) * 32 + lane * 16;
      v4u u0, u1;
      for (unsigned sp = 0; sp < SPIN_CAP; ++sp) {
        CLOBBER();
        u0 = __builtin_amdgcn_raw_buffer_load_b128(rws, off, 0, AUX_SC1);
        u1 = __builtin_amdgcn_raw_buffer_load_b128(rws, off + 1024, 0, AUX_SC1);
        const bool ok = u0.y == T32 && u0.w == T32 && u1.y == T32 && u1.w == T32;
        if (__all(ok)) break;
      }
      STAMP(5);
      float y0 = asf(u0.x) + asf(u1.x), y1 = asf(u0.z) + asf(u1.z);
      y0 += dpp<XOR2>(y0); y1 += dpp<XOR2>(y1);
      y0 += dpp<ROR4>(y0); y1 += dpp<ROR4>(y1);
      y0 += dpp<ROR8>(y0); y1 += dpp<ROR8>(y1);
      y0 = xrow_sum(y0); y1 = xrow_sum(y1);
      if (lane < 2) { LF[L_FIN + w * 4 + 2 * lane] = y0; LF[L_FIN + w * 4 + 2 * lane + 1] = y1; }
      LGKM0();
      if (lds_inc(L, L_CNTF, lane) == 3u) {
        lds_wait<1>(L, L_FFR, 1u);
        const int c = lane & 3;
        const float sv = rbf((LF[L_FIN + c] + LF[L_FIN + 4 + c]) + (LF[L_FIN + 8 + c] + LF[L_FIN + 12 + c]));
        const float o = LF[L_G + 4 + c] + rbf(sv * LF[L_G + c]);
        const unsigned pk = cvtpk(o, dpp<XOR1>(o));
        const unsigned pk2 = __builtin_bit_cast(unsigned, dpp<XOR2>(asf(pk)));
        if (lane == 0) *(v2u*)(a.out + 4 * b) = v2u{pk, pk2};
        if (b == 0 && lane == 0) {
          const u64 e = ((u64)L[L_TAG + 5] << 32) | L[L_TAG + 4];
          __hip_atomic_store((GAS u64*)(a.ws + E_OFF), e * 6364136223846793005ull + 1442695040888963407ull, __ATOMIC_RELAXED, __HIP_MEMORY_SCOPE_AGENT);
        }
        STAMP(6);
      }
    }
  } else if (w < 7) {
    const int ta = w - 4, tt = tid - 256;
    const int slot = ((j & 7) - (g >> 1)) & 7, hi = j >> 3;
    const bool is_ffn = slot < 4 || (slot == 4 && hi == 0), is_q = slot == 5 || slot == 6;
    const int ft = 18 * (g >> 1) + (slot < 4 ? (g & 1) * 8 + 2 * slot + hi : 16 + (g & 1));
    const int qt = 4 * g + 2 * (slot - 5) + hi;
    const __amdgpu_buffer_rsrc_t rx = __builtin_amdgcn_make_buffer_rsrc((void*)a.x, 0, D * 2, RSRC3);
    const int rr = lane >> 2, cc = lane & 3;
    v4u wa[12], wb[12];
    v2u dn[32];
    unsigned xr[12];
    const bool task = is_ffn || is_q;
    if (task) {
      const v4u xc0 = __builtin_amdgcn_raw_buffer_load_b128(rx, lane * 16, 0, 0);
      const v4u xc1 = __builtin_amdgcn_raw_buffer_load_b128(rx, 1024 + lane * 16, 0, 0);
#pragma unroll
      for (int i = 0; i < 12; ++i) xr[i] = __builtin_amdgcn_raw_buffer_load_b16(rx, (192 * ta + 16 * i + rr) * 2, 0, 0);
      const int ld = is_q ? QN * 2 : FF * 2;
      const unsigned char* pa = is_q ? a.wq + qt * 128 : a.wg + ft * 64;
      const unsigned char* pb = is_q ? a.wq + qt * 128 + 64 : a.wu + ft * 64;
      const unsigned span = (unsigned)(D - 1) * ld + 64;
      const __amdgpu_buffer_rsrc_t ra = __builtin_amdgcn_make_buffer_rsrc((void*)pa, 0, span, RSRC3);
      const __amdgpu_buffer_rsrc_t rb = __builtin_amdgcn_make_buffer_rsrc((void*)pb, 0, span, RSRC3);
      const int vo = (192 * ta + rr) * ld + cc * 16;
#pragma unroll
      for (int i = 0; i < 12; ++i) {
        wa[i] = __builtin_amdgcn_raw_buffer_load_b128(ra, vo, i * 16 * ld, AUX_NT);
        wb[i] = __builtin_amdgcn_raw_buffer_load_b128(rb, vo, i * 16 * ld, AUX_NT);
      }
      __builtin_amdgcn_sched_barrier(0);
      __builtin_amdgcn_s_barrier();
      float xv[16];
#pragma unroll
      for (int i = 0; i < 4; ++i) { xv[2 * i] = bflo(xc0[i]); xv[2 * i + 1] = bfhi(xc0[i]); xv[8 + 2 * i] = bflo(xc1[i]); xv[9 + 2 * i] = bfhi(xc1[i]); }
      float sm = 0.f;
#pragma unroll
      for (int i = 0; i < 16; ++i) sm += xv[i];
      const float mean = wave_sum(sm) * (1.0f / D);
      float q0 = 0.f, q1 = 0.f;
#pragma unroll
      for (int i = 0; i < 8; ++i) { const float d0 = xv[i] - mean, d1 = xv[8 + i] - mean; q0 = fmaf(d0, d0, q0); q1 = fmaf(d1, d1, q1); }
      const float var = wave_sum(q0 + (lane < 8 ? q1 : 0.f)) * (1.0f / D);
      const float mb = rbf(mean), den = rbf(sqrtf(rbf(rbf(var) + 1e-5f)));
      float acc[16];
#pragma unroll
      for (int e = 0; e < 16; ++e) acc[e] = 0.f;
#pragma unroll
      for (int i = 0; i < 12; ++i) {
        const float xn = rbf(rbf(bflo(xr[i]) - mb) / den);
#pragma unroll
        for (int e = 0; e < 4; ++e) {
          acc[2 * e] = fmaf(xn, bflo(wa[i][e]), acc[2 * e]); acc[2 * e + 1] = fmaf(xn, bfhi(wa[i][e]), acc[2 * e + 1]);
          acc[8 + 2 * e] = fmaf(xn, bflo(wb[i][e]), acc[8 + 2 * e]); acc[9 + 2 * e] = fmaf(xn, bfhi(wb[i][e]), acc[9 + 2 * e]);
        }
        asm volatile("" : "+v"(acc[0]), "+v"(acc[1]), "+v"(acc[2]), "+v"(acc[3]), "+v"(acc[4]), "+v"(acc[5]), "+v"(acc[6]), "+v"(acc[7]),
                          "+v"(acc[8]), "+v"(acc[9]), "+v"(acc[10]), "+v"(acc[11]), "+v"(acc[12]), "+v"(acc[13]), "+v"(acc[14]), "+v"(acc[15]));
      }
      if (is_ffn) {
        const __amdgpu_buffer_rsrc_t rd = __builtin_amdgcn_make_buffer_rsrc((void*)(a.wd + (size_t)ft * 32 * D * 2), 0, 32 * D * 2, RSRC3);
        const int vo2 = tt < NCG ? tt * 8 : 0x7f000000;
#pragma unroll
        for (int k = 0; k < 32; ++k) dn[k] = __builtin_amdgcn_raw_buffer_load_b64(rd, vo2, k * D * 2, AUX_NT);
        __builtin_amdgcn_sched_barrier(0);
      }
      float u[8], v4[4], z[2];
#pragma unroll
      for (int e = 0; e < 8; ++e) u[e] = swap32_add(acc[e], acc[8 + e]);
#pragma unroll
      for (int e = 0; e < 4; ++e) v4[e] = swap16_add(u[e], u[4 + e]);
#pragma unroll
      for (int e = 0; e < 2; ++e) { const float t1 = v4[e] + dpp<ROR8>(v4[e]), t2 = v4[2 + e] + dpp<ROR8>(v4[2 + e]); z[e] = (lane & 8) ? t2 : t1; }
      const float t1 = z[0] + dpp<SHL4>(z[0]), t2 = z[1] + dpp<SHR4>(z[1]);
      const float zz = (lane & 4) ? t2 : t1;
      const int col = (lane & 32) + 8 * cc + ((lane >> 2) & 4) + ((lane >> 2) & 2) + ((lane >> 2) & 1);
      LF[L_RED + ta * 64 + col] = zz;
      LGKM0();
      const unsigned arr = lds_inc(L, L_CNTA, lane);
      if (is_q) {
        if (arr == 2u) {
          const float qv = (LF[L_RED + lane] + LF[L_RED + 64 + lane]) + LF[L_RED + 128 + lane];
          lds_wait<1>(L, L_TRDY, 1u);
          const unsigned tlo = lds_ld(L, L_TAG + 2), thi = lds_ld(L, L_TAG + 3);
          __builtin_amdgcn_raw_buffer_store_b64(v2u{(cvtpk(qv, qv) & 0xffffu) | (tlo << 16), thi}, rws, QG_OFF + (qt * 64 + lane) * 8, 0, AUX_SC1);
          STAMP(1);
        }
      } else {
        lds_wait<1>(L, L_CNTA, 3u);
        const int u5 = lane & 31;
        const float gp = rbf((LF[L_RED + u5] + LF[L_RED + 64 + u5]) + LF[L_RED + 128 + u5]);
        const float up = rbf((LF[L_RED + 32 + u5] + LF[L_RED + 96 + u5]) + LF[L_RED + 160 + u5]);
        const float h = rbf(rbf(silu_f(gp)) * up);
        f32x4 y = {0.f, 0.f, 0.f, 0.f};
#pragma unroll
        for (int k = 0; k < 32; ++k) {
          const float hk = asf(__builtin_amdgcn_readlane(asu(h), k));
          y.x = fmaf(hk, bflo(dn[k].x), y.x); y.y = fmaf(hk, bfhi(dn[k].x), y.y); y.z = fmaf(hk, bflo(dn[k].y), y.z); y.w = fmaf(hk, bfhi(dn[k].y), y.w);
        }
        if (tt < NCG) __builtin_amdgcn_raw_buffer_store_b128(__builtin_bit_cast(v4u, y), rws, FP_OFF + (tt * NFT + ft) * 16, 0, AUX_SC1);
        STAMP(1);
      }
    } else {
      __builtin_amdgcn_s_barrier();
    }
    const __amdgpu_buffer_rsrc_t ro = __builtin_amdgcn_make_buffer_rsrc((void*)(a.wo + (size_t)b * 16 * D * 2), 0, 16 * D * 2, RSRC3);
    v2u wr[16];
    {
      const int vo2 = tt < NCG ? tt * 8 : 0x7f000000;
#pragma unroll
      for (int k = 0; k < 16; ++k) wr[k] = __builtin_amdgcn_raw_buffer_load_b64(ro, vo2, k * D * 2, AUX_NT);
    }
    DRAIN();
    lds_inc(L, L_CNTD, lane);
    float wf[64];
#pragma unroll
    for (int k = 0; k < 16; ++k) { wf[4 * k] = bflo(wr[k].x); wf[4 * k + 1] = bfhi(wr[k].x); wf[4 * k + 2] = bflo(wr[k].y); wf[4 * k + 3] = bfhi(wr[k].y); }
#pragma unroll
    for (int k = 0; k < 64; ++k) asm volatile("" : "+v"(wf[k]));
    STAMP(2);
    lds_wait<8>(L, L_GO, 1u);
    lds_wait<4>(L, L_SDONE, 1u);
    const unsigned T32 = lds_ld(L, L_TAG);
    STAMP(3);
    const int o1 = P1_OFF + (g * 16 + j) * 2048 + lane * 16, om = ML_OFF + ((g * 2 + hi) * 16 + (lane >> 3)) * 16;
    v4u pa, pb, ma, mb2;
    for (unsigned sp = 0; sp < SPIN_CAP; ++sp) {
      CLOBBER();
      pa = __builtin_amdgcn_raw_buffer_load_b128(rws, o1, 0, AUX_SC1);
      pb = __builtin_amdgcn_raw_buffer_load_b128(rws, o1 + 1024, 0, AUX_SC1);
      ma = __builtin_amdgcn_raw_buffer_load_b128(rws, om, 0, AUX_SC1);
      mb2 = __builtin_amdgcn_raw_buffer_load_b128(rws, om + 128, 0, AUX_SC1);
      const bool ok = pa.y == T32 && pa.w == T32 && pb.y == T32 && pb.w == T32 && ma.y == T32 && ma.w == T32 && mb2.y == T32 && mb2.w == T32;
      if (__all(ok)) break;
    }
    STAMP(4);
    float M = fmaxf(asf(ma.x), asf(mb2.x));
    M = fmaxf(M, dpp<ROR8>(M)); M = xrow_max(M);
    const float wa_ = __builtin_amdgcn_exp2f((asf(ma.x) - M) * LOG2E), wb_ = __builtin_amdgcn_exp2f((asf(mb2.x) - M) * LOG2E);
    float Ls = fmaf(wa_, asf(ma.z), wb_ * asf(mb2.z));
    float o0 = fmaf(wa_, asf(pa.x), wb_ * asf(pb.x)), o1v = fmaf(wa_, asf(pa.z), wb_ * asf(pb.z));
    Ls += dpp<ROR8>(Ls); o0 += dpp<ROR8>(o0); o1v += dpp<ROR8>(o1v);
    Ls = xrow_sum(Ls); o0 = xrow_sum(o0); o1v = xrow_sum(o1v);
    const unsigned opk = cvtpk(o0 / Ls, o1v / Ls);
    f32x4 y = {0.f, 0.f, 0.f, 0.f};
#pragma unroll
    for (int k = 0; k < 8; ++k) {
      const unsigned sp = __builtin_amdgcn_readlane(opk, k);
      const float e0 = bflo(sp), e1 = bfhi(sp);
      y.x = fmaf(e0, wf[8 * k], y.x); y.y = fmaf(e0, wf[8 * k + 1], y.y); y.z = fmaf(e0, wf[8 * k + 2], y.z); y.w = fmaf(e0, wf[8 * k + 3], y.w);
      y.x = fmaf(e1, wf[8 * k + 4], y.x); y.y = fmaf(e1, wf[8 * k + 5], y.y); y.z = fmaf(e1, wf[8 * k + 6], y.z); y.w = fmaf(e1, wf[8 * k + 7], y.w);
    }
    if (tt < NCG) {
      const int o2 = P2_OFF + (tt * NWG + b) * 32;
      __builtin_amdgcn_raw_buffer_store_b128(v4u{asu(y.x), T32, asu(y.y), T32}, rws, o2, 0, AUX_SC1);
      __builtin_amdgcn_raw_buffer_store_b128(v4u{asu(y.z), T32, asu(y.w), T32}, rws, o2 + 16, 0, AUX_SC1);
    }
    lds_inc(L, L_YD, lane);
    STAMP(5);
  } else {
    if (lane < L_NCTL) L[lane] = 0u;
    const v2u ev = __builtin_amdgcn_raw_buffer_load_b64(rws, E_OFF, 0, AUX_SC1);
    const unsigned cv = __builtin_amdgcn_raw_buffer_load_b32(rws, C_OFF + g * 256, 0, AUX_SC1);
    LGKM0();
    __builtin_amdgcn_s_barrier();
    const v4u zero = {0u, 0u, 0u, 0u};
    __builtin_amdgcn_raw_buffer_store_b128(zero, rws, p1_off0, 0, AUX_SC1);
    __builtin_amdgcn_raw_buffer_store_b128(zero, rws, p1_off1, 0, AUX_SC1);
    if (lane < 2) __builtin_amdgcn_raw_buffer_store_b128(zero, rws, ML_OFF + ((g * 2 + lane) * 16 + j) * 16, 0, AUX_SC1);
#pragma unroll
    for (int i = 0; i < 5; ++i) {
      const int e = i * 64 + lane;
      if (e < 2 * NCG) __builtin_amdgcn_raw_buffer_store_b128(zero, rws, P2_OFF + ((e >> 1) * NWG + b) * 32 + (e & 1) * 16, 0, AUX_SC1);
    }
    const unsigned elo = __builtin_amdgcn_readfirstlane(ev.x), ehi = __builtin_amdgcn_readfirstlane(ev.y);
    const unsigned T32 = (elo ^ 0x85EBCA6Bu) | 1u;
    const unsigned qlo = (elo ^ 0x7F4A7C15u) & 0xffffu, qhi = ((elo ^ 0x7F4A7C15u) >> 16) | ((ehi ^ 0x9E3779B9u) << 16);
    const unsigned rlo = elo ^ 0xC2B2AE35u, rhi = ehi ^ 0x27D4EB2Fu;
    if (lane == 0) { L[L_TAG] = T32; L[L_TAG + 1] = cv; L[L_TAG + 2] = qlo; L[L_TAG + 3] = qhi; L[L_TAG + 4] = elo; L[L_TAG + 5] = ehi; }
    LGKM0();
    if (lane == 0) L[L_TRDY] = 1u;
    STAMP(1);
    {
      const int oq = QG_OFF + g * 2048 + lane * 16;
      v4u q0, q1;
      for (unsigned sp = 0; sp < SPIN_CAP; ++sp) {
        CLOBBER();
        q0 = __builtin_amdgcn_raw_buffer_load_b128(rws, oq, 0, AUX_SC1);
        q1 = __builtin_amdgcn_raw_buffer_load_b128(rws, oq + 1024, 0, AUX_SC1);
        const bool ok = q0.y == qhi && q0.w == qhi && q1.y == qhi && q1.w == qhi && (q0.x >> 16) == qlo && (q0.z >> 16) == qlo && (q1.x >> 16) == qlo && (q1.z >> 16) == qlo;
        if (__all(ok)) break;
      }
      L[L_Q + lane] = (q0.x & 0xffffu) | (q0.z << 16);
      L[L_Q + 64 + lane] = (q1.x & 0xffffu) | (q1.z << 16);
      LGKM0();
      if (lane == 0) L[L_QRDY] = 1u;
    }
    STAMP(2);
    lds_wait<8>(L, L_CNTD, 3u);
    DRAIN();
    if (lane == 0) __builtin_amdgcn_raw_buffer_store_b64(v2u{rlo, rhi}, rws, RDY_OFF + b * 8, 0, AUX_SC1);
    STAMP(3);
    for (unsigned sp = 0; sp < SPIN_CAP; ++sp) {
      __builtin_amdgcn_s_sleep(100);
      CLOBBER();
      const v4u f0 = __builtin_amdgcn_raw_buffer_load_b128(rws, RDY_OFF + lane * 16, 0, AUX_SC1);
      const v4u f1 = __builtin_amdgcn_raw_buffer_load_b128(rws, RDY_OFF + 1024 + lane * 16, 0, AUX_SC1);
      const bool ok = f0.x == rlo && f0.y == rhi && f0.z == rlo && f0.w == rhi && f1.x == rlo && f1.y == rhi && f1.z == rlo && f1.w == rhi;
      if (__all(ok)) break;
    }
    CLOBBER();
    if (lane == 0) L[L_GO] = 1u;
    STAMP(4);
    if (b < NCG) {
      const int of = FP_OFF + b * NFT * 16 + lane * 16;
      const v4u f0 = __builtin_amdgcn_raw_buffer_load_b128(rws, of, 0, AUX_SC1);
      const v4u f1 = __builtin_amdgcn_raw_buffer_load_b128(rws, of + 1024, 0, AUX_SC1);
      const v4u f2 = __builtin_amdgcn_raw_buffer_load_b128(rws, lane < 16 ? of + 2048 : 0x7f000000, 0, AUX_SC1);
      const v2u xv = *(const v2u*)(a.x + 4 * b);
      float y[4];
#pragma unroll
      for (int i = 0; i < 4; ++i) y[i] = wave_sum((asf(f0[i]) + asf(f1[i])) + asf(f2[i]));
      if (lane == 0) {
#pragma unroll
        for (int i = 0; i < 4; ++i) LF[L_G + i] = rbf(silu_f(rbf(y[i])));
        LF[L_G + 4] = bflo(xv.x); LF[L_G + 5] = bfhi(xv.x); LF[L_G + 6] = bflo(xv.y); LF[L_G + 7] = bfhi(xv.y);
      }
      LGKM0();
      if (lane == 0) L[L_FFR] = 1u;
    }
    STAMP(5);
  }
}

}

extern "C" void kernel_launch(void* const* d_in, const int* in_sizes, int n_in, void* d_out, int out_size, void* d_ws, size_t ws_size,
                              hipStream_t stream) {
  Args a;
  a.x = (const unsigned short*)d_in[0];
  a.k = (const unsigned char*)d_in[1];
  a.v = (const unsigned char*)d_in[2];
  a.wq = (const unsigned char*)d_in[3];
  a.wo = (const unsigned char*)d_in[4];
  a.wg = (const unsigned char*)d_in[6];
  a.wu = (const unsigned char*)d_in[7];
  a.wd = (const unsigned char*)d_in[8];
  a.out = (unsigned short*)d_out;
  a.ws = (unsigned char*)d_ws;
  hipLaunchKernelGGL(fused_decode_kernel, dim3(NWG), dim3(NTHR), 0, stream, a);
}
```

```cpp
#include <hip/hip_runtime.h>
#include <hip/hip_bf16.h>
#include <stdint.h>

namespace {

typedef unsigned v4u __attribute__((ext_vector_type(4)));
typedef unsigned v2u __attribute__((ext_vector_type(2)));
typedef float f32x4 __attribute__((ext_vector_type(4)));
typedef float f32x2 __attribute__((ext_vector_type(2)));
typedef __bf16 bf16x8 __attribute__((ext_vector_type(8)));
typedef __bf16 bf16x2 __attribute__((ext_vector_type(2)));
typedef unsigned long long u64;
#define LAS __attribute__((address_space(3)))
#define GAS __attribute__((address_space(1)))

constexpr int D = 576, HKV = 16, SK = 65536, DH = 128, FF = 4608, QN = 4096;
constexpr int NWG = 256, NTHR = 512;
constexpr int NT = SK / 32;
#define NS_TILES 20
constexpr int NS = NS_TILES;
constexpr int DYN0 = NS * 64;
#define QK_DOT2 1
#define FFN_WHEN 2
#define POLL_DELAY 0
#define FM 0
#define PACE_W 8
#define DRYRUN 1
constexpr unsigned HEAD_BYTES = (unsigned)SK * DH * 2;
constexpr int NCG = D / 4;
constexpr int NFC = 36;
constexpr int NFT = FF / 32;

constexpr int E_OFF = 0;
constexpr int C_OFF = 256;
constexpr int RDY_OFF = 8192;
constexpr int QG_OFF = 16384;
constexpr int P1_OFF = 65536;
constexpr int FP_OFF = 589824;
constexpr int P2_OFF = 1048576;
constexpr int WS_BYTES = P2_OFF + NFC * NWG * 128;
constexpr int ST_OFF = 4 << 20;

constexpr int AUX_NT = 2, AUX_SC1 = 16;
constexpr unsigned RSRC3 = 0x00020000u;
constexpr unsigned SPIN_CAP = 1u << 17;

constexpr int L_QRDY = 0, L_TRDY = 1, L_CNTA = 2, L_CNTD = 3, L_CNTS = 4, L_CNTF = 5, L_GO = 6, L_FFR = 7, L_SDONE = 8, L_YD = 9, L_QPUB = 10, L_ORDY = 11, L_CNTQ = 12;
constexpr int L_NCTL = 16;
constexpr int L_TAG = 16;
constexpr int L_Q = 32;
constexpr int L_RED = 192;
constexpr int L_O = 384;
constexpr int L_ML = 400;
constexpr int L_REDQ = 432;
constexpr int L_G = 2304;
constexpr int L_FIN = 2368;
constexpr int L_ACC = 512;
constexpr int L_XN = 1536;
constexpr int L_DRY = 4096;
constexpr int L_TOTAL = 24 * 1024;

constexpr float SCALE = 0.08838834764831845f;
constexpr float LOG2E = 1.4426950408889634f;

__device__ __forceinline__ unsigned cvtpk(float lo, float hi) {
  f32x2 v = {lo, hi};
  return __builtin_bit_cast(unsigned, __builtin_convertvector(v, bf16x2));
}
__device__ __forceinline__ float bflo(unsigned u) { return __builtin_bit_cast(float, u << 16); }
__device__ __forceinline__ float bfhi(unsigned u) { return __builtin_bit_cast(float, u & 0xffff0000u); }
__device__ __forceinline__ float rbf(float x) { return bflo(cvtpk(x, x)); }
__device__ __forceinline__ float asf(unsigned u) { return __builtin_bit_cast(float, u); }
__device__ __forceinline__ unsigned asu(float f) { return __builtin_bit_cast(unsigned, f); }

template <int CTRL> __device__ __forceinline__ float dpp(float x) {
  return __builtin_bit_cast(float, __builtin_amdgcn_mov_dpp(__builtin_bit_cast(int, x), CTRL, 0xf, 0xf, true));
}
constexpr int XOR1 = 0xB1, XOR2 = 0x4E, HMIR = 0x141, ROR8 = 0x128, ROR4 = 0x124, SHL4 = 0x104, SHR4 = 0x114, QB0 = 0x00, QB2 = 0xAA;

__device__ __forceinline__ float xrow_max(float x) {
  auto s = __builtin_amdgcn_permlane16_swap(asu(x), asu(x), false, false);
  x = fmaxf(asf(s[0]), asf(s[1]));
  auto t = __builtin_amdgcn_permlane32_swap(asu(x), asu(x), false, false);
  return fmaxf(asf(t[0]), asf(t[1]));
}
__device__ __forceinline__ float xrow_sum(float x) {
  auto s = __builtin_amdgcn_permlane16_swap(asu(x), asu(x), false, false);
  x = asf(s[0]) + asf(s[1]);
  auto t = __builtin_amdgcn_permlane32_swap(asu(x), asu(x), false, false);
  return asf(t[0]) + asf(t[1]);
}
__device__ __forceinline__ float wave_sum(float x) {
  x += dpp<XOR1>(x); x += dpp<XOR2>(x); x += dpp<HMIR>(x); x += dpp<ROR8>(x);
  return xrow_sum(x);
}
__device__ __forceinline__ float swap32_add(float a, float b) {
  auto s = __builtin_amdgcn_permlane32_swap(asu(a), asu(b), false, false);
  return asf(s[0]) + asf(s[1]);
}
__device__ __forceinline__ float swap16_add(float a, float b) {
  auto s = __builtin_amdgcn_permlane16_swap(asu(a), asu(b), false, false);
  return asf(s[0]) + asf(s[1]);
}
__device__ __forceinline__ f32x4 mfma(v4u a, v4u b, f32x4 c) {
  return __builtin_amdgcn_mfma_f32_16x16x32_bf16(__builtin_bit_cast(bf16x8, a), __builtin_bit_cast(bf16x8, b), c, 0, 0, 0);
}
__device__ __forceinline__ float dot2(unsigned a, unsigned b, float c) {
  return __builtin_amdgcn_fdot2_f32_bf16(__builtin_bit_cast(bf16x2, a), __builtin_bit_cast(bf16x2, b), c, false);
}
__device__ __forceinline__ float silu_f(float x) { return x / (1.0f + __expf(-x)); }

#define WG_BARRIER() do { __builtin_amdgcn_sched_barrier(0); __builtin_amdgcn_s_barrier(); __builtin_amdgcn_sched_barrier(0); } while (0)
#define CLOBBER() asm volatile("" ::: "memory")
#define LGKM0() asm volatile("s_waitcnt lgkmcnt(0)" ::: "memory")
#define DRAIN() asm volatile("s_waitcnt vmcnt(0)" ::: "memory")

__device__ __forceinline__ unsigned lds_ld(volatile LAS unsigned* L, int i) { return __builtin_amdgcn_readfirstlane(L[i]); }
template <int SLEEP> __device__ __forceinline__ void lds_wait(volatile LAS unsigned* L, int i, unsigned want) {
#pragma nounroll
  for (unsigned s = 0; s < (SPIN_CAP << 3); ++s) {
    if (lds_ld(L, i) >= want) break;
    __builtin_amdgcn_s_sleep(SLEEP);
  }
  CLOBBER();
}
__device__ __forceinline__ unsigned lds_inc(volatile LAS unsigned* L, int i, int lane) {
  unsigned r = 0;
  if (lane == 0) r = __hip_atomic_fetch_add((LAS unsigned*)(L + i), 1u, __ATOMIC_RELAXED, __HIP_MEMORY_SCOPE_WORKGROUP);
  r = __builtin_amdgcn_readfirstlane(r);
  CLOBBER();
  return r;
}

struct Args {
  const unsigned short* x;
  const unsigned char* k;
  const unsigned char* v;
  const unsigned char* wq;
  const unsigned char* wo;
  const unsigned char* wg;
  const unsigned char* wu;
  const unsigned char* wd;
  unsigned short* out;
  unsigned char* ws;
};

#define STAMP(i) do { } while (0)

#define KOFF(i) (((i) >> 2) * 4096 + (((i) >> 1) & 1) * 1024 + ((i) & 1) * 128)
struct Tile { v4u k[8]; v4u v[8]; };

struct SState {
  f32x4 acc;
  float m, l;
  v4u fa, fb;
};

template <int QD> __device__ __forceinline__ void build_frag(v4u& f, unsigned pb) {
  constexpr int r0 = (0 - QD) & 3, r1 = (1 - QD) & 3, r2 = (2 - QD) & 3, r3 = (3 - QD) & 3;
  f.x = __builtin_amdgcn_update_dpp(f.x, pb, r0 ? 0x120 + 4 * r0 : 0xE4, 0xf, 0x1, false);
  f.y = __builtin_amdgcn_update_dpp(f.y, pb, r1 ? 0x120 + 4 * r1 : 0xE4, 0xf, 0x2, false);
  f.z = __builtin_amdgcn_update_dpp(f.z, pb, r2 ? 0x120 + 4 * r2 : 0xE4, 0xf, 0x4, false);
  f.w = __builtin_amdgcn_update_dpp(f.w, pb, r3 ? 0x120 + 4 * r3 : 0xE4, 0xf, 0x8, false);
}

__device__ __forceinline__ float pick_bank(f32x4 d) {
  unsigned p = asu(d.x);
  p = __builtin_amdgcn_update_dpp(p, asu(d.y), 0xE4, 0xf, 0x2, false);
  p = __builtin_amdgcn_update_dpp(p, asu(d.z), 0xE4, 0xf, 0x4, false);
  p = __builtin_amdgcn_update_dpp(p, asu(d.w), 0xE4, 0xf, 0x8, false);
  return asf(p);
}

__device__ __forceinline__ v4u ldkv(__amdgpu_buffer_rsrc_t r, int voff, unsigned soff) {
  return __builtin_amdgcn_raw_buffer_load_b128(r, voff, soff, AUX_NT);
}

template <bool REFILL>
__device__ __forceinline__ void process_tile(Tile& t, SState& s, const v4u (&qf)[4], __amdgpu_buffer_rsrc_t rk, __amdgpu_buffer_rsrc_t rv,
                                             int voffk, int voffv, unsigned offn, unsigned halfmask, bool hsel) {
  float x[4][2];
#pragma unroll
  for (int u = 0; u < 4; ++u) {
    float a0 = 0.f, a1 = 0.f;
#pragma unroll
    for (int hf = 0; hf < 2; ++hf) {
#pragma unroll
      for (int e = 0; e < 4; ++e) { a0 = dot2(t.k[2 * u + hf][e], qf[hf][e], a0); a1 = dot2(t.k[2 * u + hf][e], qf[2 + hf][e], a1); }
      asm volatile("" : "+v"(a0), "+v"(a1));
      if (REFILL) { t.k[2 * u + hf] = ldkv(rk, voffk + KOFF(2 * u + hf), offn); __builtin_amdgcn_sched_barrier(0); }
    }
    x[u][0] = a0; x[u][1] = a1;
  }
  float pp[2];
#pragma unroll
  for (int v = 0; v < 2; ++v) {
    float y[2];
#pragma unroll
    for (int h = 0; h < 2; ++h) {
      const float t1 = x[2 * v][h] + dpp<HMIR>(x[2 * v][h]), t2 = x[2 * v + 1][h] + dpp<HMIR>(x[2 * v + 1][h]);
      y[h] = asf(__builtin_amdgcn_update_dpp(asu(t1), asu(t2), 0xE4, 0xf, 0xA, false));
    }
    const float t1 = y[0] + dpp<XOR2>(y[0]), t2 = y[1] + dpp<XOR2>(y[1]);
    const float z = hsel ? t2 : t1;
    pp[v] = z + dpp<XOR1>(z);
  }
  const float p0 = pp[0], p1 = pp[1];
  unsigned pk = cvtpk(p0, p1);
  pk = cvtpk(bflo(pk) * SCALE, bfhi(pk) * SCALE);
  const float s0 = bflo(pk) * LOG2E, s1 = bfhi(pk) * LOG2E;
  float tm = fmaxf(s0, s1);
  tm = fmaxf(tm, dpp<ROR4>(tm));
  tm = fmaxf(tm, dpp<ROR8>(tm));
  tm = xrow_max(tm);
  const float mn = fmaxf(s.m, ceilf(tm));
  const float alpha = __builtin_amdgcn_exp2f(s.m - mn);
  const float e0 = __builtin_amdgcn_exp2f(s0 - mn);
  const float e1 = __builtin_amdgcn_exp2f(s1 - mn);
  s.m = mn;
  s.l = fmaf(s.l, alpha, e0 + e1);
  const float a0 = dpp<QB0>(alpha), a1 = dpp<QB2>(alpha);
  s.acc.x *= a0; s.acc.y *= a0; s.acc.z *= a1; s.acc.w *= a1;
  const unsigned pb0 = cvtpk(e0, e0) & halfmask, pb1 = cvtpk(e1, e1) & halfmask;
#define PV_STEP(IDX, F, PB, QD) \
  build_frag<QD>(F, PB); \
  s.acc = mfma(F, t.v[IDX], s.acc); \
  if (REFILL) { t.v[IDX] = ldkv(rv, voffv + (IDX) * 1024, offn); __builtin_amdgcn_sched_barrier(0); }
  PV_STEP(0, s.fa, pb0, 0) PV_STEP(1, s.fb, pb0, 1) PV_STEP(2, s.fa, pb0, 2) PV_STEP(3, s.fb, pb0, 3)
  PV_STEP(4, s.fa, pb1, 0) PV_STEP(5, s.fb, pb1, 1) PV_STEP(6, s.fa, pb1, 2) PV_STEP(7, s.fb, pb1, 3)
#undef PV_STEP
}

__global__ void __launch_bounds__(NTHR, 2) fused_decode_kernel(Args a) {
  __shared__ __attribute__((aligned(16))) unsigned lds_raw[L_TOTAL];
  volatile LAS unsigned* L = (volatile LAS unsigned*)lds_raw;
  LAS float* LF = (LAS float*)lds_raw;
  const int tid = threadIdx.x, lane = tid & 63, w = __builtin_amdgcn_readfirstlane(tid >> 6);
  const int b = blockIdx.x, g = b >> 4, j = b & 15;
  const __amdgpu_buffer_rsrc_t rws = __builtin_amdgcn_make_buffer_rsrc((void*)a.ws, 0, WS_BYTES, RSRC3);
  const int r4 = lane >> 4, c16 = lane & 15;
  const int p1_off0 = P1_OFF + ((g * 16 + (c16 >> 1)) * 16 + j) * 128 + (8 * (c16 & 1) + 2 * r4) * 8;
  const int p1_off1 = p1_off0 + 8 * 16 * 128;
  const int slot = ((j & 7) - (g >> 1)) & 7, hi = j >> 3;
  const int tt = tid - 64;
  const int sw = w & 3;
  int role = w == 0 ? 2 : w < 4 ? 1 : 0;
  STAMP(0);

  SState s;
  s.acc = f32x4{0.f, 0.f, 0.f, 0.f};
  s.m = -INFINITY; s.l = 0.f;
  float wf[64];

  if (role == 0) {
    const __amdgpu_buffer_rsrc_t rk = __builtin_amdgcn_make_buffer_rsrc((void*)(a.k + (size_t)g * HEAD_BYTES), 0, HEAD_BYTES, RSRC3);
    const __amdgpu_buffer_rsrc_t rv = __builtin_amdgcn_make_buffer_rsrc((void*)(a.v + (size_t)g * HEAD_BYTES), 0, HEAD_BYTES, RSRC3);
    const __amdgpu_buffer_rsrc_t rc = __builtin_amdgcn_make_buffer_rsrc((void*)(a.ws + C_OFF + g * 256), 0, 4, RSRC3);
    const int voffk = (8 * (c16 >> 3) + r4) * 256 + (lane & 7) * 16, voffv = lane * 16;
    const unsigned sb = (unsigned)(j * NS * 4 + sw) * 8192u;
    Tile tb;
    WG_BARRIER();
    {
      unsigned qr = 0;
#pragma unroll
      for (int i = 0; i < 16; ++i) {
        if (i >= PACE_W && !qr) { asm volatile("s_waitcnt vmcnt(%0)" ::"n"(PACE_W - 1) : "memory"); qr = lds_ld(L, L_QRDY); }
        if (i < 8) tb.k[i] = ldkv(rk, voffk + KOFF(i), sb);
        else tb.v[i - 8] = ldkv(rv, voffv + (i - 8) * 1024, sb);
        __builtin_amdgcn_sched_barrier(0);
      }
    }
    lds_wait<1>(L, L_QRDY, 1u);
    STAMP(1);
    v4u qf[4];
#pragma unroll
    for (int i = 0; i < 4; ++i) qf[i] = *(LAS v4u*)(lds_raw + L_Q + 32 * i + 4 * (lane & 7));
    const unsigned c0 = lds_ld(L, L_TAG + 1);
    const unsigned halfmask = (lane & 1) ? 0xffff0000u : 0x0000ffffu;
    s.fa = v4u{0u, 0u, 0u, 0u}; s.fb = v4u{0u, 0u, 0u, 0u};
    unsigned offn = sb + 32768u;
#pragma nounroll
    for (int n = 2;; ++n) {
      if (n == NS) { STAMP(2); lds_wait<1>(L, L_GO, 1u); }
      const unsigned tk = __builtin_amdgcn_raw_ptr_buffer_atomic_add_i32(1, rc, lane * 4, n >= NS ? 0 : 4, 0);
      process_tile<true>(tb, s, qf, rk, rv, voffk, voffv, offn, halfmask, (lane & 2) != 0);
      if (offn >= HEAD_BYTES) break;
      const unsigned idx = __builtin_amdgcn_readfirstlane(tk) - c0;
      const unsigned dyn = idx < (unsigned)(NT - DYN0) ? (DYN0 + idx) * 8192u : 0x7f000000u;
      offn = n >= NS ? dyn : sb + (unsigned)n * 32768u;
    }
    STAMP(3);
  } else if (role == 1) {
    const int ta = w - 1;
    __builtin_amdgcn_s_setprio(3);
    const bool is_ffn = slot < 4 || (slot == 4 && hi == 0);
    const int ft = 18 * (g >> 1) + (slot < 4 ? (g & 1) * 8 + 2 * slot + hi : 16 + (g & 1));
    const int qs = ((b & 7) * 8 + (b >> 5)) * 4 + ((b >> 3) & 3);
    const __amdgpu_buffer_rsrc_t rx = __builtin_amdgcn_make_buffer_rsrc((void*)a.x, 0, D * 2, RSRC3);
    const __amdgpu_buffer_rsrc_t rq = __builtin_amdgcn_make_buffer_rsrc((void*)(a.wq + qs * 32), 0, (D - 1) * QN * 2 + 32, RSRC3);
    const int rr = lane >> 2, cc = lane & 3;
    v4u wqr[6];
    const v4u xc0 = __builtin_amdgcn_raw_buffer_load_b128(rx, lane * 16, 0, 0);
    const v4u xc1 = __builtin_amdgcn_raw_buffer_load_b128(rx, 1024 + lane * 16, 0, 0);
    unsigned xo[3];
#pragma unroll
    for (int i = 0; i < 3; ++i) xo[i] = __builtin_amdgcn_raw_buffer_load_b16(rx, (192 * ta + 64 * i + lane) * 2, 0, 0);
    {
      const int vo = (192 * ta + (lane >> 1)) * (QN * 2) + (lane & 1) * 16;
#pragma unroll
      for (int i = 0; i < 6; ++i) wqr[i] = __builtin_amdgcn_raw_buffer_load_b128(rq, vo, i * 32 * QN * 2, AUX_NT);
    }
    const unsigned fspan = is_ffn ? (D - 1) * FF * 2 + 64 : 0;
    const __amdgpu_buffer_rsrc_t ra = __builtin_amdgcn_make_buffer_rsrc((void*)(a.wg + ft * 64), 0, fspan, RSRC3);
    const __amdgpu_buffer_rsrc_t rb = __builtin_amdgcn_make_buffer_rsrc((void*)(a.wu + ft * 64), 0, fspan, RSRC3);
    const int vof = (192 * ta + rr) * (FF * 2) + cc * 16;
    v4u wa[12], wb[12];
#define FFN_LOADS() \
    _Pragma("unroll") for (int i = 0; i < 12; ++i) { \
      wa[i] = __builtin_amdgcn_raw_buffer_load_b128(ra, vof, i * 16 * FF * 2, AUX_NT); \
      wb[i] = __builtin_amdgcn_raw_buffer_load_b128(rb, vof, i * 16 * FF * 2, AUX_NT); \
    } \
    __builtin_amdgcn_sched_barrier(0);
    WG_BARRIER();
    {
      const float x0 = bflo(__builtin_amdgcn_readfirstlane(xc0.x));
      float s0 = 0.f, s1 = 0.f, q0 = 0.f, q1 = 0.f;
#pragma unroll
      for (int i = 0; i < 4; ++i) {
        const float d0 = bflo(xc0[i]) - x0, d1 = bfhi(xc0[i]) - x0, d2 = bflo(xc1[i]) - x0, d3 = bfhi(xc1[i]) - x0;
        s0 += d0 + d1; s1 += d2 + d3;
        q0 = fmaf(d0, d0, q0); q0 = fmaf(d1, d1, q0); q1 = fmaf(d2, d2, q1); q1 = fmaf(d3, d3, q1);
      }
      const float dm = wave_sum(s0 + (lane < 8 ? s1 : 0.f)) * (1.0f / D);
      const float var = fmaxf(wave_sum(q0 + (lane < 8 ? q1 : 0.f)) * (1.0f / D) - dm * dm, 0.f);
      const float mb = rbf(x0 + dm), den = rbf(sqrtf(rbf(rbf(var) + 1e-5f)));
#pragma unroll
      for (int i = 0; i < 3; ++i) LF[L_XN + 192 * ta + 64 * i + lane] = rbf(rbf(bflo(xo[i]) - mb) / den);
      LGKM0();
    }
    {
      float acc[8];
#pragma unroll
      for (int e = 0; e < 8; ++e) acc[e] = 0.f;
#pragma unroll
      for (int i = 0; i < 6; ++i) {
        const float xn = LF[L_XN + 192 * ta + 32 * i + (lane >> 1)];
#pragma unroll
        for (int e = 0; e < 4; ++e) { acc[2 * e] = fmaf(xn, bflo(wqr[i][e]), acc[2 * e]); acc[2 * e + 1] = fmaf(xn, bfhi(wqr[i][e]), acc[2 * e + 1]); }
      }
      float u[4], v2[2];
#pragma unroll
      for (int e = 0; e < 4; ++e) u[e] = swap32_add(acc[e], acc[4 + e]);
#pragma unroll
      for (int e = 0; e < 2; ++e) v2[e] = swap16_add(u[e], u[2 + e]);
      const float t1 = v2[0] + dpp<ROR8>(v2[0]), t2 = v2[1] + dpp<ROR8>(v2[1]);
      float z = (lane & 8) ? t2 : t1;
      z += dpp<XOR2>(z);
      const float z1 = dpp<SHL4>(z), z2 = dpp<SHR4>(z);
      z += (lane & 4) ? z2 : z1;
      if ((lane & 6) == 0) LF[L_REDQ + ta * 16 + 8 * (lane & 1) + ((lane >> 3) & 7)] = z;
      LGKM0();
      if (lds_inc(L, L_CNTQ, lane) == 2u) {
        const float qv = (LF[L_REDQ + c16] + LF[L_REDQ + 16 + c16]) + LF[L_REDQ + 32 + c16];
        lds_wait<0>(L, L_TRDY, 1u);
        const unsigned tlo = lds_ld(L, L_TAG + 2), thi = lds_ld(L, L_TAG + 3);
        if (lane < 16) __builtin_amdgcn_raw_buffer_store_b64(v2u{(cvtpk(qv, qv) & 0xffffu) | (tlo << 16), thi}, rws, QG_OFF + (qs * 16 + lane) * 8, 0, AUX_SC1);
        CLOBBER();
        if (lane == 0) L[L_QPUB] = 1u;
        STAMP(1);
      }
    }
    lds_wait<1>(L, L_QPUB, 1u);
    if (is_ffn) {
      FFN_LOADS()
      float acc[16];
#pragma unroll
      for (int e = 0; e < 16; ++e) acc[e] = 0.f;
#pragma unroll
      for (int i = 0; i < 12; ++i) {
        const float xn = LF[L_XN + 192 * ta + 16 * i + rr];
#pragma unroll
        for (int e = 0; e < 4; ++e) {
          acc[2 * e] = fmaf(xn, bflo(wa[i][e]), acc[2 * e]); acc[2 * e + 1] = fmaf(xn, bfhi(wa[i][e]), acc[2 * e + 1]);
          acc[8 + 2 * e] = fmaf(xn, bflo(wb[i][e]), acc[8 + 2 * e]); acc[9 + 2 * e] = fmaf(xn, bfhi(wb[i][e]), acc[9 + 2 * e]);
        }
        asm volatile("" : "+v"(acc[0]), "+v"(acc[1]), "+v"(acc[2]), "+v"(acc[3]), "+v"(acc[4]), "+v"(acc[5]), "+v"(acc[6]), "+v"(acc[7]),
                          "+v"(acc[8]), "+v"(acc[9]), "+v"(acc[10]), "+v"(acc[11]), "+v"(acc[12]), "+v"(acc[13]), "+v"(acc[14]), "+v"(acc[15]));
      }
      v2u dn[32];
      {
        const __amdgpu_buffer_rsrc_t rd = __builtin_amdgcn_make_buffer_rsrc((void*)(a.wd + (size_t)ft * 32 * D * 2), 0, (FM & 1) ? 0 : 32 * D * 2, RSRC3);
        const int vo2 = tt < NCG ? tt * 8 : 0x7f000000;
#pragma unroll
        for (int k = 0; k < 32; ++k) dn[k] = __builtin_amdgcn_raw_buffer_load_b64(rd, vo2, k * D * 2, AUX_NT);
        __builtin_amdgcn_sched_barrier(0);
      }
      float u[8], v4[4], z[2];
#pragma unroll
      for (int e = 0; e < 8; ++e) u[e] = swap32_add(acc[e], acc[8 + e]);
#pragma unroll
      for (int e = 0; e < 4; ++e) v4[e] = swap16_add(u[e], u[4 + e]);
#pragma unroll
      for (int e = 0; e < 2; ++e) { const float t1 = v4[e] + dpp<ROR8>(v4[e]), t2 = v4[2 + e] + dpp<ROR8>(v4[2 + e]); z[e] = (lane & 8) ? t2 : t1; }
      const float t1 = z[0] + dpp<SHL4>(z[0]), t2 = z[1] + dpp<SHR4>(z[1]);
      const float zz = (lane & 4) ? t2 : t1;
      const int col = (lane & 32) + 8 * cc + ((lane >> 2) & 7);
      LF[L_RED + ta * 64 + col] = zz;
      LGKM0();
      lds_inc(L, L_CNTA, lane);
      lds_wait<1>(L, L_CNTA, 3u);
      const int u5 = lane & 31;
      const float gp = rbf((LF[L_RED + u5] + LF[L_RED + 64 + u5]) + LF[L_RED + 128 + u5]);
      const float up = rbf((LF[L_RED + 32 + u5] + LF[L_RED + 96 + u5]) + LF[L_RED + 160 + u5]);
      const float h = rbf(rbf(silu_f(gp)) * up);
      f32x4 y = {0.f, 0.f, 0.f, 0.f};
#pragma unroll
      for (int k = 0; k < 32; ++k) {
        const float hk = asf(__builtin_amdgcn_readlane(asu(h), k));
        y.x = fmaf(hk, bflo(dn[k].x), y.x); y.y = fmaf(hk, bfhi(dn[k].x), y.y); y.z = fmaf(hk, bflo(dn[k].y), y.z); y.w = fmaf(hk, bfhi(dn[k].y), y.w);
      }
      if (tt < NCG && !(FM & 4)) __builtin_amdgcn_raw_buffer_store_b128(__builtin_bit_cast(v4u, y), rws, FP_OFF + ft * (D * 4) + tt * 16, 0, AUX_SC1);
      STAMP(6);
    }
    DRAIN();
    lds_inc(L, L_CNTD, lane);
    const __amdgpu_buffer_rsrc_t ro = __builtin_amdgcn_make_buffer_rsrc((void*)(a.wo + (size_t)b * 16 * D * 2), 0, 16 * D * 2, RSRC3);
    v2u wr[16];
    {
      const int vo2 = tt < NCG ? tt * 8 : 0x7f000000;
#pragma unroll
      for (int k = 0; k < 16; ++k) wr[k] = __builtin_amdgcn_raw_buffer_load_b64(ro, vo2, k * D * 2, AUX_NT);
    }
#pragma unroll
    for (int k = 0; k < 16; ++k) { wf[4 * k] = bflo(wr[k].x); wf[4 * k + 1] = bfhi(wr[k].x); wf[4 * k + 2] = bflo(wr[k].y); wf[4 * k + 3] = bfhi(wr[k].y); }
#pragma unroll
    for (int k = 0; k < 64; ++k) asm volatile("" : "+v"(wf[k]));
    STAMP(2);
  } else {
    __builtin_amdgcn_s_setprio(3);
    const u64 t_entry = POLL_DELAY ? __builtin_amdgcn_s_memrealtime() : 0;
    const v2u ev = __builtin_amdgcn_raw_buffer_load_b64(rws, E_OFF, 0, AUX_SC1);
    const unsigned cv = __builtin_amdgcn_raw_buffer_load_b32(rws, C_OFF + g * 256, 0, AUX_SC1);
    if (lane < L_NCTL) L[lane] = 0u;
    LGKM0();
    WG_BARRIER();
    const unsigned elo = __builtin_amdgcn_readfirstlane(ev.x), ehi = __builtin_amdgcn_readfirstlane(ev.y);
    const unsigned qlo = (elo ^ 0x7F4A7C15u) & 0xffffu, qhi = ((elo ^ 0x7F4A7C15u) >> 16) | ((ehi ^ 0x9E3779B9u) << 16);
    const unsigned rlo = elo ^ 0xC2B2AE35u, rhi = ehi ^ 0x27D4EB2Fu;
    if (lane == 0) { L[L_TAG] = (elo ^ 0x85EBCA6Bu) | 1u; L[L_TAG + 1] = cv; L[L_TAG + 2] = qlo; L[L_TAG + 3] = qhi; L[L_TAG + 4] = elo; L[L_TAG + 5] = ehi; }
    LGKM0();
    if (lane == 0) L[L_TRDY] = 1u;
    STAMP(1);
    if (POLL_DELAY) { while (__builtin_amdgcn_s_memrealtime() - t_entry < POLL_DELAY) __builtin_amdgcn_s_sleep(2); }
    {
      const int oq = QG_OFF + g * 2048 + lane * 16;
      v4u q0 = {0u, ~qhi, 0u, 0u}, q1 = {0u, ~qhi, 0u, 0u};
#pragma nounroll
      for (unsigned sp = 0; sp < SPIN_CAP; ++sp) {
        CLOBBER();
        const bool ok0 = q0.y == qhi && q0.w == qhi && (q0.x >> 16) == qlo && (q0.z >> 16) == qlo;
        const bool ok1 = q1.y == qhi && q1.w == qhi && (q1.x >> 16) == qlo && (q1.z >> 16) == qlo;
        if (__all(ok0 && ok1)) break;
        const v4u n0 = __builtin_amdgcn_raw_buffer_load_b128(rws, ok0 ? 0x7f000000 : oq, 0, AUX_SC1);
        const v4u n1 = __builtin_amdgcn_raw_buffer_load_b128(rws, ok1 ? 0x7f000000 : oq + 1024, 0, AUX_SC1);
        q0 = ok0 ? q0 : n0; q1 = ok1 ? q1 : n1;
      }
      L[L_Q + lane] = (q0.x & 0xffffu) | (q0.z << 16);
      L[L_Q + 64 + lane] = (q1.x & 0xffffu) | (q1.z << 16);
      LGKM0();
      if (lane == 0) L[L_QRDY] = 1u;
    }
    STAMP(2);
    const v4u zero = {0u, 0u, 0u, 0u};
    __builtin_amdgcn_raw_buffer_store_b128(zero, rws, p1_off0, 0, AUX_SC1);
    __builtin_amdgcn_raw_buffer_store_b128(zero, rws, p1_off1, 0, AUX_SC1);
#pragma unroll
    for (int i = 0; i < 5; ++i) {
      const int e = i * 8 + (lane >> 3);
      if (e < NFC) __builtin_amdgcn_raw_buffer_store_b128(zero, rws, P2_OFF + (e * NWG + b) * 128 + (lane & 7) * 16, 0, AUX_SC1);
    }
    lds_wait<8>(L, L_CNTD, 3u);
    DRAIN();
    if (lane == 0) __builtin_amdgcn_raw_buffer_store_b64(v2u{rlo, rhi}, rws, RDY_OFF + b * 8, 0, AUX_SC1);
    STAMP(3);
#pragma nounroll
    for (unsigned sp = 0; sp < SPIN_CAP; ++sp) {
      __builtin_amdgcn_s_sleep(100);
      CLOBBER();
      const v4u f0 = __builtin_amdgcn_raw_buffer_load_b128(rws, RDY_OFF + lane * 16, 0, AUX_SC1);
      const v4u f1 = __builtin_amdgcn_raw_buffer_load_b128(rws, RDY_OFF + 1024 + lane * 16, 0, AUX_SC1);
      const bool ok = f0.x == rlo && f0.y == rhi && f0.z == rlo && f0.w == rhi && f1.x == rlo && f1.y == rhi && f1.z == rlo && f1.w == rhi;
      if (__all(ok)) break;
    }
    CLOBBER();
    if (lane == 0) L[L_GO] = 1u;
    STAMP(4);
    if (b < NFC) {
      const int fh = b >= 18 ? 1 : 0, fl = b - 18 * fh;
      const int of = FP_OFF + (lane >> 3) * (D * 4) + fl * 128 + (lane & 7) * 16 + fh * 8;
      v2u f[18];
#pragma unroll
      for (int i = 0; i < 18; ++i) f[i] = __builtin_amdgcn_raw_buffer_load_b64(rws, of, i * 8 * D * 4, AUX_SC1);
      const unsigned xv = *(const unsigned*)(a.x + 32 * fl + 4 * (lane & 7) + 2 * fh);
      float y0 = 0.f, y1 = 0.f;
#pragma unroll
      for (int i = 0; i < 18; ++i) { y0 += asf(f[i].x); y1 += asf(f[i].y); }
      y0 += dpp<ROR8>(y0); y1 += dpp<ROR8>(y1);
      y0 = xrow_sum(y0); y1 = xrow_sum(y1);
      if (lane < 8) *(LAS f32x4*)(LF + L_G + 4 * lane) = f32x4{rbf(silu_f(rbf(y0))), rbf(silu_f(rbf(y1))), bflo(xv), bfhi(xv)};
      LGKM0();
      if (lane == 0) L[L_FFR] = 1u;
    }
    STAMP(5);
  }

  int pass = (role == 2 && DRYRUN) ? 0 : 1;
#pragma nounroll
  for (; pass < 2; ++pass) {
    pass = __builtin_amdgcn_readfirstlane(pass); role = __builtin_amdgcn_readfirstlane(role);
    asm volatile("" : "+s"(pass), "+s"(role));
    const bool dry = pass == 0;
    const __amdgpu_buffer_rsrc_t rwt = __builtin_amdgcn_make_buffer_rsrc((void*)a.ws, 0, dry ? 0 : WS_BYTES, RSRC3);
    volatile LAS unsigned* LT = L + (dry ? L_DRY : 0);
    LAS float* LTF = LF + (dry ? L_DRY : 0);
    const unsigned cap = dry ? 1u : SPIN_CAP;
    if (role == 0 || dry) {
      float lw = s.l;
      lw += dpp<ROR4>(lw); lw += dpp<ROR8>(lw); lw = xrow_sum(lw);
      *(LAS f32x4*)(LTF + L_ACC + (sw * 64 + lane) * 4) = s.acc;
      if (lane == 0 || lane == 2) { LTF[L_ML + sw * 4 + lane] = s.m; LTF[L_ML + sw * 4 + lane + 1] = lw; }
      if (lane == 0) LT[L_SDONE] = 1u;
      LGKM0();
      if (lds_inc(LT, L_CNTS, lane) == 3u || dry) {
        f32x4 ml[4], ac[4];
#pragma unroll
        for (int i = 0; i < 4; ++i) { ml[i] = *(LAS f32x4*)(LTF + L_ML + i * 4); ac[i] = *(LAS f32x4*)(LTF + L_ACC + (i * 64 + lane) * 4); }
        const float M0 = fmaxf(fmaxf(ml[0].x, ml[1].x), fmaxf(ml[2].x, ml[3].x)), M1 = fmaxf(fmaxf(ml[0].z, ml[1].z), fmaxf(ml[2].z, ml[3].z));
        float L0 = 0.f, L1 = 0.f;
        f32x4 o = {0.f, 0.f, 0.f, 0.f};
#pragma unroll
        for (int i = 0; i < 4; ++i) {
          const float w0 = __builtin_amdgcn_exp2f(ml[i].x - M0), w1 = __builtin_amdgcn_exp2f(ml[i].z - M1);
          L0 = fmaf(w0, ml[i].y, L0); L1 = fmaf(w1, ml[i].w, L1);
          o.x = fmaf(w0, ac[i].x, o.x); o.y = fmaf(w0, ac[i].y, o.y); o.z = fmaf(w1, ac[i].z, o.z); o.w = fmaf(w1, ac[i].w, o.w);
        }
        __builtin_amdgcn_raw_buffer_store_b128(v4u{asu(o.x), asu(L0), asu(o.y), ~asu(M0)}, rwt, p1_off0, 0, AUX_SC1);
        __builtin_amdgcn_raw_buffer_store_b128(v4u{asu(o.z), asu(L1), asu(o.w), ~asu(M1)}, rwt, p1_off1, 0, AUX_SC1);
        if (!dry) STAMP(4);
      }
    }
    if (role == 2) {
      lds_wait<4>(LT, L_SDONE, dry ? 0u : 1u);
      if (!dry) STAMP(6);
      const int o1 = P1_OFF + (g * 16 + j) * 2048 + lane * 16;
      v4u pa = {0u, 0u, 0u, 0u}, pb = {0u, 0u, 0u, 0u};
#pragma nounroll
      for (unsigned sp = 0; sp < cap; ++sp) {
        CLOBBER();
        const bool oka = pa.y != 0u && pa.w != 0u, okb = pb.y != 0u && pb.w != 0u;
        if (__all(oka && okb)) break;
        const v4u n0 = __builtin_amdgcn_raw_buffer_load_b128(rwt, oka ? 0x7f000000 : o1, 0, AUX_SC1);
        const v4u n1 = __builtin_amdgcn_raw_buffer_load_b128(rwt, okb ? 0x7f000000 : o1 + 1024, 0, AUX_SC1);
        pa = oka ? pa : n0; pb = okb ? pb : n1;
      }
      const float m_a = asf(~pa.w), m_b = asf(~pb.w);
      float M = fmaxf(m_a, m_b);
      M = fmaxf(M, dpp<ROR8>(M)); M = xrow_max(M);
      const float wa_ = __builtin_amdgcn_exp2f(m_a - M), wb_ = __builtin_amdgcn_exp2f(m_b - M);
      float Ls = fmaf(wa_, asf(pa.y), wb_ * asf(pb.y));
      float o0 = fmaf(wa_, asf(pa.x), wb_ * asf(pb.x)), o1v = fmaf(wa_, asf(pa.z), wb_ * asf(pb.z));
      Ls += dpp<ROR8>(Ls); o0 += dpp<ROR8>(o0); o1v += dpp<ROR8>(o1v);
      Ls = xrow_sum(Ls); o0 = xrow_sum(o0); o1v = xrow_sum(o1v);
      const unsigned opk = cvtpk(o0 / Ls, o1v / Ls);
      if (lane < 8) LT[L_O + lane] = opk;
      LGKM0();
      if (lane == 0) LT[L_ORDY] = 1u;
      if (!dry) STAMP(7);
    }
    if (role == 1 || dry) {
      lds_wait<8>(LT, L_SDONE, dry ? 0u : 1u);
      if (!dry) STAMP(3);
      lds_wait<0>(LT, L_ORDY, dry ? 0u : 1u);
      if (!dry) STAMP(4);
      const unsigned opk = LT[L_O + (lane & 7)];
      f32x4 y = {0.f, 0.f, 0.f, 0.f};
#pragma unroll
      for (int k = 0; k < 8; ++k) {
        const unsigned sp = __builtin_amdgcn_readlane(opk, k);
        const float e0 = bflo(sp), e1 = bfhi(sp);
        y.x = fmaf(e0, wf[8 * k], y.x); y.y = fmaf(e0, wf[8 * k + 1], y.y); y.z = fmaf(e0, wf[8 * k + 2], y.z); y.w = fmaf(e0, wf[8 * k + 3], y.w);
        y.x = fmaf(e1, wf[8 * k + 4], y.x); y.y = fmaf(e1, wf[8 * k + 5], y.y); y.z = fmaf(e1, wf[8 * k + 6], y.z); y.w = fmaf(e1, wf[8 * k + 7], y.w);
      }
      const int o2 = (unsigned)tt < (unsigned)NCG ? P2_OFF + ((tt >> 3) * NWG + b) * 128 + (tt & 7) * 16 : 0x7f000000;
      __builtin_amdgcn_raw_buffer_store_b128(v4u{asu(y.x), 1u, asu(y.y), 1u}, rwt, o2, 0, AUX_SC1);
      __builtin_amdgcn_raw_buffer_store_b128(v4u{asu(y.z), 1u, asu(y.w), 1u}, rwt, o2, 18 * NWG * 128, AUX_SC1);
      lds_inc(LT, L_YD, lane);
      if (!dry) STAMP(5);
    }
    if ((role == 0 || dry) && b < NFC) {
      lds_wait<1>(LT, L_YD, dry ? 0u : 3u);
      const int off = P2_OFF + (b * NWG + 64 * sw) * 128 + lane * 16;
      v4u u[8];
#pragma unroll
      for (int i = 0; i < 8; ++i) u[i] = v4u{0u, 0u, 0u, 0u};
#pragma nounroll
      for (unsigned sp = 0; sp < cap; ++sp) {
        CLOBBER();
        bool ok[8], all = true;
#pragma unroll
        for (int i = 0; i < 8; ++i) { ok[i] = (u[i].y & u[i].w) != 0u; all = all && ok[i]; }
        if (__all(all)) break;
        v4u nw[8];
#pragma unroll
        for (int i = 0; i < 8; ++i) nw[i] = __builtin_amdgcn_raw_buffer_load_b128(rwt, ok[i] ? 0x7f000000 : off, i * 1024, AUX_SC1);
#pragma unroll
        for (int i = 0; i < 8; ++i) u[i] = ok[i] ? u[i] : nw[i];
      }
      if (!dry) STAMP(5);
      float y0 = 0.f, y1 = 0.f;
#pragma unroll
      for (int i = 0; i < 8; ++i) { y0 += asf(u[i].x); y1 += asf(u[i].z); }
      y0 += dpp<ROR8>(y0); y1 += dpp<ROR8>(y1);
      y0 = xrow_sum(y0); y1 = xrow_sum(y1);
      if (lane < 8) *(LAS f32x2*)(LTF + L_FIN + sw * 16 + 2 * lane) = f32x2{y0, y1};
      LGKM0();
      if (lds_inc(LT, L_CNTF, lane) == 3u || dry) {
        lds_wait<1>(LT, L_FFR, dry ? 0u : 1u);
        const int c = lane & 7;
        const f32x2 f0 = *(LAS f32x2*)(LTF + L_FIN + 2 * c), f1 = *(LAS f32x2*)(LTF + L_FIN + 16 + 2 * c), f2 = *(LAS f32x2*)(LTF + L_FIN + 32 + 2 * c),
                    f3 = *(LAS f32x2*)(LTF + L_FIN + 48 + 2 * c);
        const f32x4 gx = *(LAS f32x4*)(LTF + L_G + 4 * c);
        const float sv0 = rbf((f0.x + f1.x) + (f2.x + f3.x)), sv1 = rbf((f0.y + f1.y) + (f2.y + f3.y));
        const unsigned pk = cvtpk(gx.z + rbf(sv0 * gx.x), gx.w + rbf(sv1 * gx.y));
        const int fh = b >= 18 ? 1 : 0, fl = b - 18 * fh;
        const __amdgpu_buffer_rsrc_t rout = __builtin_amdgcn_make_buffer_rsrc((void*)a.out, 0, dry ? 0 : D * 2, RSRC3);
        __builtin_amdgcn_raw_buffer_store_b32(pk, rout, lane < 8 ? (32 * fl + 4 * lane + 2 * fh) * 2 : 0x7f000000, 0, 0);
        const u64 e = (((u64)L[L_TAG + 5] << 32) | L[L_TAG + 4]) * 6364136223846793005ull + 1442695040888963407ull;
        __builtin_amdgcn_raw_buffer_store_b64(v2u{(unsigned)e, (unsigned)(e >> 32)}, rwt, (b == 0 && lane == 0) ? E_OFF : 0x7f000000, 0, AUX_SC1);
        if (!dry) STAMP(6);
      }
    }
  }
}

}

extern "C" void kernel_launch(void* const* d_in, const int* in_sizes, int n_in, void* d_out, int out_size, void* d_ws, size_t ws_size,
                              hipStream_t stream) {
  Args a;
  a.x = (const unsigned short*)d_in[0];
  a.k = (const unsigned char*)d_in[1];
  a.v = (const unsigned char*)d_in[2];
  a.wq = (const unsigned char*)d_in[3];
  a.wo = (const unsigned char*)d_in[4];
  a.wg = (const unsigned char*)d_in[6];
  a.wu = (const unsigned char*)d_in[7];
  a.wd = (const unsigned char*)d_in[8];
  a.out = (unsigned short*)d_out;
  a.ws = (unsigned char*)d_ws;
  hipLaunchKernelGGL(fused_decode_kernel, dim3(NWG), dim3(NTHR), 0, stream, a);
}
```

```cpp
#include <hip/hip_runtime.h>
#include <hip/hip_bf16.h>
#include <stdint.h>

namespace {

typedef unsigned v4u __attribute__((ext_vector_type(4)));
typedef unsigned v2u __attribute__((ext_vector_type(2)));
typedef float f32x4 __attribute__((ext_vector_type(4)));
typedef float f32x2 __attribute__((ext_vector_type(2)));
typedef __bf16 bf16x8 __attribute__((ext_vector_type(8)));
typedef __bf16 bf16x2 __attribute__((ext_vector_type(2)));
typedef unsigned long long u64;
#define LAS __attribute__((address_space(3)))
#define GAS __attribute__((address_space(1)))

constexpr int D = 576, HKV = 16, SK = 65536, DH = 128, FF = 4608, QN = 4096;
constexpr int NWG = 256, NTHR = 512;
constexpr int NT = SK / 32;
#define NS_TILES 20
constexpr int NS = NS_TILES;
constexpr int DYN0 = NS * 64;
#define QK_DOT2 1
#define FFN_WHEN 2
#define POLL_DELAY 0
#define FM 0
#define PACE_W 8
#define DRYRUN 1
constexpr unsigned HEAD_BYTES = (unsigned)SK * DH * 2;
constexpr int NCG = D / 4;
constexpr int NFC = 36;
constexpr int NFT = FF / 32;

constexpr int E_OFF = 0;
constexpr int C_OFF = 256;
constexpr int RDY_OFF = 8192;
constexpr int QG_OFF = 16384;
constexpr int P1_OFF = 65536;
constexpr int FP_OFF = 589824;
constexpr int P2_OFF = 1048576;
constexpr int WS_BYTES = P2_OFF + NFC * NWG * 128;
constexpr int ST_OFF = 4 << 20;

#define AUXW 2
constexpr int AUX_NT = AUXW, AUX_SC1 = 16;
constexpr unsigned RSRC3 = 0x00020000u;
constexpr unsigned SPIN_CAP = 1u << 17;

constexpr int L_QRDY = 0, L_TRDY = 1, L_CNTA = 2, L_CNTD = 3, L_CNTS = 4, L_CNTF = 5, L_GO = 6, L_FFR = 7, L_SDONE = 8, L_YD = 9, L_QPUB = 10, L_ORDY = 11, L_CNTQ = 12;
constexpr int L_NCTL = 16;
constexpr int L_TAG = 16;
constexpr int L_Q = 32;
constexpr int L_RED = 192;
constexpr int L_O = 384;
constexpr int L_ML = 400;
constexpr int L_REDQ = 432;
constexpr int L_G = 2304;
constexpr int L_FIN = 2368;
constexpr int L_ACC = 512;
constexpr int L_XN = 1536;
constexpr int L_DRY = 4096;
constexpr int L_TOTAL = 24 * 1024;

constexpr float SCALE = 0.08838834764831845f;
constexpr float LOG2E = 1.4426950408889634f;

__device__ __forceinline__ unsigned cvtpk(float lo, float hi) {
  f32x2 v = {lo, hi};
  return __builtin_bit_cast(unsigned, __builtin_convertvector(v, bf16x2));
}
__device__ __forceinline__ float bflo(unsigned u) { return __builtin_bit_cast(float, u << 16); }
__device__ __forceinline__ float bfhi(unsigned u) { return __builtin_bit_cast(float, u & 0xffff0000u); }
__device__ __forceinline__ float rbf(float x) { return bflo(cvtpk(x, x)); }
__device__ __forceinline__ float asf(unsigned u) { return __builtin_bit_cast(float, u); }
__device__ __forceinline__ unsigned asu(float f) { return __builtin_bit_cast(unsigned, f); }

template <int CTRL> __device__ __forceinline__ float dpp(float x) {
  return __builtin_bit_cast(float, __builtin_amdgcn_mov_dpp(__builtin_bit_cast(int, x), CTRL, 0xf, 0xf, true));
}
constexpr int XOR1 = 0xB1, XOR2 = 0x4E, HMIR = 0x141, ROR8 = 0x128, ROR4 = 0x124, SHL4 = 0x104, SHR4 = 0x114, QB0 = 0x00, QB2 = 0xAA;

__device__ __forceinline__ float xrow_max(float x) {
  auto s = __builtin_amdgcn_permlane16_swap(asu(x), asu(x), false, false);
  x = fmaxf(asf(s[0]), asf(s[1]));
  auto t = __builtin_amdgcn_permlane32_swap(asu(x), asu(x), false, false);
  return fmaxf(asf(t[0]), asf(t[1]));
}
__device__ __forceinline__ float xrow_sum(float x) {
  auto s = __builtin_amdgcn_permlane16_swap(asu(x), asu(x), false, false);
  x = asf(s[0]) + asf(s[1]);
  auto t = __builtin_amdgcn_permlane32_swap(asu(x), asu(x), false, false);
  return asf(t[0]) + asf(t[1]);
}
__device__ __forceinline__ float wave_sum(float x) {
  x += dpp<XOR1>(x); x += dpp<XOR2>(x); x += dpp<HMIR>(x); x += dpp<ROR8>(x);
  return xrow_sum(x);
}
__device__ __forceinline__ float swap32_add(float a, float b) {
  auto s = __builtin_amdgcn_permlane32_swap(asu(a), asu(b), false, false);
  return asf(s[0]) + asf(s[1]);
}
__device__ __forceinline__ float swap16_add(float a, float b) {
  auto s = __builtin_amdgcn_permlane16_swap(asu(a), asu(b), false, false);
  return asf(s[0]) + asf(s[1]);
}
__device__ __forceinline__ f32x4 mfma(v4u a, v4u b, f32x4 c) {
  return __builtin_amdgcn_mfma_f32_16x16x32_bf16(__builtin_bit_cast(bf16x8, a), __builtin_bit_cast(bf16x8, b), c, 0, 0, 0);
}
__device__ __forceinline__ float dot2(unsigned a, unsigned b, float c) {
  return __builtin_amdgcn_fdot2_f32_bf16(__builtin_bit_cast(bf16x2, a), __builtin_bit_cast(bf16x2, b), c, false);
}
__device__ __forceinline__ float silu_f(float x) { return x / (1.0f + __expf(-x)); }

#define WG_BARRIER() do { __builtin_amdgcn_sched_barrier(0); __builtin_amdgcn_s_barrier(); __builtin_amdgcn_sched_barrier(0); } while (0)
#define CLOBBER() asm volatile("" ::: "memory")
#define LGKM0() asm volatile("s_waitcnt lgkmcnt(0)" ::: "memory")
#define DRAIN() asm volatile("s_waitcnt vmcnt(0)" ::: "memory")

__device__ __forceinline__ unsigned lds_ld(volatile LAS unsigned* L, int i) { return __builtin_amdgcn_readfirstlane(L[i]); }
template <int SLEEP> __device__ __forceinline__ void lds_wait(volatile LAS unsigned* L, int i, unsigned want) {
#pragma nounroll
  for (unsigned s = 0; s < (SPIN_CAP << 3); ++s) {
    if (lds_ld(L, i) >= want) break;
    __builtin_amdgcn_s_sleep(SLEEP);
  }
  CLOBBER();
}
__device__ __forceinline__ unsigned lds_inc(volatile LAS unsigned* L, int i, int lane) {
  unsigned r = 0;
  if (lane == 0) r = __hip_atomic_fetch_add((LAS unsigned*)(L + i), 1u, __ATOMIC_RELAXED, __HIP_MEMORY_SCOPE_WORKGROUP);
  r = __builtin_amdgcn_readfirstlane(r);
  CLOBBER();
  return r;
}

struct Args {
  const unsigned short* x;
  const unsigned char* k;
  const unsigned char* v;
  const unsigned char* wq;
  const unsigned char* wo;
  const unsigned char* wg;
  const unsigned char* wu;
  const unsigned char* wd;
  unsigned short* out;
  unsigned char* ws;
};

#define STAMP(i) do { } while (0)

#define KOFF(i) (((i) >> 2) * 4096 + (((i) >> 1) & 1) * 1024 + ((i) & 1) * 128)
struct Tile { v4u k[8]; v4u v[8]; };

struct SState {
  f32x4 acc;
  float m, l;
  v4u fa, fb;
};

template <int QD> __device__ __forceinline__ void build_frag(v4u& f, unsigned pb) {
  constexpr int r0 = (0 - QD) & 3, r1 = (1 - QD) & 3, r2 = (2 - QD) & 3, r3 = (3 - QD) & 3;
  f.x = __builtin_amdgcn_update_dpp(f.x, pb, r0 ? 0x120 + 4 * r0 : 0xE4, 0xf, 0x1, false);
  f.y = __builtin_amdgcn_update_dpp(f.y, pb, r1 ? 0x120 + 4 * r1 : 0xE4, 0xf, 0x2, false);
  f.z = __builtin_amdgcn_update_dpp(f.z, pb, r2 ? 0x120 + 4 * r2 : 0xE4, 0xf, 0x4, false);
  f.w = __builtin_amdgcn_update_dpp(f.w, pb, r3 ? 0x120 + 4 * r3 : 0xE4, 0xf, 0x8, false);
}

__device__ __forceinline__ float pick_bank(f32x4 d) {
  unsigned p = asu(d.x);
  p = __builtin_amdgcn_update_dpp(p, asu(d.y), 0xE4, 0xf, 0x2, false);
  p = __builtin_amdgcn_update_dpp(p, asu(d.z), 0xE4, 0xf, 0x4, false);
  p = __builtin_amdgcn_update_dpp(p, asu(d.w), 0xE4, 0xf, 0x8, false);
  return asf(p);
}

#define AUXK 2
#define AUXV 2
__device__ __forceinline__ v4u ldk(__amdgpu_buffer_rsrc_t r, int voff, unsigned soff) {
  return __builtin_amdgcn_raw_buffer_load_b128(r, voff, soff, AUXK);
}
__device__ __forceinline__ v4u ldv(__amdgpu_buffer_rsrc_t r, int voff, unsigned soff) {
  return __builtin_amdgcn_raw_buffer_load_b128(r, voff, soff, AUXV);
}

template <bool REFILL>
__device__ __forceinline__ void process_tile(Tile& t, SState& s, const v4u (&qf)[4], __amdgpu_buffer_rsrc_t rk, __amdgpu_buffer_rsrc_t rv,
                                             int voffk, int voffv, unsigned offn, unsigned halfmask, bool hsel) {
  float x[4][2];
#pragma unroll
  for (int u = 0; u < 4; ++u) {
    float a0 = 0.f, a1 = 0.f;
#pragma unroll
    for (int hf = 0; hf < 2; ++hf) {
#pragma unroll
      for (int e = 0; e < 4; ++e) { a0 = dot2(t.k[2 * u + hf][e], qf[hf][e], a0); a1 = dot2(t.k[2 * u + hf][e], qf[2 + hf][e], a1); }
      asm volatile("" : "+v"(a0), "+v"(a1));
      if (REFILL) { t.k[2 * u + hf] = ldk(rk, voffk + KOFF(2 * u + hf), offn); __builtin_amdgcn_sched_barrier(0); }
    }
    x[u][0] = a0; x[u][1] = a1;
  }
  float pp[2];
#pragma unroll
  for (int v = 0; v < 2; ++v) {
    float y[2];
#pragma unroll
    for (int h = 0; h < 2; ++h) {
      const float t1 = x[2 * v][h] + dpp<HMIR>(x[2 * v][h]), t2 = x[2 * v + 1][h] + dpp<HMIR>(x[2 * v + 1][h]);
      y[h] = asf(__builtin_amdgcn_update_dpp(asu(t1), asu(t2), 0xE4, 0xf, 0xA, false));
    }
    const float t1 = y[0] + dpp<XOR2>(y[0]), t2 = y[1] + dpp<XOR2>(y[1]);
    const float z = hsel ? t2 : t1;
    pp[v] = z + dpp<XOR1>(z);
  }
  const float p0 = pp[0], p1 = pp[1];
  unsigned pk = cvtpk(p0, p1);
  pk = cvtpk(bflo(pk) * SCALE, bfhi(pk) * SCALE);
  const float s0 = bflo(pk) * LOG2E, s1 = bfhi(pk) * LOG2E;
  float tm = fmaxf(s0, s1);
  tm = fmaxf(tm, dpp<ROR4>(tm));
  tm = fmaxf(tm, dpp<ROR8>(tm));
  tm = xrow_max(tm);
  const float mn = fmaxf(s.m, ceilf(tm));
  const float alpha = __builtin_amdgcn_exp2f(s.m - mn);
  const float e0 = __builtin_amdgcn_exp2f(s0 - mn);
  const float e1 = __builtin_amdgcn_exp2f(s1 - mn);
  s.m = mn;
  s.l = fmaf(s.l, alpha, e0 + e1);
  const float a0 = dpp<QB0>(alpha), a1 = dpp<QB2>(alpha);
  s.acc.x *= a0; s.acc.y *= a0; s.acc.z *= a1; s.acc.w *= a1;
  const unsigned pb0 = cvtpk(e0, e0) & halfmask, pb1 = cvtpk(e1, e1) & halfmask;
#define PV_STEP(IDX, F, PB, QD) \
  build_frag<QD>(F, PB); \
  s.acc = mfma(F, t.v[IDX], s.acc); \
  if (REFILL) { t.v[IDX] = ldv(rv, voffv + (IDX) * 1024, offn); __builtin_amdgcn_sched_barrier(0); }
  PV_STEP(0, s.fa, pb0, 0) PV_STEP(1, s.fb, pb0, 1) PV_STEP(2, s.fa, pb0, 2) PV_STEP(3, s.fb, pb0, 3)
  PV_STEP(4, s.fa, pb1, 0) PV_STEP(5, s.fb, pb1, 1) PV_STEP(6, s.fa, pb1, 2) PV_STEP(7, s.fb, pb1, 3)
#undef PV_STEP
}

__global__ void __launch_bounds__(NTHR, 2) fused_decode_kernel(Args a) {
  __shared__ __attribute__((aligned(16))) unsigned lds_raw[L_TOTAL];
  volatile LAS unsigned* L = (volatile LAS unsigned*)lds_raw;
  LAS float* LF = (LAS float*)lds_raw;
  const int tid = threadIdx.x, lane = tid & 63, w = __builtin_amdgcn_readfirstlane(tid >> 6);
  const int b = blockIdx.x, g = b >> 4, j = b & 15;
  const __amdgpu_buffer_rsrc_t rws = __builtin_amdgcn_make_buffer_rsrc((void*)a.ws, 0, WS_BYTES, RSRC3);
  const int r4 = lane >> 4, c16 = lane & 15;
  const int p1_off0 = P1_OFF + ((g * 16 + (c16 >> 1)) * 16 + j) * 128 + (8 * (c16 & 1) + 2 * r4) * 8;
  const int p1_off1 = p1_off0 + 8 * 16 * 128;
  const int slot = ((j & 7) - (g >> 1)) & 7, hi = j >> 3;
  const int tt = tid - 64;
  const int sw = w & 3;
  int role = w == 0 ? 2 : w < 4 ? 1 : 0;
  STAMP(0);

  SState s;
  s.acc = f32x4{0.f, 0.f, 0.f, 0.f};
  s.m = -INFINITY; s.l = 0.f;
  float wf[64];

  if (role == 0) {
    const __amdgpu_buffer_rsrc_t rk = __builtin_amdgcn_make_buffer_rsrc((void*)(a.k + (size_t)g * HEAD_BYTES), 0, HEAD_BYTES, RSRC3);
    const __amdgpu_buffer_rsrc_t rv = __builtin_amdgcn_make_buffer_rsrc((void*)(a.v + (size_t)g * HEAD_BYTES), 0, HEAD_BYTES, RSRC3);
    const __amdgpu_buffer_rsrc_t rc = __builtin_amdgcn_make_buffer_rsrc((void*)(a.ws + C_OFF + g * 256), 0, 4, RSRC3);
    const int voffk = (8 * (c16 >> 3) + r4) * 256 + (lane & 7) * 16, voffv = lane * 16;
    const unsigned sb = (unsigned)(j * NS * 4 + sw) * 8192u;
    Tile tb;
    WG_BARRIER();
    {
      unsigned qr = 0;
#pragma unroll
      for (int i = 0; i < 16; ++i) {
        if (i >= PACE_W && !qr) { asm volatile("s_waitcnt vmcnt(%0)" ::"n"(PACE_W - 1) : "memory"); qr = lds_ld(L, L_QRDY); }
        if (i < 8) tb.k[i] = ldk(rk, voffk + KOFF(i), sb);
        else tb.v[i - 8] = ldv(rv, voffv + (i - 8) * 1024, sb);
        __builtin_amdgcn_sched_barrier(0);
      }
    }
    lds_wait<1>(L, L_QRDY, 1u);
    STAMP(1);
    v4u qf[4];
#pragma unroll
    for (int i = 0; i < 4; ++i) qf[i] = *(LAS v4u*)(lds_raw + L_Q + 32 * i + 4 * (lane & 7));
    const unsigned c0 = lds_ld(L, L_TAG + 1);
    const unsigned halfmask = (lane & 1) ? 0xffff0000u : 0x0000ffffu;
    s.fa = v4u{0u, 0u, 0u, 0u}; s.fb = v4u{0u, 0u, 0u, 0u};
    unsigned offn = sb + 32768u;
#pragma nounroll
    for (int n = 2;; ++n) {
      if (n == NS) { STAMP(2); lds_wait<1>(L, L_GO, 1u); }
      const unsigned tk = __builtin_amdgcn_raw_ptr_buffer_atomic_add_i32(1, rc, lane * 4, n >= NS ? 0 : 4, 0);
      process_tile<true>(tb, s, qf, rk, rv, voffk, voffv, offn, halfmask, (lane & 2) != 0);
      if (offn >= HEAD_BYTES) break;
      const unsigned idx = __builtin_amdgcn_readfirstlane(tk) - c0;
      const unsigned dyn = idx < (unsigned)(NT - DYN0) ? (DYN0 + idx) * 8192u : 0x7f000000u;
      offn = n >= NS ? dyn : sb + (unsigned)n * 32768u;
    }
    STAMP(3);
  } else if (role == 1) {
    const int ta = w - 1;
    __builtin_amdgcn_s_setprio(3);
    const bool is_ffn = slot < 4 || (slot == 4 && hi == 0);
    const int ft = 18 * (g >> 1) + (slot < 4 ? (g & 1) * 8 + 2 * slot + hi : 16 + (g & 1));
    const int qs = ((b & 7) * 8 + (b >> 5)) * 4 + ((b >> 3) & 3);
    const __amdgpu_buffer_rsrc_t rx = __builtin_amdgcn_make_buffer_rsrc((void*)a.x, 0, D * 2, RSRC3);
    const __amdgpu_buffer_rsrc_t rq = __builtin_amdgcn_make_buffer_rsrc((void*)(a.wq + qs * 32), 0, (D - 1) * QN * 2 + 32, RSRC3);
    const int rr = lane >> 2, cc = lane & 3;
    v4u wqr[6];
    const v4u xc0 = __builtin_amdgcn_raw_buffer_load_b128(rx, lane * 16, 0, 0);
    const v4u xc1 = __builtin_amdgcn_raw_buffer_load_b128(rx, 1024 + lane * 16, 0, 0);
    unsigned xo[3];
#pragma unroll
    for (int i = 0; i < 3; ++i) xo[i] = __builtin_amdgcn_raw_buffer_load_b16(rx, (192 * ta + 64 * i + lane) * 2, 0, 0);
    {
      const int vo = (192 * ta + (lane >> 1)) * (QN * 2) + (lane & 1) * 16;
#pragma unroll
      for (int i = 0; i < 6; ++i) wqr[i] = __builtin_amdgcn_raw_buffer_load_b128(rq, vo, i * 32 * QN * 2, AUX_NT);
    }
    const unsigned fspan = is_ffn ? (D - 1) * FF * 2 + 64 : 0;
    const __amdgpu_buffer_rsrc_t ra = __builtin_amdgcn_make_buffer_rsrc((void*)(a.wg + ft * 64), 0, fspan, RSRC3);
    const __amdgpu_buffer_rsrc_t rb = __builtin_amdgcn_make_buffer_rsrc((void*)(a.wu + ft * 64), 0, fspan, RSRC3);
    const int vof = (192 * ta + rr) * (FF * 2) + cc * 16;
    v4u wa[12], wb[12];
#define FFN_LOADS() \
    _Pragma("unroll") for (int i = 0; i < 12; ++i) { \
      wa[i] = __builtin_amdgcn_raw_buffer_load_b128(ra, vof, i * 16 * FF * 2, AUX_NT); \
      wb[i] = __builtin_amdgcn_raw_buffer_load_b128(rb, vof, i * 16 * FF * 2, AUX_NT); \
    } \
    __builtin_amdgcn_sched_barrier(0);
    WG_BARRIER();
    {
      const float x0 = bflo(__builtin_amdgcn_readfirstlane(xc0.x));
      float s0 = 0.f, s1 = 0.f, q0 = 0.f, q1 = 0.f;
#pragma unroll
      for (int i = 0; i < 4; ++i) {
        const float d0 = bflo(xc0[i]) - x0, d1 = bfhi(xc0[i]) - x0, d2 = bflo(xc1[i]) - x0, d3 = bfhi(xc1[i]) - x0;
        s0 += d0 + d1; s1 += d2 + d3;
        q0 = fmaf(d0, d0, q0); q0 = fmaf(d1, d1, q0); q1 = fmaf(d2, d2, q1); q1 = fmaf(d3, d3, q1);
      }
      const float dm = wave_sum(s0 + (lane < 8 ? s1 : 0.f)) * (1.0f / D);
      const float var = fmaxf(wave_sum(q0 + (lane < 8 ? q1 : 0.f)) * (1.0f / D) - dm * dm, 0.f);
      const float mb = rbf(x0 + dm), den = rbf(sqrtf(rbf(rbf(var) + 1e-5f)));
#pragma unroll
      for (int i = 0; i < 3; ++i) LF[L_XN + 192 * ta + 64 * i + lane] = rbf(rbf(bflo(xo[i]) - mb) / den);
      LGKM0();
    }
    {
      float acc[8];
#pragma unroll
      for (int e = 0; e < 8; ++e) acc[e] = 0.f;
#pragma unroll
      for (int i = 0; i < 6; ++i) {
        const float xn = LF[L_XN + 192 * ta + 32 * i + (lane >> 1)];
#pragma unroll
        for (int e = 0; e < 4; ++e) { acc[2 * e] = fmaf(xn, bflo(wqr[i][e]), acc[2 * e]); acc[2 * e + 1] = fmaf(xn, bfhi(wqr[i][e]), acc[2 * e + 1]); }
      }
      float u[4], v2[2];
#pragma unroll
      for (int e = 0; e < 4; ++e) u[e] = swap32_add(acc[e], acc[4 + e]);
#pragma unroll
      for (int e = 0; e < 2; ++e) v2[e] = swap16_add(u[e], u[2 + e]);
      const float t1 = v2[0] + dpp<ROR8>(v2[0]), t2 = v2[1] + dpp<ROR8>(v2[1]);
      float z = (lane & 8) ? t2 : t1;
      z += dpp<XOR2>(z);
      const float z1 = dpp<SHL4>(z), z2 = dpp<SHR4>(z);
      z += (lane & 4) ? z2 : z1;
      if ((lane & 6) == 0) LF[L_REDQ + ta * 16 + 8 * (lane & 1) + ((lane >> 3) & 7)] = z;
      LGKM0();
      if (lds_inc(L, L_CNTQ, lane) == 2u) {
        const float qv = (LF[L_REDQ + c16] + LF[L_REDQ + 16 + c16]) + LF[L_REDQ + 32 + c16];
        lds_wait<0>(L, L_TRDY, 1u);
        const unsigned tlo = lds_ld(L, L_TAG + 2), thi = lds_ld(L, L_TAG + 3);
        if (lane < 16) __builtin_amdgcn_raw_buffer_store_b64(v2u{(cvtpk(qv, qv) & 0xffffu) | (tlo << 16), thi}, rws, QG_OFF + (qs * 16 + lane) * 8, 0, AUX_SC1);
        CLOBBER();
        if (lane == 0) L[L_QPUB] = 1u;
        STAMP(1);
      }
    }
    lds_wait<1>(L, L_QPUB, 1u);
    if (is_ffn) {
      FFN_LOADS()
      float acc[16];
#pragma unroll
      for (int e = 0; e < 16; ++e) acc[e] = 0.f;
#pragma unroll
      for (int i = 0; i < 12; ++i) {
        const float xn = LF[L_XN + 192 * ta + 16 * i + rr];
#pragma unroll
        for (int e = 0; e < 4; ++e) {
          acc[2 * e] = fmaf(xn, bflo(wa[i][e]), acc[2 * e]); acc[2 * e + 1] = fmaf(xn, bfhi(wa[i][e]), acc[2 * e + 1]);
          acc[8 + 2 * e] = fmaf(xn, bflo(wb[i][e]), acc[8 + 2 * e]); acc[9 + 2 * e] = fmaf(xn, bfhi(wb[i][e]), acc[9 + 2 * e]);
        }
        asm volatile("" : "+v"(acc[0]), "+v"(acc[1]), "+v"(acc[2]), "+v"(acc[3]), "+v"(acc[4]), "+v"(acc[5]), "+v"(acc[6]), "+v"(acc[7]),
                          "+v"(acc[8]), "+v"(acc[9]), "+v"(acc[10]), "+v"(acc[11]), "+v"(acc[12]), "+v"(acc[13]), "+v"(acc[14]), "+v"(acc[15]));
      }
      v2u dn[32];
      {
        const __amdgpu_buffer_rsrc_t rd = __builtin_amdgcn_make_buffer_rsrc((void*)(a.wd + (size_t)ft * 32 * D * 2), 0, (FM & 1) ? 0 : 32 * D * 2, RSRC3);
        const int vo2 = tt < NCG ? tt * 8 : 0x7f000000;
#pragma unroll
        for (int k = 0; k < 32; ++k) dn[k] = __builtin_amdgcn_raw_buffer_load_b64(rd, vo2, k * D * 2, AUX_NT);
        __builtin_amdgcn_sched_barrier(0);
      }
      float u[8], v4[4], z[2];
#pragma unroll
      for (int e = 0; e < 8; ++e) u[e] = swap32_add(acc[e], acc[8 + e]);
#pragma unroll
      for (int e = 0; e < 4; ++e) v4[e] = swap16_add(u[e], u[4 + e]);
#pragma unroll
      for (int e = 0; e < 2; ++e) { const float t1 = v4[e] + dpp<ROR8>(v4[e]), t2 = v4[2 + e] + dpp<ROR8>(v4[2 + e]); z[e] = (lane & 8) ? t2 : t1; }
      const float t1 = z[0] + dpp<SHL4>(z[0]), t2 = z[1] + dpp<SHR4>(z[1]);
      const float zz = (lane & 4) ? t2 : t1;
      const int col = (lane & 32) + 8 * cc + ((lane >> 2) & 7);
      LF[L_RED + ta * 64 + col] = zz;
      LGKM0();
      lds_inc(L, L_CNTA, lane);
      lds_wait<1>(L, L_CNTA, 3u);
      const int u5 = lane & 31;
      const float gp = rbf((LF[L_RED + u5] + LF[L_RED + 64 + u5]) + LF[L_RED + 128 + u5]);
      const float up = rbf((LF[L_RED + 32 + u5] + LF[L_RED + 96 + u5]) + LF[L_RED + 160 + u5]);
      const float h = rbf(rbf(silu_f(gp)) * up);
      f32x4 y = {0.f, 0.f, 0.f, 0.f};
#pragma unroll
      for (int k = 0; k < 32; ++k) {
        const float hk = asf(__builtin_amdgcn_readlane(asu(h), k));
        y.x = fmaf(hk, bflo(dn[k].x), y.x); y.y = fmaf(hk, bfhi(dn[k].x), y.y); y.z = fmaf(hk, bflo(dn[k].y), y.z); y.w = fmaf(hk, bfhi(dn[k].y), y.w);
      }
      if (tt < NCG && !(FM & 4)) __builtin_amdgcn_raw_buffer_store_b128(__builtin_bit_cast(v4u, y), rws, FP_OFF + ft * (D * 4) + tt * 16, 0, AUX_SC1);
      STAMP(6);
    }
    DRAIN();
    lds_inc(L, L_CNTD, lane);
    const __amdgpu_buffer_rsrc_t ro = __builtin_amdgcn_make_buffer_rsrc((void*)(a.wo + (size_t)b * 16 * D * 2), 0, 16 * D * 2, RSRC3);
    v2u wr[16];
    {
      const int vo2 = tt < NCG ? tt * 8 : 0x7f000000;
#pragma unroll
      for (int k = 0; k < 16; ++k) wr[k] = __builtin_amdgcn_raw_buffer_load_b64(ro, vo2, k * D * 2, AUX_NT);
    }
#pragma unroll
    for (int k = 0; k < 16; ++k) { wf[4 * k] = bflo(wr[k].x); wf[4 * k + 1] = bfhi(wr[k].x); wf[4 * k + 2] = bflo(wr[k].y); wf[4 * k + 3] = bfhi(wr[k].y); }
#pragma unroll
    for (int k = 0; k < 64; ++k) asm volatile("" : "+v"(wf[k]));
    STAMP(2);
  } else {
    __builtin_amdgcn_s_setprio(3);
    const u64 t_entry = POLL_DELAY ? __builtin_amdgcn_s_memrealtime() : 0;
    const v2u ev = __builtin_amdgcn_raw_buffer_load_b64(rws, E_OFF, 0, AUX_SC1);
    const unsigned cv = __builtin_amdgcn_raw_buffer_load_b32(rws, C_OFF + g * 256, 0, AUX_SC1);
    if (lane < L_NCTL) L[lane] = 0u;
    LGKM0();
    WG_BARRIER();
    const unsigned elo = __builtin_amdgcn_readfirstlane(ev.x), ehi = __builtin_amdgcn_readfirstlane(ev.y);
    const unsigned qlo = (elo ^ 0x7F4A7C15u) & 0xffffu, qhi = ((elo ^ 0x7F4A7C15u) >> 16) | ((ehi ^ 0x9E3779B9u) << 16);
    const unsigned rlo = elo ^ 0xC2B2AE35u, rhi = ehi ^ 0x27D4EB2Fu;
    if (lane == 0) { L[L_TAG] = (elo ^ 0x85EBCA6Bu) | 1u; L[L_TAG + 1] = cv; L[L_TAG + 2] = qlo; L[L_TAG + 3] = qhi; L[L_TAG + 4] = elo; L[L_TAG + 5] = ehi; }
    LGKM0();
    if (lane == 0) L[L_TRDY] = 1u;
    STAMP(1);
    if (POLL_DELAY) { while (__builtin_amdgcn_s_memrealtime() - t_entry < POLL_DELAY) __builtin_amdgcn_s_sleep(2); }
    {
      const int oq = QG_OFF + g * 2048 + lane * 16;
      v4u q0 = {0u, ~qhi, 0u, 0u}, q1 = {0u, ~qhi, 0u, 0u};
#pragma nounroll
      for (unsigned sp = 0; sp < SPIN_CAP; ++sp) {
        CLOBBER();
        const bool ok0 = q0.y == qhi && q0.w == qhi && (q0.x >> 16) == qlo && (q0.z >> 16) == qlo;
        const bool ok1 = q1.y == qhi && q1.w == qhi && (q1.x >> 16) == qlo && (q1.z >> 16) == qlo;
        if (__all(ok0 && ok1)) break;
        const v4u n0 = __builtin_amdgcn_raw_buffer_load_b128(rws, ok0 ? 0x7f000000 : oq, 0, AUX_SC1);
        const v4u n1 = __builtin_amdgcn_raw_buffer_load_b128(rws, ok1 ? 0x7f000000 : oq + 1024, 0, AUX_SC1);
        q0 = ok0 ? q0 : n0; q1 = ok1 ? q1 : n1;
      }
      L[L_Q + lane] = (q0.x & 0xffffu) | (q0.z << 16);
      L[L_Q + 64 + lane] = (q1.x & 0xffffu) | (q1.z << 16);
      LGKM0();
      if (lane == 0) L[L_QRDY] = 1u;
    }
    STAMP(2);
    const v4u zero = {0u, 0u, 0u, 0u};
    __builtin_amdgcn_raw_buffer_store_b128(zero, rws, p1_off0, 0, AUX_SC1);
    __builtin_amdgcn_raw_buffer_store_b128(zero, rws, p1_off1, 0, AUX_SC1);
#pragma unroll
    for (int i = 0; i < 5; ++i) {
      const int e = i * 8 + (lane >> 3);
      if (e < NFC) __builtin_amdgcn_raw_buffer_store_b128(zero, rws, P2_OFF + (e * NWG + b) * 128 + (lane & 7) * 16, 0, AUX_SC1);
    }
    lds_wait<8>(L, L_CNTD, 3u);
    DRAIN();
    if (lane == 0) __builtin_amdgcn_raw_buffer_store_b64(v2u{rlo, rhi}, rws, RDY_OFF + b * 8, 0, AUX_SC1);
    STAMP(3);
#pragma nounroll
    for (unsigned sp = 0; sp < SPIN_CAP; ++sp) {
      __builtin_amdgcn_s_sleep(100);
      CLOBBER();
      const v4u f0 = __builtin_amdgcn_raw_buffer_load_b128(rws, RDY_OFF + lane * 16, 0, AUX_SC1);
      const v4u f1 = __builtin_amdgcn_raw_buffer_load_b128(rws, RDY_OFF + 1024 + lane * 16, 0, AUX_SC1);
      const bool ok = f0.x == rlo && f0.y == rhi && f0.z == rlo && f0.w == rhi && f1.x == rlo && f1.y == rhi && f1.z == rlo && f1.w == rhi;
      if (__all(ok)) break;
    }
    CLOBBER();
    if (lane == 0) L[L_GO] = 1u;
    STAMP(4);
    if (b < NFC) {
      const int fh = b >= 18 ? 1 : 0, fl = b - 18 * fh;
      const int of = FP_OFF + (lane >> 3) * (D * 4) + fl * 128 + (lane & 7) * 16 + fh * 8;
      v2u f[18];
#pragma unroll
      for (int i = 0; i < 18; ++i) f[i] = __builtin_amdgcn_raw_buffer_load_b64(rws, of, i * 8 * D * 4, AUX_SC1);
      const unsigned xv = *(const unsigned*)(a.x + 32 * fl + 4 * (lane & 7) + 2 * fh);
      float y0 = 0.f, y1 = 0.f;
#pragma unroll
      for (int i = 0; i < 18; ++i) { y0 += asf(f[i].x); y1 += asf(f[i].y); }
      y0 += dpp<ROR8>(y0); y1 += dpp<ROR8>(y1);
      y0 = xrow_sum(y0); y1 = xrow_sum(y1);
      if (lane < 8) *(LAS f32x4*)(LF + L_G + 4 * lane) = f32x4{rbf(silu_f(rbf(y0))), rbf(silu_f(rbf(y1))), bflo(xv), bfhi(xv)};
      LGKM0();
      if (lane == 0) L[L_FFR] = 1u;
    }
    STAMP(5);
  }

  int pass = (role == 2 && DRYRUN) ? 0 : 1;
#pragma nounroll
  for (; pass < 2; ++pass) {
    pass = __builtin_amdgcn_readfirstlane(pass); role = __builtin_amdgcn_readfirstlane(role);
    asm volatile("" : "+s"(pass), "+s"(role));
    const bool dry = pass == 0;
    const __amdgpu_buffer_rsrc_t rwt = __builtin_amdgcn_make_buffer_rsrc((void*)a.ws, 0, dry ? 0 : WS_BYTES, RSRC3);
    volatile LAS unsigned* LT = L + (dry ? L_DRY : 0);
    LAS float* LTF = LF + (dry ? L_DRY : 0);
    const unsigned cap = dry ? 1u : SPIN_CAP;
    if (role == 0 || dry) {
      float lw = s.l;
      lw += dpp<ROR4>(lw); lw += dpp<ROR8>(lw); lw = xrow_sum(lw);
      *(LAS f32x4*)(LTF + L_ACC + (sw * 64 + lane) * 4) = s.acc;
      if (lane == 0 || lane == 2) { LTF[L_ML + sw * 4 + lane] = s.m; LTF[L_ML + sw * 4 + lane + 1] = lw; }
      if (lane == 0) LT[L_SDONE] = 1u;
      LGKM0();
      if (lds_inc(LT, L_CNTS, lane) == 3u || dry) {
        f32x4 ml[4], ac[4];
#pragma unroll
        for (int i = 0; i < 4; ++i) { ml[i] = *(LAS f32x4*)(LTF + L_ML + i * 4); ac[i] = *(LAS f32x4*)(LTF + L_ACC + (i * 64 + lane) * 4); }
        const float M0 = fmaxf(fmaxf(ml[0].x, ml[1].x), fmaxf(ml[2].x, ml[3].x)), M1 = fmaxf(fmaxf(ml[0].z, ml[1].z), fmaxf(ml[2].z, ml[3].z));
        float L0 = 0.f, L1 = 0.f;
        f32x4 o = {0.f, 0.f, 0.f, 0.f};
#pragma unroll
        for (int i = 0; i < 4; ++i) {
          const float w0 = __builtin_amdgcn_exp2f(ml[i].x - M0), w1 = __builtin_amdgcn_exp2f(ml[i].z - M1);
          L0 = fmaf(w0, ml[i].y, L0); L1 = fmaf(w1, ml[i].w, L1);
          o.x = fmaf(w0, ac[i].x, o.x); o.y = fmaf(w0, ac[i].y, o.y); o.z = fmaf(w1, ac[i].z, o.z); o.w = fmaf(w1, ac[i].w, o.w);
        }
        __builtin_amdgcn_raw_buffer_store_b128(v4u{asu(o.x), asu(L0), asu(o.y), ~asu(M0)}, rwt, p1_off0, 0, AUX_SC1);
        __builtin_amdgcn_raw_buffer_store_b128(v4u{asu(o.z), asu(L1), asu(o.w), ~asu(M1)}, rwt, p1_off1, 0, AUX_SC1);
        if (!dry) STAMP(4);
      }
    }
    if (role == 2) {
      lds_wait<4>(LT, L_SDONE, dry ? 0u : 1u);
      if (!dry) STAMP(6);
      const int o1 = P1_OFF + (g * 16 + j) * 2048 + lane * 16;
      v4u pa = {0u, 0u, 0u, 0u}, pb = {0u, 0u, 0u, 0u};
#pragma nounroll
      for (unsigned sp = 0; sp < cap; ++sp) {
        CLOBBER();
        const bool oka = pa.y != 0u && pa.w != 0u, okb = pb.y != 0u && pb.w != 0u;
        if (__all(oka && okb)) break;
        const v4u n0 = __builtin_amdgcn_raw_buffer_load_b128(rwt, oka ? 0x7f000000 : o1, 0, AUX_SC1);
        const v4u n1 = __builtin_amdgcn_raw_buffer_load_b128(rwt, okb ? 0x7f000000 : o1 + 1024, 0, AUX_SC1);
        pa = oka ? pa : n0; pb = okb ? pb : n1;
      }
      const float m_a = asf(~pa.w), m_b = asf(~pb.w);
      float M = fmaxf(m_a, m_b);
      M = fmaxf(M, dpp<ROR8>(M)); M = xrow_max(M);
      const float wa_ = __builtin_amdgcn_exp2f(m_a - M), wb_ = __builtin_amdgcn_exp2f(m_b - M);
      float Ls = fmaf(wa_, asf(pa.y), wb_ * asf(pb.y));
      float o0 = fmaf(wa_, asf(pa.x), wb_ * asf(pb.x)), o1v = fmaf(wa_, asf(pa.z), wb_ * asf(pb.z));
      Ls += dpp<ROR8>(Ls); o0 += dpp<ROR8>(o0); o1v += dpp<ROR8>(o1v);
      Ls = xrow_sum(Ls); o0 = xrow_sum(o0); o1v = xrow_sum(o1v);
      const unsigned opk = cvtpk(o0 / Ls, o1v / Ls);
      if (lane < 8) LT[L_O + lane] = opk;
      LGKM0();
      if (lane == 0) LT[L_ORDY] = 1u;
      if (!dry) STAMP(7);
    }
    if (role == 1 || dry) {
      lds_wait<8>(LT, L_SDONE, dry ? 0u : 1u);
      if (!dry) STAMP(3);
      lds_wait<0>(LT, L_ORDY, dry ? 0u : 1u);
      if (!dry) STAMP(4);
      const unsigned opk = LT[L_O + (lane & 7)];
      f32x4 y = {0.f, 0.f, 0.f, 0.f};
#pragma unroll
      for (int k = 0; k < 8; ++k) {
        const unsigned sp = __builtin_amdgcn_readlane(opk, k);
        const float e0 = bflo(sp), e1 = bfhi(sp);
        y.x = fmaf(e0, wf[8 * k], y.x); y.y = fmaf(e0, wf[8 * k + 1], y.y); y.z = fmaf(e0, wf[8 * k + 2], y.z); y.w = fmaf(e0, wf[8 * k + 3], y.w);
        y.x = fmaf(e1, wf[8 * k + 4], y.x); y.y = fmaf(e1, wf[8 * k + 5], y.y); y.z = fmaf(e1, wf[8 * k + 6], y.z); y.w = fmaf(e1, wf[8 * k + 7], y.w);
      }
      const int o2 = (unsigned)tt < (unsigned)NCG ? P2_OFF + ((tt >> 3) * NWG + b) * 128 + (tt & 7) * 16 : 0x7f000000;
      __builtin_amdgcn_raw_buffer_store_b128(v4u{asu(y.x), 1u, asu(y.y), 1u}, rwt, o2, 0, AUX_SC1);
      __builtin_amdgcn_raw_buffer_store_b128(v4u{asu(y.z), 1u, asu(y.w), 1u}, rwt, o2, 18 * NWG * 128, AUX_SC1);
      lds_inc(LT, L_YD, lane);
      if (!dry) STAMP(5);
    }
    if ((role == 0 || dry) && b < NFC) {
      lds_wait<1>(LT, L_YD, dry ? 0u : 3u);
      const int off = P2_OFF + (b * NWG + 64 * sw) * 128 + lane * 16;
      v4u u[8];
#pragma unroll
      for (int i = 0; i < 8; ++i) u[i] = v4u{0u, 0u, 0u, 0u};
#pragma nounroll
      for (unsigned sp = 0; sp < cap; ++sp) {
        CLOBBER();
        bool ok[8], all = true;
#pragma unroll
        for (int i = 0; i < 8; ++i) { ok[i] = (u[i].y & u[i].w) != 0u; all = all && ok[i]; }
        if (__all(all)) break;
        v4u nw[8];
#pragma unroll
        for (int i = 0; i < 8; ++i) nw[i] = __builtin_amdgcn_raw_buffer_load_b128(rwt, ok[i] ? 0x7f000000 : off, i * 1024, AUX_SC1);
#pragma unroll
        for (int i = 0; i < 8; ++i) u[i] = ok[i] ? u[i] : nw[i];
      }
      if (!dry) STAMP(5);
      float y0 = 0.f, y1 = 0.f;
#pragma unroll
      for (int i = 0; i < 8; ++i) { y0 += asf(u[i].x); y1 += asf(u[i].z); }
      y0 += dpp<ROR8>(y0); y1 += dpp<ROR8>(y1);
      y0 = xrow_sum(y0); y1 = xrow_sum(y1);
      if (lane < 8) *(LAS f32x2*)(LTF + L_FIN + sw * 16 + 2 * lane) = f32x2{y0, y1};
      LGKM0();
      if (lds_inc(LT, L_CNTF, lane) == 3u || dry) {
        lds_wait<1>(LT, L_FFR, dry ? 0u : 1u);
        const int c = lane & 7;
        const f32x2 f0 = *(LAS f32x2*)(LTF + L_FIN + 2 * c), f1 = *(LAS f32x2*)(LTF + L_FIN + 16 + 2 * c), f2 = *(LAS f32x2*)(LTF + L_FIN + 32 + 2 * c),
                    f3 = *(LAS f32x2*)(LTF + L_FIN + 48 + 2 * c);
        const f32x4 gx = *(LAS f32x4*)(LTF + L_G + 4 * c);
        const float sv0 = rbf((f0.x + f1.x) + (f2.x + f3.x)), sv1 = rbf((f0.y + f1.y) + (f2.y + f3.y));
        const unsigned pk = cvtpk(gx.z + rbf(sv0 * gx.x), gx.w + rbf(sv1 * gx.y));
        const int fh = b >= 18 ? 1 : 0, fl = b - 18 * fh;
        const __amdgpu_buffer_rsrc_t rout = __builtin_amdgcn_make_buffer_rsrc((void*)a.out, 0, dry ? 0 : D * 2, RSRC3);
        __builtin_amdgcn_raw_buffer_store_b32(pk, rout, lane < 8 ? (32 * fl + 4 * lane + 2 * fh) * 2 : 0x7f000000, 0, 0);
        const u64 e = (((u64)L[L_TAG + 5] << 32) | L[L_TAG + 4]) * 6364136223846793005ull + 1442695040888963407ull;
        __builtin_amdgcn_raw_buffer_store_b64(v2u{(unsigned)e, (unsigned)(e >> 32)}, rwt, (b == 0 && lane == 0) ? E_OFF : 0x7f000000, 0, AUX_SC1);
        if (!dry) STAMP(6);
      }
    }
  }
}

}

extern "C" void kernel_launch(void* const* d_in, const int* in_sizes, int n_in, void* d_out, int out_size, void* d_ws, size_t ws_size,
                              hipStream_t stream) {
  Args a;
  a.x = (const unsigned short*)d_in[0];
  a.k = (const unsigned char*)d_in[1];
  a.v = (const unsigned char*)d_in[2];
  a.wq = (const unsigned char*)d_in[3];
  a.wo = (const unsigned char*)d_in[4];
  a.wg = (const unsigned char*)d_in[6];
  a.wu = (const unsigned char*)d_in[7];
  a.wd = (const unsigned char*)d_in[8];
  a.out = (unsigned short*)d_out;
  a.ws = (unsigned char*)d_ws;
  hipLaunchKernelGGL(fused_decode_kernel, dim3(NWG), dim3(NTHR), 0, stream, a);
}
```

```cpp
#include <hip/hip_runtime.h>
#include <hip/hip_bf16.h>
#include <stdint.h>

namespace {

typedef unsigned v4u __attribute__((ext_vector_type(4)));
typedef unsigned v2u __attribute__((ext_vector_type(2)));
typedef float f32x4 __attribute__((ext_vector_type(4)));
typedef float f32x2 __attribute__((ext_vector_type(2)));
typedef __bf16 bf16x8 __attribute__((ext_vector_type(8)));
typedef __bf16 bf16x2 __attribute__((ext_vector_type(2)));
typedef unsigned long long u64;
#define LAS __attribute__((address_space(3)))

#define NS_TILES 20
#define PACE_W 8
#define HALF_N 64
#define DBG_NOW 0
#define RING2 0
#define ATOM_BRANCH 0
#define AUXQ 0
#define AUXG 0
#define AUXD 0

constexpr int D = 576, SK = 65536, DH = 128, FF = 4608, QN = 4096;
constexpr int NWG = 256, NTHR = 512;
constexpr int NT = SK / 32;
constexpr int NS = NS_TILES;
constexpr int DYN0 = NS * 64;
constexpr unsigned TK_FULL = NT - DYN0 - HALF_N, TK_END = TK_FULL + 2 * HALF_N;
constexpr unsigned HEAD_BYTES = (unsigned)SK * DH * 2;
constexpr int NCG = D / 4;
constexpr int NFC = 36;
constexpr int NFT = FF / 32;
constexpr int OOB = 0x7f000000;

constexpr int E_OFF = 0;
constexpr int C_OFF = 256;
constexpr int RDY_OFF = 8192;
constexpr int QG_OFF = 16384;
constexpr int P1_OFF = 65536;
constexpr int P2_OFF = P1_OFF + 16 * 16 * 16 * 128;
constexpr int FP_OFF = P2_OFF + NFC * NWG * 128;
constexpr int WS_BYTES = FP_OFF + NFT * D * 4;
constexpr int ST_OFF = 4 << 20;
static_assert(WS_BYTES <= ST_OFF, "");

constexpr int AUX_NT = 2, AUX_SC1 = 16;
constexpr unsigned RSRC3 = 0x00020000u;
constexpr unsigned SPIN_CAP = 1u << 17;

constexpr int L_QRDY = 0, L_TRDY = 1, L_CNTA = 2, L_CNTQ = 3, L_CNTS = 4, L_CNTF = 5, L_GO = 6, L_FFR = 7, L_SDONE = 8, L_YD = 9, L_QPUB = 10, L_ORDY = 11, L_CNTD = 12;
constexpr int L_NCTL = 16;
constexpr int L_TAG = 16;
constexpr int L_Q = 32;
constexpr int L_RED = 192;
constexpr int L_O = 384;
constexpr int L_REDQ = 400;
constexpr int L_G = 448;
constexpr int L_FIN = 512;
constexpr int L_XN = 576;
constexpr int L_ML = 1152;
constexpr int L_ACC = 1280;
constexpr int L_TOTAL = 24 * 1024;

constexpr float SCALE = 0.08838834764831845f;
constexpr float LOG2E = 1.4426950408889634f;

__device__ __forceinline__ unsigned cvtpk(float lo, float hi) {
  f32x2 v = {lo, hi};
  return __builtin_bit_cast(unsigned, __builtin_convertvector(v, bf16x2));
}
__device__ __forceinline__ float bflo(unsigned u) { return __builtin_bit_cast(float, u << 16); }
__device__ __forceinline__ float bfhi(unsigned u) { return __builtin_bit_cast(float, u & 0xffff0000u); }
__device__ __forceinline__ float rbf(float x) { return bflo(cvtpk(x, x)); }
__device__ __forceinline__ float asf(unsigned u) { return __builtin_bit_cast(float, u); }
__device__ __forceinline__ unsigned asu(float f) { return __builtin_bit_cast(unsigned, f); }

template <int CTRL> __device__ __forceinline__ float dpp(float x) {
  return __builtin_bit_cast(float, __builtin_amdgcn_mov_dpp(__builtin_bit_cast(int, x), CTRL, 0xf, 0xf, true));
}
constexpr int XOR1 = 0xB1, XOR2 = 0x4E, HMIR = 0x141, ROR8 = 0x128, ROR4 = 0x124, SHL4 = 0x104, SHR4 = 0x114, QB0 = 0x00, QB2 = 0xAA;

__device__ __forceinline__ float xrow_max(float x) {
  auto s = __builtin_amdgcn_permlane16_swap(asu(x), asu(x), false, false);
  x = fmaxf(asf(s[0]), asf(s[1]));
  auto t = __builtin_amdgcn_permlane32_swap(asu(x), asu(x), false, false);
  return fmaxf(asf(t[0]), asf(t[1]));
}
__device__ __forceinline__ float xrow_sum(float x) {
  auto s = __builtin_amdgcn_permlane16_swap(asu(x), asu(x), false, false);
  x = asf(s[0]) + asf(s[1]);
  auto t = __builtin_amdgcn_permlane32_swap(asu(x), asu(x), false, false);
  return asf(t[0]) + asf(t[1]);
}
__device__ __forceinline__ float wave_sum(float x) {
  x += dpp<XOR1>(x); x += dpp<XOR2>(x); x += dpp<HMIR>(x); x += dpp<ROR8>(x);
  return xrow_sum(x);
}
__device__ __forceinline__ float swap32_add(float a, float b) {
  auto s = __builtin_amdgcn_permlane32_swap(asu(a), asu(b), false, false);
  return asf(s[0]) + asf(s[1]);
}
__device__ __forceinline__ float swap16_add(float a, float b) {
  auto s = __builtin_amdgcn_permlane16_swap(asu(a), asu(b), false, false);
  return asf(s[0]) + asf(s[1]);
}
__device__ __forceinline__ f32x4 mfma(v4u a, v4u b, f32x4 c) {
  return __builtin_amdgcn_mfma_f32_16x16x32_bf16(__builtin_bit_cast(bf16x8, a), __builtin_bit_cast(bf16x8, b), c, 0, 0, 0);
}
__device__ __forceinline__ float dot2(unsigned a, unsigned b, float c) {
  return __builtin_amdgcn_fdot2_f32_bf16(__builtin_bit_cast(bf16x2, a), __builtin_bit_cast(bf16x2, b), c, false);
}
__device__ __forceinline__ float silu_f(float x) { return x / (1.0f + __expf(-x)); }

#define WG_BARRIER() do { __builtin_amdgcn_sched_barrier(0); __builtin_amdgcn_s_barrier(); __builtin_amdgcn_sched_barrier(0); } while (0)
#define CLOBBER() asm volatile("" ::: "memory")
#define LGKM0() asm volatile("s_waitcnt lgkmcnt(0)" ::: "memory")
#define DRAIN() asm volatile("s_waitcnt vmcnt(0)" ::: "memory")

__device__ __forceinline__ unsigned lds_ld(volatile LAS unsigned* L, int i) { return __builtin_amdgcn_readfirstlane(L[i]); }
template <int SLEEP> __device__ __forceinline__ void lds_wait(volatile LAS unsigned* L, int i, unsigned want) {
#pragma nounroll
  for (unsigned s = 0; s < (SPIN_CAP << 3); ++s) {
    if (lds_ld(L, i) >= want) break;
    __builtin_amdgcn_s_sleep(SLEEP);
  }
  CLOBBER();
}
__device__ __forceinline__ unsigned lds_inc(volatile LAS unsigned* L, int i, int lane) {
  unsigned r = 0;
  if (lane == 0) r = __hip_atomic_fetch_add((LAS unsigned*)(L + i), 1u, __ATOMIC_RELAXED, __HIP_MEMORY_SCOPE_WORKGROUP);
  r = __builtin_amdgcn_readfirstlane(r);
  CLOBBER();
  return r;
}

struct Args {
  const unsigned short* x;
  const unsigned char* k;
  const unsigned char* v;
  const unsigned char* wq;
  const unsigned char* wo;
  const unsigned char* wg;
  const unsigned char* wu;
  const unsigned char* wd;
  unsigned short* out;
  unsigned char* ws;
};

#define STAMP(i) do { } while (0)

#define KOFF(i) (((i) >> 2) * 4096 + (((i) >> 1) & 1) * 1024 + ((i) & 1) * 128)
struct Tile { v4u k[8]; v4u v[8]; };

struct SState {
  f32x4 acc;
  float m, l;
  v4u fa, fb;
};

template <int QD> __device__ __forceinline__ void build_frag(v4u& f, unsigned pb) {
  constexpr int r0 = (0 - QD) & 3, r1 = (1 - QD) & 3, r2 = (2 - QD) & 3, r3 = (3 - QD) & 3;
  f.x = __builtin_amdgcn_update_dpp(f.x, pb, r0 ? 0x120 + 4 * r0 : 0xE4, 0xf, 0x1, false);
  f.y = __builtin_amdgcn_update_dpp(f.y, pb, r1 ? 0x120 + 4 * r1 : 0xE4, 0xf, 0x2, false);
  f.z = __builtin_amdgcn_update_dpp(f.z, pb, r2 ? 0x120 + 4 * r2 : 0xE4, 0xf, 0x4, false);
  f.w = __builtin_amdgcn_update_dpp(f.w, pb, r3 ? 0x120 + 4 * r3 : 0xE4, 0xf, 0x8, false);
}

__device__ __forceinline__ v4u ldkv(__amdgpu_buffer_rsrc_t r, int voff, unsigned soff) {
  return __builtin_amdgcn_raw_buffer_load_b128(r, voff, soff, AUX_NT);
}

__device__ __forceinline__ void process_tile(Tile& t, SState& s, const v4u (&qf)[4], __amdgpu_buffer_rsrc_t rk, __amdgpu_buffer_rsrc_t rv,
                                             int voffk, int voffv, unsigned off0, unsigned off1, float cap1, unsigned halfmask, bool hsel) {
  float x[4][2];
#pragma unroll
  for (int u = 0; u < 4; ++u) {
    float a0 = 0.f, a1 = 0.f;
#pragma unroll
    for (int hf = 0; hf < 2; ++hf) {
#pragma unroll
      for (int e = 0; e < 4; ++e) { a0 = dot2(t.k[2 * u + hf][e], qf[hf][e], a0); a1 = dot2(t.k[2 * u + hf][e], qf[2 + hf][e], a1); }
      asm volatile("" : "+v"(a0), "+v"(a1));
      t.k[2 * u + hf] = ldkv(rk, voffk + KOFF(2 * u + hf), u < 2 ? off0 : off1);
      __builtin_amdgcn_sched_barrier(0);
    }
    x[u][0] = a0; x[u][1] = a1;
  }
  float pp[2];
#pragma unroll
  for (int v = 0; v < 2; ++v) {
    float y[2];
#pragma unroll
    for (int h = 0; h < 2; ++h) {
      const float t1 = x[2 * v][h] + dpp<HMIR>(x[2 * v][h]), t2 = x[2 * v + 1][h] + dpp<HMIR>(x[2 * v + 1][h]);
      y[h] = asf(__builtin_amdgcn_update_dpp(asu(t1), asu(t2), 0xE4, 0xf, 0xA, false));
    }
    const float t1 = y[0] + dpp<XOR2>(y[0]), t2 = y[1] + dpp<XOR2>(y[1]);
    const float z = hsel ? t2 : t1;
    pp[v] = z + dpp<XOR1>(z);
  }
  unsigned pk = cvtpk(pp[0], pp[1]);
  pk = cvtpk(bflo(pk) * SCALE, bfhi(pk) * SCALE);
  const float s0 = bflo(pk) * LOG2E, s1 = fminf(bfhi(pk) * LOG2E, cap1);
  float tm = fmaxf(s0, s1);
  tm = fmaxf(tm, dpp<ROR4>(tm));
  tm = fmaxf(tm, dpp<ROR8>(tm));
  tm = xrow_max(tm);
  const float mn = fmaxf(s.m, ceilf(tm));
  const float alpha = __builtin_amdgcn_exp2f(s.m - mn);
  const float e0 = __builtin_amdgcn_exp2f(s0 - mn);
  const float e1 = __builtin_amdgcn_exp2f(s1 - mn);
  s.m = mn;
  s.l = fmaf(s.l, alpha, e0 + e1);
  const float a0 = dpp<QB0>(alpha), a1 = dpp<QB2>(alpha);
  s.acc.x *= a0; s.acc.y *= a0; s.acc.z *= a1; s.acc.w *= a1;
  const unsigned pb0 = cvtpk(e0, e0) & halfmask, pb1 = cvtpk(e1, e1) & halfmask;
#define PV_STEP(IDX, F, PB, QD) \
  build_frag<QD>(F, PB); \
  s.acc = mfma(F, t.v[IDX], s.acc); \
  t.v[IDX] = ldkv(rv, voffv + (IDX) * 1024, (IDX) < 4 ? off0 : off1); \
  __builtin_amdgcn_sched_barrier(0);
  PV_STEP(0, s.fa, pb0, 0) PV_STEP(1, s.fb, pb0, 1) PV_STEP(2, s.fa, pb0, 2) PV_STEP(3, s.fb, pb0, 3)
  PV_STEP(4, s.fa, pb1, 0) PV_STEP(5, s.fb, pb1, 1) PV_STEP(6, s.fa, pb1, 2) PV_STEP(7, s.fb, pb1, 3)
#undef PV_STEP
}

template <int N> __device__ __forceinline__ void poll_lines(v4u (&u)[N], __amdgpu_buffer_rsrc_t rws, int off) {
#pragma unroll
  for (int i = 0; i < N; ++i) u[i] = v4u{0u, 0u, 0u, 0u};
#pragma nounroll
  for (unsigned sp = 0; sp < SPIN_CAP; ++sp) {
    CLOBBER();
    bool ok[N], all = true;
#pragma unroll
    for (int i = 0; i < N; ++i) { ok[i] = u[i].y != 0u && u[i].w != 0u; all = all && ok[i]; }
    if (__all(all)) break;
    v4u nw[N];
#pragma unroll
    for (int i = 0; i < N; ++i) nw[i] = __builtin_amdgcn_raw_buffer_load_b128(rws, ok[i] ? OOB : off, i * 1024, AUX_SC1);
#pragma unroll
    for (int i = 0; i < N; ++i) u[i] = ok[i] ? u[i] : nw[i];
  }
}

__global__ void __launch_bounds__(NTHR, 2) fused_decode_kernel(Args a) {
  __shared__ __attribute__((aligned(16))) unsigned lds_raw[L_TOTAL];
  volatile LAS unsigned* L = (volatile LAS unsigned*)lds_raw;
  LAS float* LF = (LAS float*)lds_raw;
  const int tid = threadIdx.x, lane = tid & 63, w = __builtin_amdgcn_readfirstlane(tid >> 6);
  const int b = blockIdx.x, g = b >> 4, j = b & 15;
  const __amdgpu_buffer_rsrc_t rws = __builtin_amdgcn_make_buffer_rsrc((void*)a.ws, 0, WS_BYTES, RSRC3);
  const int r4 = lane >> 4, c16 = lane & 15;
  const int p1_off = P1_OFF + ((g * 16 + (c16 >> 1)) * 16 + j) * 128 + (8 * (c16 & 1) + 2 * r4) * 8;
  constexpr int P1_H1 = 8 * 16 * 128;
  const int slot = ((j & 7) - (g >> 1)) & 7, hi = j >> 3;
  const bool is_ffn = slot < 4 || (slot == 4 && hi == 0);
  const int ft = 18 * (g >> 1) + (slot < 4 ? (g & 1) * 8 + 2 * slot + hi : 16 + (g & 1));
  const int fh = b >= 18 ? 1 : 0, fl = b - 18 * fh;
  STAMP(0);

  if (w >= 4) {
    const int sw = w - 4;
    const __amdgpu_buffer_rsrc_t rk = __builtin_amdgcn_make_buffer_rsrc((void*)(a.k + (size_t)g * HEAD_BYTES), 0, HEAD_BYTES, RSRC3);
    const __amdgpu_buffer_rsrc_t rv = __builtin_amdgcn_make_buffer_rsrc((void*)(a.v + (size_t)g * HEAD_BYTES), 0, HEAD_BYTES, RSRC3);
    const __amdgpu_buffer_rsrc_t rc = __builtin_amdgcn_make_buffer_rsrc((void*)(a.ws + C_OFF + g * 256), 0, 4, RSRC3);
    const int voffk = (8 * (c16 >> 3) + r4) * 256 + (lane & 7) * 16, voffv = lane * 16;
    const unsigned sb = (unsigned)(j * NS * 4 + sw) * 8192u;
    Tile tb;
    WG_BARRIER();
    {
      unsigned qr = 0;
#pragma unroll
      for (int i = 0; i < 16 * (1 + RING2); ++i) {
        if (i >= PACE_W && !qr) { asm volatile("s_waitcnt vmcnt(%0)" ::"n"(PACE_W - 1) : "memory"); qr = lds_ld(L, L_QRDY); }
        Tile& t = tb;
        if ((i & 15) < 8) t.k[i & 7] = ldkv(rk, voffk + KOFF(i & 7), sb + (i >> 4) * 32768u);
        else t.v[i & 7] = ldkv(rv, voffv + (i & 7) * 1024, sb + (i >> 4) * 32768u);
        __builtin_amdgcn_sched_barrier(0);
      }
    }
    lds_wait<1>(L, L_QRDY, 1u);
    STAMP(1);
    v4u qf[4];
#pragma unroll
    for (int i = 0; i < 4; ++i) qf[i] = *(LAS v4u*)(lds_raw + L_Q + 32 * i + 4 * (lane & 7));
    const unsigned c0 = lds_ld(L, L_TAG + 1);
    const unsigned halfmask = (lane & 1) ? 0xffff0000u : 0x0000ffffu;
    SState s;
    s.acc = f32x4{0.f, 0.f, 0.f, 0.f};
    s.m = -INFINITY; s.l = 0.f;
    s.fa = v4u{0u, 0u, 0u, 0u}; s.fb = v4u{0u, 0u, 0u, 0u};
    unsigned off0 = sb + (1 + RING2) * 32768u, off1 = off0;
#define STEP(T, CAP) \
      if (n == NS) { STAMP(2); lds_wait<1>(L, L_GO, 1u); }      \
      unsigned tk = 0; \
      if (!ATOM_BRANCH || n >= NS) tk = __builtin_amdgcn_raw_ptr_buffer_atomic_add_i32(1, rc, lane * 4, n >= NS ? 0 : 4, 0); \
      process_tile(T, s, qf, rk, rv, voffk, voffv, off0, off1, CAP, halfmask, (lane & 2) != 0); \
      const bool more = off0 < HEAD_BYTES; \
      CAP = off1 >= HEAD_BYTES ? -INFINITY : INFINITY; \
      if (n >= NS) { \
        const unsigned idx = __builtin_amdgcn_readfirstlane(tk) - c0; \
        off0 = idx < TK_FULL ? (DYN0 + idx) * 8192u : idx < TK_END ? (DYN0 + TK_FULL) * 8192u + (idx - TK_FULL) * 4096u : (unsigned)OOB; \
        off1 = idx < TK_FULL ? off0 : (unsigned)OOB;         \
      } else { \
        off0 = off1 = sb + (unsigned)n * 32768u; \
      } \
      ++n;
    {
      float cap1 = INFINITY;
      int n = 2;
#pragma nounroll
      for (;;) {
        STEP(tb, cap1)
        if (!more) break;
      }
    }
    STAMP(3);
    {
      float lw = s.l;
      lw += dpp<ROR4>(lw); lw += dpp<ROR8>(lw); lw = xrow_sum(lw);
      *(LAS f32x4*)(LF + L_ACC + (sw * 64 + lane) * 4) = s.acc;
      if (lane == 0 || lane == 2) { LF[L_ML + sw * 4 + lane] = s.m; LF[L_ML + sw * 4 + lane + 1] = lw; }
      if (lane == 0) L[L_SDONE] = 1u;
      LGKM0();
      if (lds_inc(L, L_CNTS, lane) == 3u) {
        f32x4 ml[4], ac[4];
#pragma unroll
        for (int i = 0; i < 4; ++i) { ml[i] = *(LAS f32x4*)(LF + L_ML + i * 4); ac[i] = *(LAS f32x4*)(LF + L_ACC + (i * 64 + lane) * 4); }
        const float M0 = fmaxf(fmaxf(ml[0].x, ml[1].x), fmaxf(ml[2].x, ml[3].x)), M1 = fmaxf(fmaxf(ml[0].z, ml[1].z), fmaxf(ml[2].z, ml[3].z));
        float L0 = 0.f, L1 = 0.f;
        f32x4 o = {0.f, 0.f, 0.f, 0.f};
#pragma unroll
        for (int i = 0; i < 4; ++i) {
          const float w0 = __builtin_amdgcn_exp2f(ml[i].x - M0), w1 = __builtin_amdgcn_exp2f(ml[i].z - M1);
          L0 = fmaf(w0, ml[i].y, L0); L1 = fmaf(w1, ml[i].w, L1);
          o.x = fmaf(w0, ac[i].x, o.x); o.y = fmaf(w0, ac[i].y, o.y); o.z = fmaf(w1, ac[i].z, o.z); o.w = fmaf(w1, ac[i].w, o.w);
        }
        __builtin_amdgcn_raw_buffer_store_b128(v4u{asu(o.x), asu(L0), asu(o.y), ~asu(M0)}, rws, p1_off, 0, AUX_SC1);
        __builtin_amdgcn_raw_buffer_store_b128(v4u{asu(o.z), asu(L1), asu(o.w), ~asu(M1)}, rws, p1_off, P1_H1, AUX_SC1);
      }
    }
    STAMP(4);
    if (b < NFC) {
      lds_wait<1>(L, L_YD, 3u);
      v4u u[8];
      poll_lines<8>(u, rws, P2_OFF + (b * NWG + 64 * sw) * 128 + lane * 16);
      STAMP(5);
      float y0 = 0.f, y1 = 0.f;
#pragma unroll
      for (int i = 0; i < 8; ++i) { y0 += asf(u[i].x); y1 += asf(u[i].z); }
      y0 += dpp<ROR8>(y0); y1 += dpp<ROR8>(y1);
      y0 = xrow_sum(y0); y1 = xrow_sum(y1);
      if (lane < 8) *(LAS f32x2*)(LF + L_FIN + sw * 16 + 2 * lane) = f32x2{y0, y1};
      LGKM0();
      if (lds_inc(L, L_CNTF, lane) == 3u) {
        lds_wait<0>(L, L_FFR, 1u);
        const int c = lane & 7;
        const f32x2 f0 = *(LAS f32x2*)(LF + L_FIN + 2 * c), f1 = *(LAS f32x2*)(LF + L_FIN + 16 + 2 * c), f2 = *(LAS f32x2*)(LF + L_FIN + 32 + 2 * c),
                    f3 = *(LAS f32x2*)(LF + L_FIN + 48 + 2 * c);
        const f32x4 gx = *(LAS f32x4*)(LF + L_G + 4 * c);
        const float sv0 = rbf((f0.x + f1.x) + (f2.x + f3.x)), sv1 = rbf((f0.y + f1.y) + (f2.y + f3.y));
        const unsigned pk = cvtpk(gx.z + rbf(sv0 * gx.x), gx.w + rbf(sv1 * gx.y));
        if (lane < 8) *(unsigned*)(a.out + 32 * fl + 4 * lane + 2 * fh) = pk;
        if (b == 0 && lane == 0) {
          const u64 e = (((u64)L[L_TAG + 5] << 32) | L[L_TAG + 4]) * 6364136223846793005ull + 1442695040888963407ull;
          __builtin_amdgcn_raw_buffer_store_b64(v2u{(unsigned)e, (unsigned)(e >> 32)}, rws, E_OFF, 0, AUX_SC1);
        }
        STAMP(6);
      }
    }
  } else if (w >= 1) {
    const int ta = w - 1, tt = tid - 64;
    __builtin_amdgcn_s_setprio(3);
    const int qs = ((b & 7) * 8 + (b >> 5)) * 4 + ((b >> 3) & 3);
    const __amdgpu_buffer_rsrc_t rx = __builtin_amdgcn_make_buffer_rsrc((void*)a.x, 0, D * 2, RSRC3);
    const __amdgpu_buffer_rsrc_t rq = __builtin_amdgcn_make_buffer_rsrc((void*)(a.wq + qs * 32), 0, DBG_NOW ? 0 : (D - 1) * QN * 2 + 32, RSRC3);
    const int rr = lane >> 2, cc = lane & 3;
    v4u wqr[6];
    const v4u xc0 = __builtin_amdgcn_raw_buffer_load_b128(rx, lane * 16, 0, 0);
    const v4u xc1 = __builtin_amdgcn_raw_buffer_load_b128(rx, 1024 + lane * 16, 0, 0);
    unsigned xo[3];
#pragma unroll
    for (int i = 0; i < 3; ++i) xo[i] = __builtin_amdgcn_raw_buffer_load_b16(rx, (192 * ta + 64 * i + lane) * 2, 0, 0);
    {
      const int vo = (192 * ta + (lane >> 1)) * (QN * 2) + (lane & 1) * 16;
#pragma unroll
      for (int i = 0; i < 6; ++i) wqr[i] = __builtin_amdgcn_raw_buffer_load_b128(rq, vo, i * 32 * QN * 2, AUXQ);
    }
    WG_BARRIER();
    {
      const float x0 = bflo(__builtin_amdgcn_readfirstlane(xc0.x));
      float s0 = 0.f, s1 = 0.f, q0 = 0.f, q1 = 0.f;
#pragma unroll
      for (int i = 0; i < 4; ++i) {
        const float d0 = bflo(xc0[i]) - x0, d1 = bfhi(xc0[i]) - x0, d2 = bflo(xc1[i]) - x0, d3 = bfhi(xc1[i]) - x0;
        s0 += d0 + d1; s1 += d2 + d3;
        q0 = fmaf(d0, d0, q0); q0 = fmaf(d1, d1, q0); q1 = fmaf(d2, d2, q1); q1 = fmaf(d3, d3, q1);
      }
      const float dm = wave_sum(s0 + (lane < 8 ? s1 : 0.f)) * (1.0f / D);
      const float var = fmaxf(wave_sum(q0 + (lane < 8 ? q1 : 0.f)) * (1.0f / D) - dm * dm, 0.f);
      const float mb = rbf(x0 + dm), den = rbf(sqrtf(rbf(rbf(var) + 1e-5f)));
#pragma unroll
      for (int i = 0; i < 3; ++i) LF[L_XN + 192 * ta + 64 * i + lane] = rbf(rbf(bflo(xo[i]) - mb) / den);
      LGKM0();
    }
    {
      float acc[8];
#pragma unroll
      for (int e = 0; e < 8; ++e) acc[e] = 0.f;
#pragma unroll
      for (int i = 0; i < 6; ++i) {
        const float xn = LF[L_XN + 192 * ta + 32 * i + (lane >> 1)];
#pragma unroll
        for (int e = 0; e < 4; ++e) { acc[2 * e] = fmaf(xn, bflo(wqr[i][e]), acc[2 * e]); acc[2 * e + 1] = fmaf(xn, bfhi(wqr[i][e]), acc[2 * e + 1]); }
      }
      float u[4], v2[2];
#pragma unroll
      for (int e = 0; e < 4; ++e) u[e] = swap32_add(acc[e], acc[4 + e]);
#pragma unroll
      for (int e = 0; e < 2; ++e) v2[e] = swap16_add(u[e], u[2 + e]);
      const float t1 = v2[0] + dpp<ROR8>(v2[0]), t2 = v2[1] + dpp<ROR8>(v2[1]);
      float z = (lane & 8) ? t2 : t1;
      z += dpp<XOR2>(z);
      const float z1 = dpp<SHL4>(z), z2 = dpp<SHR4>(z);
      z += (lane & 4) ? z2 : z1;
      if ((lane & 6) == 0) LF[L_REDQ + ta * 16 + 8 * (lane & 1) + ((lane >> 3) & 7)] = z;
      LGKM0();
      if (lds_inc(L, L_CNTQ, lane) == 2u) {
        const float qv = (LF[L_REDQ + c16] + LF[L_REDQ + 16 + c16]) + LF[L_REDQ + 32 + c16];
        lds_wait<0>(L, L_TRDY, 1u);
        const unsigned tlo = lds_ld(L, L_TAG + 2), thi = lds_ld(L, L_TAG + 3);
        if (lane < 16) __builtin_amdgcn_raw_buffer_store_b64(v2u{(cvtpk(qv, qv) & 0xffffu) | (tlo << 16), thi}, rws, QG_OFF + (qs * 16 + lane) * 8, 0, AUX_SC1);
        CLOBBER();
        if (lane == 0) L[L_QPUB] = 1u;
        STAMP(1);
      }
    }
    lds_wait<1>(L, L_QPUB, 1u);
    const int vo2 = tt < NCG ? tt * 8 : OOB;
    const int o2 = tt < NCG ? (tt & 7) * 16 : OOB;
    v2u wr[16];
    {
      const __amdgpu_buffer_rsrc_t ro = __builtin_amdgcn_make_buffer_rsrc((void*)(a.wo + (size_t)b * 16 * D * 2), 0, DBG_NOW ? 0 : 16 * D * 2, RSRC3);
#pragma unroll
      for (int k = 0; k < 16; ++k) wr[k] = __builtin_amdgcn_raw_buffer_load_b64(ro, vo2, k * D * 2, AUXD);
      __builtin_amdgcn_sched_barrier(0);
    }
    STAMP(2);
    if (is_ffn) {
      STAMP(7);
      const __amdgpu_buffer_rsrc_t ra = __builtin_amdgcn_make_buffer_rsrc((void*)(a.wg + ft * 64), 0, D * FF * 2 - ft * 64, RSRC3);
      const __amdgpu_buffer_rsrc_t rb = __builtin_amdgcn_make_buffer_rsrc((void*)(a.wu + ft * 64), 0, D * FF * 2 - ft * 64, RSRC3);
      const __amdgpu_buffer_rsrc_t rd = __builtin_amdgcn_make_buffer_rsrc((void*)(a.wd + (size_t)ft * 32 * D * 2), 0, 32 * D * 2, RSRC3);
      const int vof = (192 * ta + rr) * (FF * 2) + cc * 16;
      constexpr int fstep = 16 * FF * 2;
      v4u wa[12], wb[12];
      v2u dn[32];
#pragma unroll
      for (int i = 0; i < 12; ++i) {
        wa[i] = __builtin_amdgcn_raw_buffer_load_b128(ra, vof, i * fstep, AUXG);
        wb[i] = __builtin_amdgcn_raw_buffer_load_b128(rb, vof, i * fstep, AUXG);
      }
#pragma unroll
      for (int k = 0; k < 32; ++k) dn[k] = __builtin_amdgcn_raw_buffer_load_b64(rd, vo2, k * D * 2, AUXD);
      __builtin_amdgcn_sched_barrier(0);
      float acc[16];
#pragma unroll
      for (int e = 0; e < 16; ++e) acc[e] = 0.f;
#pragma unroll
      for (int i = 0; i < 12; ++i) {
        const float xn = LF[L_XN + 192 * ta + 16 * i + rr];
#pragma unroll
        for (int e = 0; e < 4; ++e) {
          acc[2 * e] = fmaf(xn, bflo(wa[i][e]), acc[2 * e]); acc[2 * e + 1] = fmaf(xn, bfhi(wa[i][e]), acc[2 * e + 1]);
          acc[8 + 2 * e] = fmaf(xn, bflo(wb[i][e]), acc[8 + 2 * e]); acc[9 + 2 * e] = fmaf(xn, bfhi(wb[i][e]), acc[9 + 2 * e]);
        }
        asm volatile("" : "+v"(acc[0]), "+v"(acc[1]), "+v"(acc[2]), "+v"(acc[3]), "+v"(acc[4]), "+v"(acc[5]), "+v"(acc[6]), "+v"(acc[7]),
                          "+v"(acc[8]), "+v"(acc[9]), "+v"(acc[10]), "+v"(acc[11]), "+v"(acc[12]), "+v"(acc[13]), "+v"(acc[14]), "+v"(acc[15]));
      }
      float u[8], v4[4], z[2];
#pragma unroll
      for (int e = 0; e < 8; ++e) u[e] = swap32_add(acc[e], acc[8 + e]);
#pragma unroll
      for (int e = 0; e < 4; ++e) v4[e] = swap16_add(u[e], u[4 + e]);
#pragma unroll
      for (int e = 0; e < 2; ++e) { const float t1 = v4[e] + dpp<ROR8>(v4[e]), t2 = v4[2 + e] + dpp<ROR8>(v4[2 + e]); z[e] = (lane & 8) ? t2 : t1; }
      const float t1 = z[0] + dpp<SHL4>(z[0]), t2 = z[1] + dpp<SHR4>(z[1]);
      const float zz = (lane & 4) ? t2 : t1;
      LF[L_RED + ta * 64 + (lane & 32) + 8 * cc + ((lane >> 2) & 7)] = zz;
      LGKM0();
      lds_inc(L, L_CNTA, lane);
      lds_wait<0>(L, L_CNTA, 3u);
      const int u5 = lane & 31;
      const float gp = rbf((LF[L_RED + u5] + LF[L_RED + 64 + u5]) + LF[L_RED + 128 + u5]);
      const float up = rbf((LF[L_RED + 32 + u5] + LF[L_RED + 96 + u5]) + LF[L_RED + 160 + u5]);
      const float h = rbf(rbf(silu_f(gp)) * up);
      f32x4 y = {0.f, 0.f, 0.f, 0.f};
#pragma unroll
      for (int k = 0; k < 32; ++k) {
        const float hk = asf(__builtin_amdgcn_readlane(asu(h), k));
        y.x = fmaf(hk, bflo(dn[k].x), y.x); y.y = fmaf(hk, bfhi(dn[k].x), y.y); y.z = fmaf(hk, bflo(dn[k].y), y.z); y.w = fmaf(hk, bfhi(dn[k].y), y.w);
      }
      __builtin_amdgcn_raw_buffer_store_b128(__builtin_bit_cast(v4u, y), rws, tt < NCG ? FP_OFF + ft * (D * 4) + tt * 16 : OOB, 0, AUX_SC1);
      STAMP(6);
    }
    DRAIN();
    lds_inc(L, L_CNTD, lane);
    float wf[64];
#pragma unroll
    for (int k = 0; k < 16; ++k) { wf[4 * k] = bflo(wr[k].x); wf[4 * k + 1] = bfhi(wr[k].x); wf[4 * k + 2] = bflo(wr[k].y); wf[4 * k + 3] = bfhi(wr[k].y); }
#pragma unroll
    for (int k = 0; k < 64; ++k) asm volatile("" : "+v"(wf[k]));
    lds_wait<8>(L, L_SDONE, 1u);
    STAMP(3);
    lds_wait<0>(L, L_ORDY, 1u);
    STAMP(4);
    const unsigned opk = L[L_O + (lane & 7)];
    f32x4 y = {0.f, 0.f, 0.f, 0.f};
#pragma unroll
    for (int k = 0; k < 8; ++k) {
      const unsigned sp = __builtin_amdgcn_readlane(opk, k);
      const float e0 = bflo(sp), e1 = bfhi(sp);
      y.x = fmaf(e0, wf[8 * k], y.x); y.y = fmaf(e0, wf[8 * k + 1], y.y); y.z = fmaf(e0, wf[8 * k + 2], y.z); y.w = fmaf(e0, wf[8 * k + 3], y.w);
      y.x = fmaf(e1, wf[8 * k + 4], y.x); y.y = fmaf(e1, wf[8 * k + 5], y.y); y.z = fmaf(e1, wf[8 * k + 6], y.z); y.w = fmaf(e1, wf[8 * k + 7], y.w);
    }
    const int op = P2_OFF + ((tt >> 3) * NWG + b) * 128;
    __builtin_amdgcn_raw_buffer_store_b128(v4u{asu(y.x), 1u, asu(y.y), 1u}, rws, o2 + op, 0, AUX_SC1);
    __builtin_amdgcn_raw_buffer_store_b128(v4u{asu(y.z), 1u, asu(y.w), 1u}, rws, o2 + op, 18 * NWG * 128, AUX_SC1);
    lds_inc(L, L_YD, lane);
    STAMP(5);
  } else {
    __builtin_amdgcn_s_setprio(3);
    const v2u ev = __builtin_amdgcn_raw_buffer_load_b64(rws, E_OFF, 0, AUX_SC1);
    const unsigned cv = __builtin_amdgcn_raw_buffer_load_b32(rws, C_OFF + g * 256, 0, AUX_SC1);
    if (lane < L_NCTL) L[lane] = 0u;
    LGKM0();
    WG_BARRIER();
    const unsigned elo = __builtin_amdgcn_readfirstlane(ev.x), ehi = __builtin_amdgcn_readfirstlane(ev.y);
    const unsigned qlo = (elo ^ 0x7F4A7C15u) & 0xffffu, qhi = ((elo ^ 0x7F4A7C15u) >> 16) | ((ehi ^ 0x9E3779B9u) << 16);
    const unsigned rlo = elo ^ 0xC2B2AE35u, rhi = ehi ^ 0x27D4EB2Fu;
    if (lane == 0) { L[L_TAG + 1] = cv; L[L_TAG + 2] = qlo; L[L_TAG + 3] = qhi; L[L_TAG + 4] = elo; L[L_TAG + 5] = ehi; }
    LGKM0();
    if (lane == 0) L[L_TRDY] = 1u;
    STAMP(1);
    {
      const int oq = QG_OFF + g * 2048 + lane * 16;
      v4u q0 = {0u, ~qhi, 0u, 0u}, q1 = {0u, ~qhi, 0u, 0u};
#pragma nounroll
      for (unsigned sp = 0; sp < SPIN_CAP; ++sp) {
        CLOBBER();
        const bool ok0 = q0.y == qhi && q0.w == qhi && (q0.x >> 16) == qlo && (q0.z >> 16) == qlo;
        const bool ok1 = q1.y == qhi && q1.w == qhi && (q1.x >> 16) == qlo && (q1.z >> 16) == qlo;
        if (__all(ok0 && ok1)) break;
        const v4u n0 = __builtin_amdgcn_raw_buffer_load_b128(rws, ok0 ? OOB : oq, 0, AUX_SC1);
        const v4u n1 = __builtin_amdgcn_raw_buffer_load_b128(rws, ok1 ? OOB : oq + 1024, 0, AUX_SC1);
        q0 = ok0 ? q0 : n0; q1 = ok1 ? q1 : n1;
      }
      L[L_Q + lane] = (q0.x & 0xffffu) | (q0.z << 16);
      L[L_Q + 64 + lane] = (q1.x & 0xffffu) | (q1.z << 16);
      LGKM0();
      if (lane == 0) L[L_QRDY] = 1u;
    }
    STAMP(2);
    {
      const v4u zero = {0u, 0u, 0u, 0u};
      __builtin_amdgcn_raw_buffer_store_b128(zero, rws, p1_off, 0, AUX_SC1);
      __builtin_amdgcn_raw_buffer_store_b128(zero, rws, p1_off, P1_H1, AUX_SC1);
#pragma unroll
      for (int i = 0; i < 5; ++i) {
        const int e = i * 8 + (lane >> 3);
        __builtin_amdgcn_raw_buffer_store_b128(zero, rws, e < NFC ? P2_OFF + (e * NWG + b) * 128 + (lane & 7) * 16 : OOB, 0, AUX_SC1);
      }
    }
    lds_wait<8>(L, L_CNTD, 3u);
    DRAIN();
    if (lane == 0) __builtin_amdgcn_raw_buffer_store_b64(v2u{rlo, rhi}, rws, RDY_OFF + b * 8, 0, AUX_SC1);
    STAMP(3);
#pragma nounroll
    for (unsigned sp = 0; sp < SPIN_CAP; ++sp) {
      __builtin_amdgcn_s_sleep(100);
      CLOBBER();
      const v4u f0 = __builtin_amdgcn_raw_buffer_load_b128(rws, RDY_OFF + lane * 16, 0, AUX_SC1);
      const v4u f1 = __builtin_amdgcn_raw_buffer_load_b128(rws, RDY_OFF + 1024 + lane * 16, 0, AUX_SC1);
      const bool ok = f0.x == rlo && f0.y == rhi && f0.z == rlo && f0.w == rhi && f1.x == rlo && f1.y == rhi && f1.z == rlo && f1.w == rhi;
      if (__all(ok)) break;
    }
    CLOBBER();
    if (lane == 0) L[L_GO] = 1u;
    STAMP(4);
    if (b < NFC) {
      const int of = FP_OFF + (lane >> 3) * (D * 4) + fl * 128 + (lane & 7) * 16 + fh * 8;
      v2u f[18];
#pragma unroll
      for (int i = 0; i < 18; ++i) f[i] = __builtin_amdgcn_raw_buffer_load_b64(rws, of, i * 8 * D * 4, AUX_SC1);
      const unsigned xv = *(const unsigned*)(a.x + 32 * fl + 4 * (lane & 7) + 2 * fh);
      float y0 = 0.f, y1 = 0.f;
#pragma unroll
      for (int i = 0; i < 18; ++i) { y0 += asf(f[i].x); y1 += asf(f[i].y); }
      y0 += dpp<ROR8>(y0); y1 += dpp<ROR8>(y1);
      y0 = xrow_sum(y0); y1 = xrow_sum(y1);
      if (lane < 8) *(LAS f32x4*)(LF + L_G + 4 * lane) = f32x4{rbf(silu_f(rbf(y0))), rbf(silu_f(rbf(y1))), bflo(xv), bfhi(xv)};
      LGKM0();
      if (lane == 0) L[L_FFR] = 1u;
    }
    STAMP(5);
    lds_wait<4>(L, L_SDONE, 1u);
    STAMP(6);
    {
      v4u p[2];
      poll_lines<2>(p, rws, P1_OFF + (g * 16 + j) * 2048 + lane * 16);
      float M = fmaxf(asf(~p[0].w), asf(~p[1].w));
      M = fmaxf(M, dpp<ROR8>(M)); M = xrow_max(M);
      float Ls = 0.f, o0 = 0.f, o1 = 0.f;
#pragma unroll
      for (int i = 0; i < 2; ++i) {
        const float wt = __builtin_amdgcn_exp2f(asf(~p[i].w) - M);
        Ls = fmaf(wt, asf(p[i].y), Ls); o0 = fmaf(wt, asf(p[i].x), o0); o1 = fmaf(wt, asf(p[i].z), o1);
      }
      Ls += dpp<ROR8>(Ls); o0 += dpp<ROR8>(o0); o1 += dpp<ROR8>(o1);
      Ls = xrow_sum(Ls); o0 = xrow_sum(o0); o1 = xrow_sum(o1);
      const unsigned opk = cvtpk(o0 / Ls, o1 / Ls);
      if (lane < 8) L[L_O + lane] = opk;
      LGKM0();
      if (lane == 0) L[L_ORDY] = 1u;
    }
    STAMP(7);
  }
}

}

extern "C" void kernel_launch(void* const* d_in, const int* in_sizes, int n_in, void* d_out, int out_size, void* d_ws, size_t ws_size,
                              hipStream_t stream) {
  Args a;
  a.x = (const unsigned short*)d_in[0];
  a.k = (const unsigned char*)d_in[1];
  a.v = (const unsigned char*)d_in[2];
  a.wq = (const unsigned char*)d_in[3];
  a.wo = (const unsigned char*)d_in[4];
  a.wg = (const unsigned char*)d_in[6];
  a.wu = (const unsigned char*)d_in[7];
  a.wd = (const unsigned char*)d_in[8];
  a.out = (unsigned short*)d_out;
  a.ws = (unsigned char*)d_ws;
  hipLaunchKernelGGL(fused_decode_kernel, dim3(NWG), dim3(NTHR), 0, stream, a);
}
```

```cpp
#include <hip/hip_runtime.h>
#include <hip/hip_bf16.h>
#include <stdint.h>
#include <stdio.h>

namespace {

typedef unsigned v4u __attribute__((ext_vector_type(4)));
typedef unsigned v2u __attribute__((ext_vector_type(2)));
typedef float f32x4 __attribute__((ext_vector_type(4)));
typedef float f32x2 __attribute__((ext_vector_type(2)));
typedef __bf16 bf16x8 __attribute__((ext_vector_type(8)));
typedef __bf16 bf16x2 __attribute__((ext_vector_type(2)));
typedef unsigned long long u64;
#define LAS __attribute__((address_space(3)))

#define NS_TILES 20
#define PACE_W 8
#define HALF_N 64
#define DBG_NOW 0
#define RING2 0
#define ATOM_BRANCH 0
#define AUXQ 0
#define AUXG 0
#define AUXD 0

constexpr int D = 576, SK = 65536, DH = 128, FF = 4608, QN = 4096;
constexpr int NWG = 256, NTHR = 512;
constexpr int NT = SK / 32;
constexpr int NS = NS_TILES;
constexpr int DYN0 = NS * 64;
constexpr unsigned TK_FULL = NT - DYN0 - HALF_N, TK_END = TK_FULL + 2 * HALF_N;
constexpr unsigned HEAD_BYTES = (unsigned)SK * DH * 2;
constexpr int NCG = D / 4;
constexpr int NFC = 36;
constexpr int NFT = FF / 32;
constexpr int OOB = 0x7f000000;

constexpr int E_OFF = 0;
constexpr int C_OFF = 256;
constexpr int RDY_OFF = 8192;
constexpr int QG_OFF = 16384;
constexpr int P1_OFF = 65536;
constexpr int P2_OFF = P1_OFF + 16 * 16 * 16 * 128;
constexpr int FP_OFF = P2_OFF + NFC * NWG * 128;
constexpr int WS_BYTES = FP_OFF + NFT * D * 4;
constexpr int ST_OFF = 4 << 20;
static_assert(WS_BYTES <= ST_OFF, "");

constexpr int AUX_NT = 2, AUX_SC1 = 16;
constexpr unsigned RSRC3 = 0x00020000u;
constexpr unsigned SPIN_CAP = 1u << 17;

constexpr int L_QRDY = 0, L_TRDY = 1, L_CNTA = 2, L_CNTQ = 3, L_CNTS = 4, L_CNTF = 5, L_GO = 6, L_FFR = 7, L_SDONE = 8, L_YD = 9, L_QPUB = 10, L_ORDY = 11, L_CNTD = 12;
constexpr int L_NCTL = 16;
constexpr int L_TAG = 16;
constexpr int L_Q = 32;
constexpr int L_RED = 192;
constexpr int L_O = 384;
constexpr int L_REDQ = 400;
constexpr int L_G = 448;
constexpr int L_FIN = 512;
constexpr int L_XN = 576;
constexpr int L_ML = 1152;
constexpr int L_ACC = 1280;
constexpr int L_TOTAL = 24 * 1024;

constexpr float SCALE = 0.08838834764831845f;
constexpr float LOG2E = 1.4426950408889634f;

__device__ __forceinline__ unsigned cvtpk(float lo, float hi) {
  f32x2 v = {lo, hi};
  return __builtin_bit_cast(unsigned, __builtin_convertvector(v, bf16x2));
}
__device__ __forceinline__ float bflo(unsigned u) { return __builtin_bit_cast(float, u << 16); }
__device__ __forceinline__ float bfhi(unsigned u) { return __builtin_bit_cast(float, u & 0xffff0000u); }
__device__ __forceinline__ float rbf(float x) { return bflo(cvtpk(x, x)); }
__device__ __forceinline__ float asf(unsigned u) { return __builtin_bit_cast(float, u); }
__device__ __forceinline__ unsigned asu(float f) { return __builtin_bit_cast(unsigned, f); }

template <int CTRL> __device__ __forceinline__ float dpp(float x) {
  return __builtin_bit_cast(float, __builtin_amdgcn_mov_dpp(__builtin_bit_cast(int, x), CTRL, 0xf, 0xf, true));
}
constexpr int XOR1 = 0xB1, XOR2 = 0x4E, HMIR = 0x141, ROR8 = 0x128, ROR4 = 0x124, SHL4 = 0x104, SHR4 = 0x114, QB0 = 0x00, QB2 = 0xAA;

__device__ __forceinline__ float xrow_max(float x) {
  auto s = __builtin_amdgcn_permlane16_swap(asu(x), asu(x), false, false);
  x = fmaxf(asf(s[0]), asf(s[1]));
  auto t = __builtin_amdgcn_permlane32_swap(asu(x), asu(x), false, false);
  return fmaxf(asf(t[0]), asf(t[1]));
}
__device__ __forceinline__ float xrow_sum(float x) {
  auto s = __builtin_amdgcn_permlane16_swap(asu(x), asu(x), false, false);
  x = asf(s[0]) + asf(s[1]);
  auto t = __builtin_amdgcn_permlane32_swap(asu(x), asu(x), false, false);
  return asf(t[0]) + asf(t[1]);
}
__device__ __forceinline__ float wave_sum(float x) {
  x += dpp<XOR1>(x); x += dpp<XOR2>(x); x += dpp<HMIR>(x); x += dpp<ROR8>(x);
  return xrow_sum(x);
}
__device__ __forceinline__ float swap32_add(float a, float b) {
  auto s = __builtin_amdgcn_permlane32_swap(asu(a), asu(b), false, false);
  return asf(s[0]) + asf(s[1]);
}
__device__ __forceinline__ float swap16_add(float a, float b) {
  auto s = __builtin_amdgcn_permlane16_swap(asu(a), asu(b), false, false);
  return asf(s[0]) + asf(s[1]);
}
__device__ __forceinline__ f32x4 mfma(v4u a, v4u b, f32x4 c) {
  return __builtin_amdgcn_mfma_f32_16x16x32_bf16(__builtin_bit_cast(bf16x8, a), __builtin_bit_cast(bf16x8, b), c, 0, 0, 0);
}
__device__ __forceinline__ float dot2(unsigned a, unsigned b, float c) {
  return __builtin_amdgcn_fdot2_f32_bf16(__builtin_bit_cast(bf16x2, a), __builtin_bit_cast(bf16x2, b), c, false);
}
__device__ __forceinline__ float silu_f(float x) { return x / (1.0f + __expf(-x)); }

#define WG_BARRIER() do { __builtin_amdgcn_sched_barrier(0); __builtin_amdgcn_s_barrier(); __builtin_amdgcn_sched_barrier(0); } while (0)
#define CLOBBER() asm volatile("" ::: "memory")
#define LGKM0() asm volatile("s_waitcnt lgkmcnt(0)" ::: "memory")
#define DRAIN() asm volatile("s_waitcnt vmcnt(0)" ::: "memory")

__device__ __forceinline__ unsigned lds_ld(volatile LAS unsigned* L, int i) { return __builtin_amdgcn_readfirstlane(L[i]); }
template <int SLEEP> __device__ __forceinline__ void lds_wait(volatile LAS unsigned* L, int i, unsigned want) {
#pragma nounroll
  for (unsigned s = 0; s < (SPIN_CAP << 3); ++s) {
    if (lds_ld(L, i) >= want) break;
    __builtin_amdgcn_s_sleep(SLEEP);
  }
  CLOBBER();
}
__device__ __forceinline__ unsigned lds_inc(volatile LAS unsigned* L, int i, int lane) {
  unsigned r = 0;
  if (lane == 0) r = __hip_atomic_fetch_add((LAS unsigned*)(L + i), 1u, __ATOMIC_RELAXED, __HIP_MEMORY_SCOPE_WORKGROUP);
  r = __builtin_amdgcn_readfirstlane(r);
  CLOBBER();
  return r;
}

struct Args {
  const unsigned short* x;
  const unsigned char* k;
  const unsigned char* v;
  const unsigned char* wq;
  const unsigned char* wo;
  const unsigned char* wg;
  const unsigned char* wu;
  const unsigned char* wd;
  unsigned short* out;
  unsigned char* ws;
};

#define STAMP(i) do { } while (0)

#define KOFF(i) (((i) >> 2) * 4096 + (((i) >> 1) & 1) * 1024 + ((i) & 1) * 128)
struct Tile { v4u k[8]; v4u v[8]; };

struct SState {
  f32x4 acc;
  float m, l;
  v4u fa, fb;
};

template <int QD> __device__ __forceinline__ void build_frag(v4u& f, unsigned pb) {
  constexpr int r0 = (0 - QD) & 3, r1 = (1 - QD) & 3, r2 = (2 - QD) & 3, r3 = (3 - QD) & 3;
  f.x = __builtin_amdgcn_update_dpp(f.x, pb, r0 ? 0x120 + 4 * r0 : 0xE4, 0xf, 0x1, false);
  f.y = __builtin_amdgcn_update_dpp(f.y, pb, r1 ? 0x120 + 4 * r1 : 0xE4, 0xf, 0x2, false);
  f.z = __builtin_amdgcn_update_dpp(f.z, pb, r2 ? 0x120 + 4 * r2 : 0xE4, 0xf, 0x4, false);
  f.w = __builtin_amdgcn_update_dpp(f.w, pb, r3 ? 0x120 + 4 * r3 : 0xE4, 0xf, 0x8, false);
}

__device__ __forceinline__ v4u ldkv(__amdgpu_buffer_rsrc_t r, int voff, unsigned soff) {
  return __builtin_amdgcn_raw_buffer_load_b128(r, voff, soff, AUX_NT);
}

__device__ __forceinline__ void process_tile(Tile& t, SState& s, const v4u (&qf)[4], __amdgpu_buffer_rsrc_t rk, __amdgpu_buffer_rsrc_t rv,
                                             int voffk, int voffv, unsigned off0, unsigned off1, float cap1, unsigned halfmask, bool hsel) {
  float x[4][2];
#pragma unroll
  for (int u = 0; u < 4; ++u) {
    float a0 = 0.f, a1 = 0.f;
#pragma unroll
    for (int hf = 0; hf < 2; ++hf) {
#pragma unroll
      for (int e = 0; e < 4; ++e) { a0 = dot2(t.k[2 * u + hf][e], qf[hf][e], a0); a1 = dot2(t.k[2 * u + hf][e], qf[2 + hf][e], a1); }
      asm volatile("" : "+v"(a0), "+v"(a1));
      t.k[2 * u + hf] = ldkv(rk, voffk + KOFF(2 * u + hf), u < 2 ? off0 : off1);
      __builtin_amdgcn_sched_barrier(0);
    }
    x[u][0] = a0; x[u][1] = a1;
  }
  float pp[2];
#pragma unroll
  for (int v = 0; v < 2; ++v) {
    float y[2];
#pragma unroll
    for (int h = 0; h < 2; ++h) {
      const float t1 = x[2 * v][h] + dpp<HMIR>(x[2 * v][h]), t2 = x[2 * v + 1][h] + dpp<HMIR>(x[2 * v + 1][h]);
      y[h] = asf(__builtin_amdgcn_update_dpp(asu(t1), asu(t2), 0xE4, 0xf, 0xA, false));
    }
    const float t1 = y[0] + dpp<XOR2>(y[0]), t2 = y[1] + dpp<XOR2>(y[1]);
    const float z = hsel ? t2 : t1;
    pp[v] = z + dpp<XOR1>(z);
  }
  unsigned pk = cvtpk(pp[0], pp[1]);
  pk = cvtpk(bflo(pk) * SCALE, bfhi(pk) * SCALE);
  const float s0 = bflo(pk) * LOG2E, s1 = fminf(bfhi(pk) * LOG2E, cap1);
  float tm = fmaxf(s0, s1);
  tm = fmaxf(tm, dpp<ROR4>(tm));
  tm = fmaxf(tm, dpp<ROR8>(tm));
  tm = xrow_max(tm);
  const float mn = fmaxf(s.m, ceilf(tm));
  const float alpha = __builtin_amdgcn_exp2f(s.m - mn);
  const float e0 = __builtin_amdgcn_exp2f(s0 - mn);
  const float e1 = __builtin_amdgcn_exp2f(s1 - mn);
  s.m = mn;
  s.l = fmaf(s.l, alpha, e0 + e1);
  const float a0 = dpp<QB0>(alpha), a1 = dpp<QB2>(alpha);
  s.acc.x *= a0; s.acc.y *= a0; s.acc.z *= a1; s.acc.w *= a1;
  const unsigned pb0 = cvtpk(e0, e0) & halfmask, pb1 = cvtpk(e1, e1) & halfmask;
#define PV_STEP(IDX, F, PB, QD) \
  build_frag<QD>(F, PB); \
  s.acc = mfma(F, t.v[IDX], s.acc); \
  t.v[IDX] = ldkv(rv, voffv + (IDX) * 1024, (IDX) < 4 ? off0 : off1); \
  __builtin_amdgcn_sched_barrier(0);
  PV_STEP(0, s.fa, pb0, 0) PV_STEP(1, s.fb, pb0, 1) PV_STEP(2, s.fa, pb0, 2) PV_STEP(3, s.fb, pb0, 3)
  PV_STEP(4, s.fa, pb1, 0) PV_STEP(5, s.fb, pb1, 1) PV_STEP(6, s.fa, pb1, 2) PV_STEP(7, s.fb, pb1, 3)
#undef PV_STEP
}

template <int N> __device__ __forceinline__ void poll_lines(v4u (&u)[N], __amdgpu_buffer_rsrc_t rws, int off) {
#pragma unroll
  for (int i = 0; i < N; ++i) u[i] = v4u{0u, 0u, 0u, 0u};
#pragma nounroll
  for (unsigned sp = 0; sp < SPIN_CAP; ++sp) {
    CLOBBER();
    bool ok[N], all = true;
#pragma unroll
    for (int i = 0; i < N; ++i) { ok[i] = u[i].y != 0u && u[i].w != 0u; all = all && ok[i]; }
    if (__all(all)) break;
    v4u nw[N];
#pragma unroll
    for (int i = 0; i < N; ++i) nw[i] = __builtin_amdgcn_raw_buffer_load_b128(rws, ok[i] ? OOB : off, i * 1024, AUX_SC1);
#pragma unroll
    for (int i = 0; i < N; ++i) u[i] = ok[i] ? u[i] : nw[i];
  }
}

__global__ void __launch_bounds__(NTHR, 2) fused_decode_kernel(Args a) {
  __shared__ __attribute__((aligned(16))) unsigned lds_raw[L_TOTAL];
  volatile LAS unsigned* L = (volatile LAS unsigned*)lds_raw;
  LAS float* LF = (LAS float*)lds_raw;
  const int tid = threadIdx.x, lane = tid & 63, w = __builtin_amdgcn_readfirstlane(tid >> 6);
  const int b = blockIdx.x, g = b >> 4, j = b & 15;
  const __amdgpu_buffer_rsrc_t rws = __builtin_amdgcn_make_buffer_rsrc((void*)a.ws, 0, WS_BYTES, RSRC3);
  const int r4 = lane >> 4, c16 = lane & 15;
  const int p1_off = P1_OFF + ((g * 16 + (c16 >> 1)) * 16 + j) * 128 + (8 * (c16 & 1) + 2 * r4) * 8;
  constexpr int P1_H1 = 8 * 16 * 128;
  const int slot = ((j & 7) - (g >> 1)) & 7, hi = j >> 3;
  const bool is_ffn = slot < 4 || (slot == 4 && hi == 0);
  const int ft = 18 * (g >> 1) + (slot < 4 ? (g & 1) * 8 + 2 * slot + hi : 16 + (g & 1));
  const int fh = b >= 18 ? 1 : 0, fl = b - 18 * fh;
  STAMP(0);

  if (w >= 4) {
    const int sw = w - 4;
    const __amdgpu_buffer_rsrc_t rk = __builtin_amdgcn_make_buffer_rsrc((void*)(a.k + (size_t)g * HEAD_BYTES), 0, HEAD_BYTES, RSRC3);
    const __amdgpu_buffer_rsrc_t rv = __builtin_amdgcn_make_buffer_rsrc((void*)(a.v + (size_t)g * HEAD_BYTES), 0, HEAD_BYTES, RSRC3);
    const __amdgpu_buffer_rsrc_t rc = __builtin_amdgcn_make_buffer_rsrc((void*)(a.ws + C_OFF + g * 256), 0, 4, RSRC3);
    const int voffk = (8 * (c16 >> 3) + r4) * 256 + (lane & 7) * 16, voffv = lane * 16;
    const unsigned sb = (unsigned)(j * NS * 4 + sw) * 8192u;
    Tile tb;
    WG_BARRIER();
    {
      unsigned qr = 0;
#pragma unroll
      for (int i = 0; i < 16 * (1 + RING2); ++i) {
        if (i >= PACE_W && !qr) { asm volatile("s_waitcnt vmcnt(%0)" ::"n"(PACE_W - 1) : "memory"); qr = lds_ld(L, L_QRDY); }
        Tile& t = tb;
        if ((i & 15) < 8) t.k[i & 7] = ldkv(rk, voffk + KOFF(i & 7), sb + (i >> 4) * 32768u);
        else t.v[i & 7] = ldkv(rv, voffv + (i & 7) * 1024, sb + (i >> 4) * 32768u);
        __builtin_amdgcn_sched_barrier(0);
      }
    }
    lds_wait<1>(L, L_QRDY, 1u);
    STAMP(1);
    v4u qf[4];
#pragma unroll
    for (int i = 0; i < 4; ++i) qf[i] = *(LAS v4u*)(lds_raw + L_Q + 32 * i + 4 * (lane & 7));
    const unsigned c0 = lds_ld(L, L_TAG + 1);
    const unsigned halfmask = (lane & 1) ? 0xffff0000u : 0x0000ffffu;
    SState s;
    s.acc = f32x4{0.f, 0.f, 0.f, 0.f};
    s.m = -INFINITY; s.l = 0.f;
    s.fa = v4u{0u, 0u, 0u, 0u}; s.fb = v4u{0u, 0u, 0u, 0u};
    unsigned off0 = sb + (1 + RING2) * 32768u, off1 = off0;
#define STEP(T, CAP) \
      if (n == NS) { STAMP(2); lds_wait<1>(L, L_GO, 1u); }      \
      unsigned tk = 0; \
      if (!ATOM_BRANCH || n >= NS) tk = __builtin_amdgcn_raw_ptr_buffer_atomic_add_i32(1, rc, lane * 4, n >= NS ? 0 : 4, 0); \
      process_tile(T, s, qf, rk, rv, voffk, voffv, off0, off1, CAP, halfmask, (lane & 2) != 0); \
      const bool more = off0 < HEAD_BYTES; \
      CAP = off1 >= HEAD_BYTES ? -INFINITY : INFINITY; \
      if (n >= NS) { \
        const unsigned idx = __builtin_amdgcn_readfirstlane(tk) - c0; \
        off0 = idx < TK_FULL ? (DYN0 + idx) * 8192u : idx < TK_END ? (DYN0 + TK_FULL) * 8192u + (idx - TK_FULL) * 4096u : (unsigned)OOB; \
        off1 = idx < TK_FULL ? off0 : (unsigned)OOB;         \
      } else { \
        off0 = off1 = sb + (unsigned)n * 32768u; \
      } \
      ++n;
    {
      float cap1 = INFINITY;
      int n = 2;
#pragma nounroll
      for (;;) {
        STEP(tb, cap1)
        if (!more) break;
      }
    }
    STAMP(3);
    {
      float lw = s.l;
      lw += dpp<ROR4>(lw); lw += dpp<ROR8>(lw); lw = xrow_sum(lw);
      *(LAS f32x4*)(LF + L_ACC + (sw * 64 + lane) * 4) = s.acc;
      if (lane == 0 || lane == 2) { LF[L_ML + sw * 4 + lane] = s.m; LF[L_ML + sw * 4 + lane + 1] = lw; }
      if (lane == 0) L[L_SDONE] = 1u;
      LGKM0();
      if (lds_inc(L, L_CNTS, lane) == 3u) {
        f32x4 ml[4], ac[4];
#pragma unroll
        for (int i = 0; i < 4; ++i) { ml[i] = *(LAS f32x4*)(LF + L_ML + i * 4); ac[i] = *(LAS f32x4*)(LF + L_ACC + (i * 64 + lane) * 4); }
        const float M0 = fmaxf(fmaxf(ml[0].x, ml[1].x), fmaxf(ml[2].x, ml[3].x)), M1 = fmaxf(fmaxf(ml[0].z, ml[1].z), fmaxf(ml[2].z, ml[3].z));
        float L0 = 0.f, L1 = 0.f;
        f32x4 o = {0.f, 0.f, 0.f, 0.f};
#pragma unroll
        for (int i = 0; i < 4; ++i) {
          const float w0 = __builtin_amdgcn_exp2f(ml[i].x - M0), w1 = __builtin_amdgcn_exp2f(ml[i].z - M1);
          L0 = fmaf(w0, ml[i].y, L0); L1 = fmaf(w1, ml[i].w, L1);
          o.x = fmaf(w0, ac[i].x, o.x); o.y = fmaf(w0, ac[i].y, o.y); o.z = fmaf(w1, ac[i].z, o.z); o.w = fmaf(w1, ac[i].w, o.w);
        }
        __builtin_amdgcn_raw_buffer_store_b128(v4u{asu(o.x), asu(L0), asu(o.y), ~asu(M0)}, rws, p1_off, 0, AUX_SC1);
        __builtin_amdgcn_raw_buffer_store_b128(v4u{asu(o.z), asu(L1), asu(o.w), ~asu(M1)}, rws, p1_off, P1_H1, AUX_SC1);
      }
    }
    STAMP(4);
    if (b < NFC) {
      lds_wait<1>(L, L_YD, 3u);
      v4u u[8];
      poll_lines<8>(u, rws, P2_OFF + (b * NWG + 64 * sw) * 128 + lane * 16);
      STAMP(5);
      float y0 = 0.f, y1 = 0.f;
#pragma unroll
      for (int i = 0; i < 8; ++i) { y0 += asf(u[i].x); y1 += asf(u[i].z); }
      y0 += dpp<ROR8>(y0); y1 += dpp<ROR8>(y1);
      y0 = xrow_sum(y0); y1 = xrow_sum(y1);
      if (lane < 8) *(LAS f32x2*)(LF + L_FIN + sw * 16 + 2 * lane) = f32x2{y0, y1};
      LGKM0();
      if (lds_inc(L, L_CNTF, lane) == 3u) {
        lds_wait<0>(L, L_FFR, 1u);
        const int c = lane & 7;
        const f32x2 f0 = *(LAS f32x2*)(LF + L_FIN + 2 * c), f1 = *(LAS f32x2*)(LF + L_FIN + 16 + 2 * c), f2 = *(LAS f32x2*)(LF + L_FIN + 32 + 2 * c),
                    f3 = *(LAS f32x2*)(LF + L_FIN + 48 + 2 * c);
        const f32x4 gx = *(LAS f32x4*)(LF + L_G + 4 * c);
        const float sv0 = rbf((f0.x + f1.x) + (f2.x + f3.x)), sv1 = rbf((f0.y + f1.y) + (f2.y + f3.y));
        const unsigned pk = cvtpk(gx.z + rbf(sv0 * gx.x), gx.w + rbf(sv1 * gx.y));
        if (lane < 8) *(unsigned*)(a.out + 32 * fl + 4 * lane + 2 * fh) = pk;
        if (b == 0 && lane == 0) {
          const u64 e = (((u64)L[L_TAG + 5] << 32) | L[L_TAG + 4]) * 6364136223846793005ull + 1442695040888963407ull;
          __builtin_amdgcn_raw_buffer_store_b64(v2u{(unsigned)e, (unsigned)(e >> 32)}, rws, E_OFF, 0, AUX_SC1);
        }
        STAMP(6);
      }
    }
  } else if (w >= 1) {
    const int ta = w - 1, tt = tid - 64;
    __builtin_amdgcn_s_setprio(3);
    const int qs = ((b & 7) * 8 + (b >> 5)) * 4 + ((b >> 3) & 3);
    const __amdgpu_buffer_rsrc_t rx = __builtin_amdgcn_make_buffer_rsrc((void*)a.x, 0, D * 2, RSRC3);
    const __amdgpu_buffer_rsrc_t rq = __builtin_amdgcn_make_buffer_rsrc((void*)(a.wq + qs * 32), 0, DBG_NOW ? 0 : (D - 1) * QN * 2 + 32, RSRC3);
    const int rr = lane >> 2, cc = lane & 3;
    v4u wqr[6];
    const v4u xc0 = __builtin_amdgcn_raw_buffer_load_b128(rx, lane * 16, 0, 0);
    const v4u xc1 = __builtin_amdgcn_raw_buffer_load_b128(rx, 1024 + lane * 16, 0, 0);
    unsigned xo[3];
#pragma unroll
    for (int i = 0; i < 3; ++i) xo[i] = __builtin_amdgcn_raw_buffer_load_b16(rx, (192 * ta + 64 * i + lane) * 2, 0, 0);
    {
      const int vo = (192 * ta + (lane >> 1)) * (QN * 2) + (lane & 1) * 16;
#pragma unroll
      for (int i = 0; i < 6; ++i) wqr[i] = __builtin_amdgcn_raw_buffer_load_b128(rq, vo, i * 32 * QN * 2, AUXQ);
    }
    WG_BARRIER();
    {
      const float x0 = bflo(__builtin_amdgcn_readfirstlane(xc0.x));
      float s0 = 0.f, s1 = 0.f, q0 = 0.f, q1 = 0.f;
#pragma unroll
      for (int i = 0; i < 4; ++i) {
        const float d0 = bflo(xc0[i]) - x0, d1 = bfhi(xc0[i]) - x0, d2 = bflo(xc1[i]) - x0, d3 = bfhi(xc1[i]) - x0;
        s0 += d0 + d1; s1 += d2 + d3;
        q0 = fmaf(d0, d0, q0); q0 = fmaf(d1, d1, q0); q1 = fmaf(d2, d2, q1); q1 = fmaf(d3, d3, q1);
      }
      const float dm = wave_sum(s0 + (lane < 8 ? s1 : 0.f)) * (1.0f / D);
      const float var = fmaxf(wave_sum(q0 + (lane < 8 ? q1 : 0.f)) * (1.0f / D) - dm * dm, 0.f);
      const float mb = rbf(x0 + dm), den = rbf(sqrtf(rbf(rbf(var) + 1e-5f)));
#pragma unroll
      for (int i = 0; i < 3; ++i) LF[L_XN + 192 * ta + 64 * i + lane] = rbf(rbf(bflo(xo[i]) - mb) / den);
      LGKM0();
    }
    {
      float acc[8];
#pragma unroll
      for (int e = 0; e < 8; ++e) acc[e] = 0.f;
#pragma unroll
      for (int i = 0; i < 6; ++i) {
        const float xn = LF[L_XN + 192 * ta + 32 * i + (lane >> 1)];
#pragma unroll
        for (int e = 0; e < 4; ++e) { acc[2 * e] = fmaf(xn, bflo(wqr[i][e]), acc[2 * e]); acc[2 * e + 1] = fmaf(xn, bfhi(wqr[i][e]), acc[2 * e + 1]); }
      }
      float u[4], v2[2];
#pragma unroll
      for (int e = 0; e < 4; ++e) u[e] = swap32_add(acc[e], acc[4 + e]);
#pragma unroll
      for (int e = 0; e < 2; ++e) v2[e] = swap16_add(u[e], u[2 + e]);
      const float t1 = v2[0] + dpp<ROR8>(v2[0]), t2 = v2[1] + dpp<ROR8>(v2[1]);
      float z = (lane & 8) ? t2 : t1;
      z += dpp<XOR2>(z);
      const float z1 = dpp<SHL4>(z), z2 = dpp<SHR4>(z);
      z += (lane & 4) ? z2 : z1;
      if ((lane & 6) == 0) LF[L_REDQ + ta * 16 + 8 * (lane & 1) + ((lane >> 3) & 7)] = z;
      LGKM0();
      if (lds_inc(L, L_CNTQ, lane) == 2u) {
        const float qv = (LF[L_REDQ + c16] + LF[L_REDQ + 16 + c16]) + LF[L_REDQ + 32 + c16];
        lds_wait<0>(L, L_TRDY, 1u);
        const unsigned tlo = lds_ld(L, L_TAG + 2), thi = lds_ld(L, L_TAG + 3);
        if (lane < 16) __builtin_amdgcn_raw_buffer_store_b64(v2u{(cvtpk(qv, qv) & 0xffffu) | (tlo << 16), thi}, rws, QG_OFF + (qs * 16 + lane) * 8, 0, AUX_SC1);
        CLOBBER();
        if (lane == 0) L[L_QPUB] = 1u;
        STAMP(1);
      }
    }
    lds_wait<1>(L, L_QPUB, 1u);
    const int vo2 = tt < NCG ? tt * 8 : OOB;
    const int o2 = tt < NCG ? (tt & 7) * 16 : OOB;
    v2u wr[16];
    {
      const __amdgpu_buffer_rsrc_t ro = __builtin_amdgcn_make_buffer_rsrc((void*)(a.wo + (size_t)b * 16 * D * 2), 0, DBG_NOW ? 0 : 16 * D * 2, RSRC3);
#pragma unroll
      for (int k = 0; k < 16; ++k) wr[k] = __builtin_amdgcn_raw_buffer_load_b64(ro, vo2, k * D * 2, AUXD);
      __builtin_amdgcn_sched_barrier(0);
    }
    STAMP(2);
    if (is_ffn) {
      STAMP(7);
      const __amdgpu_buffer_rsrc_t ra = __builtin_amdgcn_make_buffer_rsrc((void*)(a.wg + ft * 64), 0, D * FF * 2 - ft * 64, RSRC3);
      const __amdgpu_buffer_rsrc_t rb = __builtin_amdgcn_make_buffer_rsrc((void*)(a.wu + ft * 64), 0, D * FF * 2 - ft * 64, RSRC3);
      const __amdgpu_buffer_rsrc_t rd = __builtin_amdgcn_make_buffer_rsrc((void*)(a.wd + (size_t)ft * 32 * D * 2), 0, 32 * D * 2, RSRC3);
      const int vof = (192 * ta + rr) * (FF * 2) + cc * 16;
      constexpr int fstep = 16 * FF * 2;
      v4u wa[12], wb[12];
      v2u dn[32];
#pragma unroll
      for (int i = 0; i < 12; ++i) {
        wa[i] = __builtin_amdgcn_raw_buffer_load_b128(ra, vof, i * fstep, AUXG);
        wb[i] = __builtin_amdgcn_raw_buffer_load_b128(rb, vof, i * fstep, AUXG);
      }
#pragma unroll
      for (int k = 0; k < 32; ++k) dn[k] = __builtin_amdgcn_raw_buffer_load_b64(rd, vo2, k * D * 2, AUXD);
      __builtin_amdgcn_sched_barrier(0);
      float acc[16];
#pragma unroll
      for (int e = 0; e < 16; ++e) acc[e] = 0.f;
#pragma unroll
      for (int i = 0; i < 12; ++i) {
        const float xn = LF[L_XN + 192 * ta + 16 * i + rr];
#pragma unroll
        for (int e = 0; e < 4; ++e) {
          acc[2 * e] = fmaf(xn, bflo(wa[i][e]), acc[2 * e]); acc[2 * e + 1] = fmaf(xn, bfhi(wa[i][e]), acc[2 * e + 1]);
          acc[8 + 2 * e] = fmaf(xn, bflo(wb[i][e]), acc[8 + 2 * e]); acc[9 + 2 * e] = fmaf(xn, bfhi(wb[i][e]), acc[9 + 2 * e]);
        }
        asm volatile("" : "+v"(acc[0]), "+v"(acc[1]), "+v"(acc[2]), "+v"(acc[3]), "+v"(acc[4]), "+v"(acc[5]), "+v"(acc[6]), "+v"(acc[7]),
                          "+v"(acc[8]), "+v"(acc[9]), "+v"(acc[10]), "+v"(acc[11]), "+v"(acc[12]), "+v"(acc[13]), "+v"(acc[14]), "+v"(acc[15]));
      }
      float u[8], v4[4], z[2];
#pragma unroll
      for (int e = 0; e < 8; ++e) u[e] = swap32_add(acc[e], acc[8 + e]);
#pragma unroll
      for (int e = 0; e < 4; ++e) v4[e] = swap16_add(u[e], u[4 + e]);
#pragma unroll
      for (int e = 0; e < 2; ++e) { const float t1 = v4[e] + dpp<ROR8>(v4[e]), t2 = v4[2 + e] + dpp<ROR8>(v4[2 + e]); z[e] = (lane & 8) ? t2 : t1; }
      const float t1 = z[0] + dpp<SHL4>(z[0]), t2 = z[1] + dpp<SHR4>(z[1]);
      const float zz = (lane & 4) ? t2 : t1;
      LF[L_RED + ta * 64 + (lane & 32) + 8 * cc + ((lane >> 2) & 7)] = zz;
      LGKM0();
      lds_inc(L, L_CNTA, lane);
      lds_wait<0>(L, L_CNTA, 3u);
      const int u5 = lane & 31;
      const float gp = rbf((LF[L_RED + u5] + LF[L_RED + 64 + u5]) + LF[L_RED + 128 + u5]);
      const float up = rbf((LF[L_RED + 32 + u5] + LF[L_RED + 96 + u5]) + LF[L_RED + 160 + u5]);
      const float h = rbf(rbf(silu_f(gp)) * up);
      f32x4 y = {0.f, 0.f, 0.f, 0.f};
#pragma unroll
      for (int k = 0; k < 32; ++k) {
        const float hk = asf(__builtin_amdgcn_readlane(asu(h), k));
        y.x = fmaf(hk, bflo(dn[k].x), y.x); y.y = fmaf(hk, bfhi(dn[k].x), y.y); y.z = fmaf(hk, bflo(dn[k].y), y.z); y.w = fmaf(hk, bfhi(dn[k].y), y.w);
      }
      __builtin_amdgcn_raw_buffer_store_b128(__builtin_bit_cast(v4u, y), rws, tt < NCG ? FP_OFF + ft * (D * 4) + tt * 16 : OOB, 0, AUX_SC1);
      STAMP(6);
    }
    DRAIN();
    lds_inc(L, L_CNTD, lane);
    float wf[64];
#pragma unroll
    for (int k = 0; k < 16; ++k) { wf[4 * k] = bflo(wr[k].x); wf[4 * k + 1] = bfhi(wr[k].x); wf[4 * k + 2] = bflo(wr[k].y); wf[4 * k + 3] = bfhi(wr[k].y); }
#pragma unroll
    for (int k = 0; k < 64; ++k) asm volatile("" : "+v"(wf[k]));
    lds_wait<8>(L, L_SDONE, 1u);
    STAMP(3);
    lds_wait<0>(L, L_ORDY, 1u);
    STAMP(4);
    const unsigned opk = L[L_O + (lane & 7)];
    f32x4 y = {0.f, 0.f, 0.f, 0.f};
#pragma unroll
    for (int k = 0; k < 8; ++k) {
      const unsigned sp = __builtin_amdgcn_readlane(opk, k);
      const float e0 = bflo(sp), e1 = bfhi(sp);
      y.x = fmaf(e0, wf[8 * k], y.x); y.y = fmaf(e0, wf[8 * k + 1], y.y); y.z = fmaf(e0, wf[8 * k + 2], y.z); y.w = fmaf(e0, wf[8 * k + 3], y.w);
      y.x = fmaf(e1, wf[8 * k + 4], y.x); y.y = fmaf(e1, wf[8 * k + 5], y.y); y.z = fmaf(e1, wf[8 * k + 6], y.z); y.w = fmaf(e1, wf[8 * k + 7], y.w);
    }
    const int op = P2_OFF + ((tt >> 3) * NWG + b) * 128;
    __builtin_amdgcn_raw_buffer_store_b128(v4u{asu(y.x), 1u, asu(y.y), 1u}, rws, o2 + op, 0, AUX_SC1);
    __builtin_amdgcn_raw_buffer_store_b128(v4u{asu(y.z), 1u, asu(y.w), 1u}, rws, o2 + op, 18 * NWG * 128, AUX_SC1);
    lds_inc(L, L_YD, lane);
    STAMP(5);
  } else {
    __builtin_amdgcn_s_setprio(3);
    const v2u ev = __builtin_amdgcn_raw_buffer_load_b64(rws, E_OFF, 0, AUX_SC1);
    const unsigned cv = __builtin_amdgcn_raw_buffer_load_b32(rws, C_OFF + g * 256, 0, AUX_SC1);
    if (lane < L_NCTL) L[lane] = 0u;
    LGKM0();
    WG_BARRIER();
    const unsigned elo = __builtin_amdgcn_readfirstlane(ev.x), ehi = __builtin_amdgcn_readfirstlane(ev.y);
    const unsigned qlo = (elo ^ 0x7F4A7C15u) & 0xffffu, qhi = ((elo ^ 0x7F4A7C15u) >> 16) | ((ehi ^ 0x9E3779B9u) << 16);
    const unsigned rlo = elo ^ 0xC2B2AE35u, rhi = ehi ^ 0x27D4EB2Fu;
    if (lane == 0) { L[L_TAG + 1] = cv; L[L_TAG + 2] = qlo; L[L_TAG + 3] = qhi; L[L_TAG + 4] = elo; L[L_TAG + 5] = ehi; }
    LGKM0();
    if (lane == 0) L[L_TRDY] = 1u;
    STAMP(1);
    {
      const int oq = QG_OFF + g * 2048 + lane * 16;
      v4u q0 = {0u, ~qhi, 0u, 0u}, q1 = {0u, ~qhi, 0u, 0u};
#pragma nounroll
      for (unsigned sp = 0; sp < SPIN_CAP; ++sp) {
        CLOBBER();
        const bool ok0 = q0.y == qhi && q0.w == qhi && (q0.x >> 16) == qlo && (q0.z >> 16) == qlo;
        const bool ok1 = q1.y == qhi && q1.w == qhi && (q1.x >> 16) == qlo && (q1.z >> 16) == qlo;
        if (__all(ok0 && ok1)) break;
        const v4u n0 = __builtin_amdgcn_raw_buffer_load_b128(rws, ok0 ? OOB : oq, 0, AUX_SC1);
        const v4u n1 = __builtin_amdgcn_raw_buffer_load_b128(rws, ok1 ? OOB : oq + 1024, 0, AUX_SC1);
        q0 = ok0 ? q0 : n0; q1 = ok1 ? q1 : n1;
      }
      L[L_Q + lane] = (q0.x & 0xffffu) | (q0.z << 16);
      L[L_Q + 64 + lane] = (q1.x & 0xffffu) | (q1.z << 16);
      LGKM0();
      if (lane == 0) L[L_QRDY] = 1u;
    }
    STAMP(2);
    {
      const v4u zero = {0u, 0u, 0u, 0u};
      __builtin_amdgcn_raw_buffer_store_b128(zero, rws, p1_off, 0, AUX_SC1);
      __builtin_amdgcn_raw_buffer_store_b128(zero, rws, p1_off, P1_H1, AUX_SC1);
#pragma unroll
      for (int i = 0; i < 5; ++i) {
        const int e = i * 8 + (lane >> 3);
        __builtin_amdgcn_raw_buffer_store_b128(zero, rws, e < NFC ? P2_OFF + (e * NWG + b) * 128 + (lane & 7) * 16 : OOB, 0, AUX_SC1);
      }
    }
    lds_wait<8>(L, L_CNTD, 3u);
    DRAIN();
    if (lane == 0) __builtin_amdgcn_raw_buffer_store_b64(v2u{rlo, rhi}, rws, RDY_OFF + b * 8, 0, AUX_SC1);
    STAMP(3);
#pragma nounroll
    for (unsigned sp = 0; sp < SPIN_CAP; ++sp) {
      __builtin_amdgcn_s_sleep(100);
      CLOBBER();
      const v4u f0 = __builtin_amdgcn_raw_buffer_load_b128(rws, RDY_OFF + lane * 16, 0, AUX_SC1);
      const v4u f1 = __builtin_amdgcn_raw_buffer_load_b128(rws, RDY_OFF + 1024 + lane * 16, 0, AUX_SC1);
      const bool ok = f0.x == rlo && f0.y == rhi && f0.z == rlo && f0.w == rhi && f1.x == rlo && f1.y == rhi && f1.z == rlo && f1.w == rhi;
      if (__all(ok)) break;
    }
    CLOBBER();
    if (lane == 0) L[L_GO] = 1u;
    STAMP(4);
    if (b < NFC) {
      const int of = FP_OFF + (lane >> 3) * (D * 4) + fl * 128 + (lane & 7) * 16 + fh * 8;
      v2u f[18];
#pragma unroll
      for (int i = 0; i < 18; ++i) f[i] = __builtin_amdgcn_raw_buffer_load_b64(rws, of, i * 8 * D * 4, AUX_SC1);
      const unsigned xv = *(const unsigned*)(a.x + 32 * fl + 4 * (lane & 7) + 2 * fh);
      float y0 = 0.f, y1 = 0.f;
#pragma unroll
      for (int i = 0; i < 18; ++i) { y0 += asf(f[i].x); y1 += asf(f[i].y); }
      y0 += dpp<ROR8>(y0); y1 += dpp<ROR8>(y1);
      y0 = xrow_sum(y0); y1 = xrow_sum(y1);
      if (lane < 8) *(LAS f32x4*)(LF + L_G + 4 * lane) = f32x4{rbf(silu_f(rbf(y0))), rbf(silu_f(rbf(y1))), bflo(xv), bfhi(xv)};
      LGKM0();
      if (lane == 0) L[L_FFR] = 1u;
    }
    STAMP(5);
    lds_wait<4>(L, L_SDONE, 1u);
    STAMP(6);
    {
      v4u p[2];
      poll_lines<2>(p, rws, P1_OFF + (g * 16 + j) * 2048 + lane * 16);
      float M = fmaxf(asf(~p[0].w), asf(~p[1].w));
      M = fmaxf(M, dpp<ROR8>(M)); M = xrow_max(M);
      float Ls = 0.f, o0 = 0.f, o1 = 0.f;
#pragma unroll
      for (int i = 0; i < 2; ++i) {
        const float wt = __builtin_amdgcn_exp2f(asf(~p[i].w) - M);
        Ls = fmaf(wt, asf(p[i].y), Ls); o0 = fmaf(wt, asf(p[i].x), o0); o1 = fmaf(wt, asf(p[i].z), o1);
      }
      Ls += dpp<ROR8>(Ls); o0 += dpp<ROR8>(o0); o1 += dpp<ROR8>(o1);
      Ls = xrow_sum(Ls); o0 = xrow_sum(o0); o1 = xrow_sum(o1);
      const unsigned opk = cvtpk(o0 / Ls, o1 / Ls);
      if (lane < 8) L[L_O + lane] = opk;
      LGKM0();
      if (lane == 0) L[L_ORDY] = 1u;
    }
    STAMP(7);
  }
}

__global__ void __launch_bounds__(256) probe_k(const unsigned char* A, const unsigned char* B, unsigned bytes, unsigned long long* rec) {
  const int lane = threadIdx.x & 63, w = __builtin_amdgcn_readfirstlane(threadIdx.x >> 6), b = blockIdx.x;
  const unsigned per = bytes / 256;
  const __amdgpu_buffer_rsrc_t ra = __builtin_amdgcn_make_buffer_rsrc((void*)(A + (size_t)b * per), 0, per, 0x00020000);
  const __amdgpu_buffer_rsrc_t rb = __builtin_amdgcn_make_buffer_rsrc((void*)(B + (size_t)b * per), 0, per, 0x00020000);
  unsigned long long t0 = __builtin_amdgcn_s_memrealtime();
  v4u a[16]; v4u acc = {0, 0, 0, 0};
#pragma unroll
  for (int i = 0; i < 16; ++i) a[i] = __builtin_amdgcn_raw_buffer_load_b128(i < 8 ? ra : rb, lane * 16 + (i & 3) * 1024, w * 8192 + ((i >> 2) & 1) * 4096, 2);
  for (unsigned off = 32768; off < per + 32768; off += 32768) {
#pragma unroll
    for (int i = 0; i < 16; ++i) { acc ^= a[i]; asm volatile("" : "+v"(acc)); a[i] = __builtin_amdgcn_raw_buffer_load_b128(i < 8 ? ra : rb, lane * 16 + (i & 3) * 1024, off + w * 8192 + ((i >> 2) & 1) * 4096, 2); __builtin_amdgcn_sched_barrier(0); }
  }
  unsigned long long t1 = __builtin_amdgcn_s_memrealtime();
  if (lane == 0) { rec[(b * 4 + w) * 2] = t0 + (acc.x == 0x12345u); rec[(b * 4 + w) * 2 + 1] = t1; }
}
__global__ void probe_print(const unsigned long long* rec, int np, int ncol) {
  for (int p = 0; p < np; ++p) {
    unsigned long long mn = ~0ull, sum = 0;
    for (int i = 0; i < 1024; ++i) mn = rec[(size_t)p * 2048 + 2 * i] < mn ? rec[(size_t)p * 2048 + 2 * i] : mn;
    for (int i = 0; i < 1024; ++i) sum += rec[(size_t)p * 2048 + 2 * i + 1] - mn;
    printf(" %5d", (int)(sum / 1024));
    if (p % ncol == ncol - 1) printf("\n");
  }
}
}

extern "C" void kernel_launch(void* const* d_in, const int* in_sizes, int n_in, void* d_out, int out_size, void* d_ws, size_t ws_size,
                              hipStream_t stream) {
  fprintf(stderr, "PTRS K %p V %p ws %p wq %p wg %p out %p\n", d_in[1], d_in[2], d_ws, d_in[3], d_in[6], d_out);
  {
    const unsigned RB = 32u << 20;
    unsigned long long* rec = (unsigned long long*)((char*)d_ws + (32 << 20));
    auto reg = [&](int r) { return (const unsigned char*)d_in[1 + (r >> 3)] + (size_t)(r & 7) * RB; };
    int p = 0;
    for (int i = 0; i < 16; ++i)
      for (int j = 0; j < 16; ++j) { hipLaunchKernelGGL(probe_k, dim3(256), dim3(256), 0, stream, reg(i), reg(j == i ? (i ^ 1) : j), RB, rec + (size_t)p * 2048); ++p; }
    hipLaunchKernelGGL(probe_print, dim3(1), dim3(1), 0, stream, rec, p, 16);
  }
  Args a;
  a.x = (const unsigned short*)d_in[0];
  a.k = (const unsigned char*)d_in[1];
  a.v = (const unsigned char*)d_in[2];
  a.wq = (const unsigned char*)d_in[3];
  a.wo = (const unsigned char*)d_in[4];
  a.wg = (const unsigned char*)d_in[6];
  a.wu = (const unsigned char*)d_in[7];
  a.wd = (const unsigned char*)d_in[8];
  a.out = (unsigned short*)d_out;
  a.ws = (unsigned char*)d_ws;
  hipLaunchKernelGGL(fused_decode_kernel, dim3(NWG), dim3(NTHR), 0, stream, a);
}
```

```cpp
#include <hip/hip_runtime.h>
#include <hip/hip_bf16.h>
#include <stdint.h>

namespace {

typedef unsigned v4u __attribute__((ext_vector_type(4)));
typedef unsigned v2u __attribute__((ext_vector_type(2)));
typedef float f32x4 __attribute__((ext_vector_type(4)));
typedef float f32x2 __attribute__((ext_vector_type(2)));
typedef __bf16 bf16x8 __attribute__((ext_vector_type(8)));
typedef __bf16 bf16x2 __attribute__((ext_vector_type(2)));
typedef unsigned long long u64;
#define LAS __attribute__((address_space(3)))

#define NS_TILES 20
#define PACE_W 8
#define HALF_N 64
#define DBG_NOW 0
#define RING2 0
#define ATOM_BRANCH 0
#define AUXQ 0
#define AUXG 0
#define AUXD 0

constexpr int D = 576, SK = 65536, DH = 128, FF = 4608, QN = 4096;
constexpr int NWG = 256, NTHR = 512;
constexpr int NT = SK / 32;
constexpr int NS = NS_TILES;
constexpr int DYN0 = NS * 64;
constexpr unsigned TK_FULL = NT - DYN0 - HALF_N, TK_END = TK_FULL + 2 * HALF_N;
constexpr unsigned HEAD_BYTES = (unsigned)SK * DH * 2;
constexpr int NCG = D / 4;
constexpr int NFC = 36;
constexpr int NFT = FF / 32;
constexpr int OOB = 0x7f000000;

constexpr int E_OFF = 0;
constexpr int C_OFF = 256;
constexpr int RDY_OFF = 8192;
constexpr int QG_OFF = 16384;
constexpr int P1_OFF = 65536;
constexpr int P2_OFF = P1_OFF + 16 * 16 * 16 * 128;
constexpr int FP_OFF = P2_OFF + NFC * NWG * 128;
constexpr int WS_BYTES = FP_OFF + NFT * D * 4;
constexpr int ST_OFF = 4 << 20;
static_assert(WS_BYTES <= ST_OFF, "");

constexpr int AUX_NT = 2, AUX_SC1 = 16;
constexpr unsigned RSRC3 = 0x00020000u;
constexpr unsigned SPIN_CAP = 1u << 17;

constexpr int L_QRDY = 0, L_TRDY = 1, L_CNTA = 2, L_CNTQ = 3, L_CNTS = 4, L_CNTF = 5, L_GO = 6, L_FFR = 7, L_SDONE = 8, L_YD = 9, L_QPUB = 10, L_ORDY = 11, L_CNTD = 12;
constexpr int L_NCTL = 16;
constexpr int L_TAG = 16;
constexpr int L_Q = 32;
constexpr int L_RED = 192;
constexpr int L_O = 384;
constexpr int L_REDQ = 400;
constexpr int L_G = 448;
constexpr int L_FIN = 512;
constexpr int L_XN = 576;
constexpr int L_ML = 1152;
constexpr int L_ACC = 1280;
constexpr int L_TOTAL = 24 * 1024;

constexpr float SCALE = 0.08838834764831845f;
constexpr float LOG2E = 1.4426950408889634f;

__device__ __forceinline__ unsigned cvtpk(float lo, float hi) {
  f32x2 v = {lo, hi};
  return __builtin_bit_cast(unsigned, __builtin_convertvector(v, bf16x2));
}
__device__ __forceinline__ float bflo(unsigned u) { return __builtin_bit_cast(float, u << 16); }
__device__ __forceinline__ float bfhi(unsigned u) { return __builtin_bit_cast(float, u & 0xffff0000u); }
__device__ __forceinline__ float rbf(float x) { return bflo(cvtpk(x, x)); }
__device__ __forceinline__ float asf(unsigned u) { return __builtin_bit_cast(float, u); }
__device__ __forceinline__ unsigned asu(float f) { return __builtin_bit_cast(unsigned, f); }

template <int CTRL> __device__ __forceinline__ float dpp(float x) {
  return __builtin_bit_cast(float, __builtin_amdgcn_mov_dpp(__builtin_bit_cast(int, x), CTRL, 0xf, 0xf, true));
}
constexpr int XOR1 = 0xB1, XOR2 = 0x4E, HMIR = 0x141, ROR8 = 0x128, ROR4 = 0x124, SHL4 = 0x104, SHR4 = 0x114, QB0 = 0x00, QB2 = 0xAA;

__device__ __forceinline__ float xrow_max(float x) {
  auto s = __builtin_amdgcn_permlane16_swap(asu(x), asu(x), false, false);
  x = fmaxf(asf(s[0]), asf(s[1]));
  auto t = __builtin_amdgcn_permlane32_swap(asu(x), asu(x), false, false);
  return fmaxf(asf(t[0]), asf(t[1]));
}
__device__ __forceinline__ float xrow_sum(float x) {
  auto s = __builtin_amdgcn_permlane16_swap(asu(x), asu(x), false, false);
  x = asf(s[0]) + asf(s[1]);
  auto t = __builtin_amdgcn_permlane32_swap(asu(x), asu(x), false, false);
  return asf(t[0]) + asf(t[1]);
}
__device__ __forceinline__ float wave_sum(float x) {
  x += dpp<XOR1>(x); x += dpp<XOR2>(x); x += dpp<HMIR>(x); x += dpp<ROR8>(x);
  return xrow_sum(x);
}
__device__ __forceinline__ float swap32_add(float a, float b) {
  auto s = __builtin_amdgcn_permlane32_swap(asu(a), asu(b), false, false);
  return asf(s[0]) + asf(s[1]);
}
__device__ __forceinline__ float swap16_add(float a, float b) {
  auto s = __builtin_amdgcn_permlane16_swap(asu(a), asu(b), false, false);
  return asf(s[0]) + asf(s[1]);
}
__device__ __forceinline__ f32x4 mfma(v4u a, v4u b, f32x4 c) {
  return __builtin_amdgcn_mfma_f32_16x16x32_bf16(__builtin_bit_cast(bf16x8, a), __builtin_bit_cast(bf16x8, b), c, 0, 0, 0);
}
__device__ __forceinline__ float dot2(unsigned a, unsigned b, float c) {
  return __builtin_amdgcn_fdot2_f32_bf16(__builtin_bit_cast(bf16x2, a), __builtin_bit_cast(bf16x2, b), c, false);
}
__device__ __forceinline__ float silu_f(float x) { return x / (1.0f + __expf(-x)); }

#define WG_BARRIER() do { __builtin_amdgcn_sched_barrier(0); __builtin_amdgcn_s_barrier(); __builtin_amdgcn_sched_barrier(0); } while (0)
#define CLOBBER() asm volatile("" ::: "memory")
#define LGKM0() asm volatile("s_waitcnt lgkmcnt(0)" ::: "memory")
#define DRAIN() asm volatile("s_waitcnt vmcnt(0)" ::: "memory")

__device__ __forceinline__ unsigned lds_ld(volatile LAS unsigned* L, int i) { return __builtin_amdgcn_readfirstlane(L[i]); }
template <int SLEEP> __device__ __forceinline__ void lds_wait(volatile LAS unsigned* L, int i, unsigned want) {
#pragma nounroll
  for (unsigned s = 0; s < (SPIN_CAP << 3); ++s) {
    if (lds_ld(L, i) >= want) break;
    __builtin_amdgcn_s_sleep(SLEEP);
  }
  CLOBBER();
}
__device__ __forceinline__ unsigned lds_inc(volatile LAS unsigned* L, int i, int lane) {
  unsigned r = 0;
  if (lane == 0) r = __hip_atomic_fetch_add((LAS unsigned*)(L + i), 1u, __ATOMIC_RELAXED, __HIP_MEMORY_SCOPE_WORKGROUP);
  r = __builtin_amdgcn_readfirstlane(r);
  CLOBBER();
  return r;
}

struct Args {
  const unsigned short* x;
  const unsigned char* k;
  const unsigned char* v;
  const unsigned char* wq;
  const unsigned char* wo;
  const unsigned char* wg;
  const unsigned char* wu;
  const unsigned char* wd;
  unsigned short* out;
  unsigned char* ws;
};

#define STAMP(i) do { } while (0)

#define KOFF(i) (((i) >> 2) * 4096 + (((i) >> 1) & 1) * 1024 + ((i) & 1) * 128)
struct Tile { v4u k[8]; v4u v[8]; };

struct SState {
  f32x4 acc;
  float m, l;
  v4u fa, fb;
};

template <int QD> __device__ __forceinline__ void build_frag(v4u& f, unsigned pb) {
  constexpr int r0 = (0 - QD) & 3, r1 = (1 - QD) & 3, r2 = (2 - QD) & 3, r3 = (3 - QD) & 3;
  f.x = __builtin_amdgcn_update_dpp(f.x, pb, r0 ? 0x120 + 4 * r0 : 0xE4, 0xf, 0x1, false);
  f.y = __builtin_amdgcn_update_dpp(f.y, pb, r1 ? 0x120 + 4 * r1 : 0xE4, 0xf, 0x2, false);
  f.z = __builtin_amdgcn_update_dpp(f.z, pb, r2 ? 0x120 + 4 * r2 : 0xE4, 0xf, 0x4, false);
  f.w = __builtin_amdgcn_update_dpp(f.w, pb, r3 ? 0x120 + 4 * r3 : 0xE4, 0xf, 0x8, false);
}

__device__ __forceinline__ v4u ldkv(__amdgpu_buffer_rsrc_t r, int voff, unsigned soff) {
  return __builtin_amdgcn_raw_buffer_load_b128(r, voff, soff, AUX_NT);
}

__device__ __forceinline__ void process_tile(Tile& t, SState& s, const v4u (&qf)[4], __amdgpu_buffer_rsrc_t rk, __amdgpu_buffer_rsrc_t rv,
                                             int voffk, int voffv, unsigned off0, unsigned off1, float cap1, unsigned halfmask, bool hsel) {
  float x[4][2];
#pragma unroll
  for (int u = 0; u < 4; ++u) {
    float a0 = 0.f, a1 = 0.f;
#pragma unroll
    for (int hf = 0; hf < 2; ++hf) {
#pragma unroll
      for (int e = 0; e < 4; ++e) { a0 = dot2(t.k[2 * u + hf][e], qf[hf][e], a0); a1 = dot2(t.k[2 * u + hf][e], qf[2 + hf][e], a1); }
      asm volatile("" : "+v"(a0), "+v"(a1));
      t.k[2 * u + hf] = ldkv(rk, voffk + KOFF(2 * u + hf), u < 2 ? off0 : off1);
      __builtin_amdgcn_sched_barrier(0);
    }
    x[u][0] = a0; x[u][1] = a1;
  }
  float pp[2];
#pragma unroll
  for (int v = 0; v < 2; ++v) {
    float y[2];
#pragma unroll
    for (int h = 0; h < 2; ++h) {
      const float t1 = x[2 * v][h] + dpp<HMIR>(x[2 * v][h]), t2 = x[2 * v + 1][h] + dpp<HMIR>(x[2 * v + 1][h]);
      y[h] = asf(__builtin_amdgcn_update_dpp(asu(t1), asu(t2), 0xE4, 0xf, 0xA, false));
    }
    const float t1 = y[0] + dpp<XOR2>(y[0]), t2 = y[1] + dpp<XOR2>(y[1]);
    const float z = hsel ? t2 : t1;
    pp[v] = z + dpp<XOR1>(z);
  }
  unsigned pk = cvtpk(pp[0], pp[1]);
  pk = cvtpk(bflo(pk) * SCALE, bfhi(pk) * SCALE);
  const float s0 = bflo(pk) * LOG2E, s1 = fminf(bfhi(pk) * LOG2E, cap1);
  float tm = fmaxf(s0, s1);
  tm = fmaxf(tm, dpp<ROR4>(tm));
  tm = fmaxf(tm, dpp<ROR8>(tm));
  tm = xrow_max(tm);
  const float mn = fmaxf(s.m, ceilf(tm));
  const float alpha = __builtin_amdgcn_exp2f(s.m - mn);
  const float e0 = __builtin_amdgcn_exp2f(s0 - mn);
  const float e1 = __builtin_amdgcn_exp2f(s1 - mn);
  s.m = mn;
  s.l = fmaf(s.l, alpha, e0 + e1);
  const float a0 = dpp<QB0>(alpha), a1 = dpp<QB2>(alpha);
  s.acc.x *= a0; s.acc.y *= a0; s.acc.z *= a1; s.acc.w *= a1;
  const unsigned pb0 = cvtpk(e0, e0) & halfmask, pb1 = cvtpk(e1, e1) & halfmask;
#define PV_STEP(IDX, F, PB, QD) \
  build_frag<QD>(F, PB); \
  s.acc = mfma(F, t.v[IDX], s.acc); \
  t.v[IDX] = ldkv(rv, voffv + (IDX) * 1024, (IDX) < 4 ? off0 : off1); \
  __builtin_amdgcn_sched_barrier(0);
  PV_STEP(0, s.fa, pb0, 0) PV_STEP(1, s.fb, pb0, 1) PV_STEP(2, s.fa, pb0, 2) PV_STEP(3, s.fb, pb0, 3)
  PV_STEP(4, s.fa, pb1, 0) PV_STEP(5, s.fb, pb1, 1) PV_STEP(6, s.fa, pb1, 2) PV_STEP(7, s.fb, pb1, 3)
#undef PV_STEP
}

template <int N> __device__ __forceinline__ void poll_lines(v4u (&u)[N], __amdgpu_buffer_rsrc_t rws, int off) {
#pragma unroll
  for (int i = 0; i < N; ++i) u[i] = v4u{0u, 0u, 0u, 0u};
#pragma nounroll
  for (unsigned sp = 0; sp < SPIN_CAP; ++sp) {
    CLOBBER();
    bool ok[N], all = true;
#pragma unroll
    for (int i = 0; i < N; ++i) { ok[i] = u[i].y != 0u && u[i].w != 0u; all = all && ok[i]; }
    if (__all(all)) break;
    v4u nw[N];
#pragma unroll
    for (int i = 0; i < N; ++i) nw[i] = __builtin_amdgcn_raw_buffer_load_b128(rws, ok[i] ? OOB : off, i * 1024, AUX_SC1);
#pragma unroll
    for (int i = 0; i < N; ++i) u[i] = ok[i] ? u[i] : nw[i];
  }
}

__global__ void __launch_bounds__(NTHR, 2) fused_decode_kernel(Args a) {
  __shared__ __attribute__((aligned(16))) unsigned lds_raw[L_TOTAL];
  volatile LAS unsigned* L = (volatile LAS unsigned*)lds_raw;
  LAS float* LF = (LAS float*)lds_raw;
  const int tid = threadIdx.x, lane = tid & 63, w = __builtin_amdgcn_readfirstlane(tid >> 6);
  const int b = blockIdx.x, g = b >> 4, j = b & 15;
  const __amdgpu_buffer_rsrc_t rws = __builtin_amdgcn_make_buffer_rsrc((void*)a.ws, 0, WS_BYTES, RSRC3);
  const int r4 = lane >> 4, c16 = lane & 15;
  const int p1_off = P1_OFF + ((g * 16 + (c16 >> 1)) * 16 + j) * 128 + (8 * (c16 & 1) + 2 * r4) * 8;
  constexpr int P1_H1 = 8 * 16 * 128;
  const int slot = ((j & 7) - (g >> 1)) & 7, hi = j >> 3;
  const bool is_ffn = slot < 4 || (slot == 4 && hi == 0);
  const int ft = 18 * (g >> 1) + (slot < 4 ? (g & 1) * 8 + 2 * slot + hi : 16 + (g & 1));
  const int fh = b >= 18 ? 1 : 0, fl = b - 18 * fh;
  const u64 tq0 = __builtin_amdgcn_s_memrealtime();

  if (w >= 4) {
    const int sw = w - 4;
    const __amdgpu_buffer_rsrc_t rk = __builtin_amdgcn_make_buffer_rsrc((void*)(a.k + (size_t)g * HEAD_BYTES), 0, HEAD_BYTES, RSRC3);
    const __amdgpu_buffer_rsrc_t rv = __builtin_amdgcn_make_buffer_rsrc((void*)(a.v + (size_t)g * HEAD_BYTES), 0, HEAD_BYTES, RSRC3);
    const __amdgpu_buffer_rsrc_t rc = __builtin_amdgcn_make_buffer_rsrc((void*)(a.ws + C_OFF + g * 256), 0, 4, RSRC3);
    const int voffk = (8 * (c16 >> 3) + r4) * 256 + (lane & 7) * 16, voffv = lane * 16;
    const unsigned sb = (unsigned)(j * NS * 4 + sw) * 8192u;
    Tile tb;
    WG_BARRIER();
    {
      unsigned qr = 0;
#pragma unroll
      for (int i = 0; i < 16 * (1 + RING2); ++i) {
        if (i >= PACE_W && !qr) { asm volatile("s_waitcnt vmcnt(%0)" ::"n"(PACE_W - 1) : "memory"); qr = lds_ld(L, L_QRDY); }
        Tile& t = tb;
        if ((i & 15) < 8) t.k[i & 7] = ldkv(rk, voffk + KOFF(i & 7), sb + (i >> 4) * 32768u);
        else t.v[i & 7] = ldkv(rv, voffv + (i & 7) * 1024, sb + (i >> 4) * 32768u);
        __builtin_amdgcn_sched_barrier(0);
      }
    }
    lds_wait<1>(L, L_QRDY, 1u);
    STAMP(1);
    v4u qf[4];
#pragma unroll
    for (int i = 0; i < 4; ++i) qf[i] = *(LAS v4u*)(lds_raw + L_Q + 32 * i + 4 * (lane & 7));
    const unsigned c0 = lds_ld(L, L_TAG + 1);
    const unsigned halfmask = (lane & 1) ? 0xffff0000u : 0x0000ffffu;
    SState s;
    s.acc = f32x4{0.f, 0.f, 0.f, 0.f};
    s.m = -INFINITY; s.l = 0.f;
    s.fa = v4u{0u, 0u, 0u, 0u}; s.fb = v4u{0u, 0u, 0u, 0u};
    unsigned off0 = sb + (1 + RING2) * 32768u, off1 = off0;
#define STEP(T, CAP) \
      if (n == NS) { STAMP(2); lds_wait<1>(L, L_GO, 1u); }      \
      unsigned tk = 0; \
      if (!ATOM_BRANCH || n >= NS) tk = __builtin_amdgcn_raw_ptr_buffer_atomic_add_i32(1, rc, lane * 4, n >= NS ? 0 : 4, 0); \
      process_tile(T, s, qf, rk, rv, voffk, voffv, off0, off1, CAP, halfmask, (lane & 2) != 0); \
      const bool more = off0 < HEAD_BYTES; \
      CAP = off1 >= HEAD_BYTES ? -INFINITY : INFINITY; \
      if (n >= NS) { \
        const unsigned idx = __builtin_amdgcn_readfirstlane(tk) - c0; \
        off0 = idx < TK_FULL ? (DYN0 + idx) * 8192u : idx < TK_END ? (DYN0 + TK_FULL) * 8192u + (idx - TK_FULL) * 4096u : (unsigned)OOB; \
        off1 = idx < TK_FULL ? off0 : (unsigned)OOB;         \
      } else { \
        off0 = off1 = sb + (unsigned)n * 32768u; \
      } \
      ++n;
    {
      float cap1 = INFINITY;
      int n = 2;
#pragma nounroll
      for (;;) {
        STEP(tb, cap1)
        if (!more) break;
      }
    }
    STAMP(3);
    {
      float lw = s.l;
      lw += dpp<ROR4>(lw); lw += dpp<ROR8>(lw); lw = xrow_sum(lw);
      *(LAS f32x4*)(LF + L_ACC + (sw * 64 + lane) * 4) = s.acc;
      if (lane == 0 || lane == 2) { LF[L_ML + sw * 4 + lane] = s.m; LF[L_ML + sw * 4 + lane + 1] = lw; }
      if (lane == 0) L[L_SDONE] = 1u;
      LGKM0();
      if (lds_inc(L, L_CNTS, lane) == 3u) {
        f32x4 ml[4], ac[4];
#pragma unroll
        for (int i = 0; i < 4; ++i) { ml[i] = *(LAS f32x4*)(LF + L_ML + i * 4); ac[i] = *(LAS f32x4*)(LF + L_ACC + (i * 64 + lane) * 4); }
        const float M0 = fmaxf(fmaxf(ml[0].x, ml[1].x), fmaxf(ml[2].x, ml[3].x)), M1 = fmaxf(fmaxf(ml[0].z, ml[1].z), fmaxf(ml[2].z, ml[3].z));
        float L0 = 0.f, L1 = 0.f;
        f32x4 o = {0.f, 0.f, 0.f, 0.f};
#pragma unroll
        for (int i = 0; i < 4; ++i) {
          const float w0 = __builtin_amdgcn_exp2f(ml[i].x - M0), w1 = __builtin_amdgcn_exp2f(ml[i].z - M1);
          L0 = fmaf(w0, ml[i].y, L0); L1 = fmaf(w1, ml[i].w, L1);
          o.x = fmaf(w0, ac[i].x, o.x); o.y = fmaf(w0, ac[i].y, o.y); o.z = fmaf(w1, ac[i].z, o.z); o.w = fmaf(w1, ac[i].w, o.w);
        }
        __builtin_amdgcn_raw_buffer_store_b128(v4u{asu(o.x), asu(L0), asu(o.y), ~asu(M0)}, rws, p1_off, 0, AUX_SC1);
        __builtin_amdgcn_raw_buffer_store_b128(v4u{asu(o.z), asu(L1), asu(o.w), ~asu(M1)}, rws, p1_off, P1_H1, AUX_SC1);
      }
    }
    STAMP(4);
    if (b < NFC) {
      lds_wait<1>(L, L_YD, 3u);
      v4u u[8];
      poll_lines<8>(u, rws, P2_OFF + (b * NWG + 64 * sw) * 128 + lane * 16);
      STAMP(5);
      float y0 = 0.f, y1 = 0.f;
#pragma unroll
      for (int i = 0; i < 8; ++i) { y0 += asf(u[i].x); y1 += asf(u[i].z); }
      y0 += dpp<ROR8>(y0); y1 += dpp<ROR8>(y1);
      y0 = xrow_sum(y0); y1 = xrow_sum(y1);
      if (lane < 8) *(LAS f32x2*)(LF + L_FIN + sw * 16 + 2 * lane) = f32x2{y0, y1};
      LGKM0();
      if (lds_inc(L, L_CNTF, lane) == 3u) {
        lds_wait<0>(L, L_FFR, 1u);
        const int c = lane & 7;
        const f32x2 f0 = *(LAS f32x2*)(LF + L_FIN + 2 * c), f1 = *(LAS f32x2*)(LF + L_FIN + 16 + 2 * c), f2 = *(LAS f32x2*)(LF + L_FIN + 32 + 2 * c),
                    f3 = *(LAS f32x2*)(LF + L_FIN + 48 + 2 * c);
        const f32x4 gx = *(LAS f32x4*)(LF + L_G + 4 * c);
        const float sv0 = rbf((f0.x + f1.x) + (f2.x + f3.x)), sv1 = rbf((f0.y + f1.y) + (f2.y + f3.y));
        const unsigned pk = cvtpk(gx.z + rbf(sv0 * gx.x), gx.w + rbf(sv1 * gx.y));
        if (lane < 8) *(unsigned*)(a.out + 32 * fl + 4 * lane + 2 * fh) = pk;
        if (b == 0 && lane == 0) {
          const u64 e = (((u64)L[L_TAG + 5] << 32) | L[L_TAG + 4]) * 6364136223846793005ull + 1442695040888963407ull;
          __builtin_amdgcn_raw_buffer_store_b64(v2u{(unsigned)e, (unsigned)(e >> 32)}, rws, E_OFF, 0, AUX_SC1);
        }
        STAMP(6);
      }
    }
  } else if (w >= 1) {
    const int ta = w - 1, tt = tid - 64;
    __builtin_amdgcn_s_setprio(3);
    const int qs = ((b & 7) * 8 + (b >> 5)) * 4 + ((b >> 3) & 3);
    const __amdgpu_buffer_rsrc_t rx = __builtin_amdgcn_make_buffer_rsrc((void*)a.x, 0, D * 2, RSRC3);
    const __amdgpu_buffer_rsrc_t rq = __builtin_amdgcn_make_buffer_rsrc((void*)(a.wq + qs * 32), 0, DBG_NOW ? 0 : (D - 1) * QN * 2 + 32, RSRC3);
    const int rr = lane >> 2, cc = lane & 3;
    v4u wqr[6];
    const v4u xc0 = __builtin_amdgcn_raw_buffer_load_b128(rx, lane * 16, 0, 0);
    const v4u xc1 = __builtin_amdgcn_raw_buffer_load_b128(rx, 1024 + lane * 16, 0, 0);
    unsigned xo[3];
#pragma unroll
    for (int i = 0; i < 3; ++i) xo[i] = __builtin_amdgcn_raw_buffer_load_b16(rx, (192 * ta + 64 * i + lane) * 2, 0, 0);
    {
      const int vo = (192 * ta + (lane >> 1)) * (QN * 2) + (lane & 1) * 16;
#pragma unroll
      for (int i = 0; i < 6; ++i) wqr[i] = __builtin_amdgcn_raw_buffer_load_b128(rq, vo, i * 32 * QN * 2, AUXQ);
    }
    WG_BARRIER();
    {
      const float x0 = bflo(__builtin_amdgcn_readfirstlane(xc0.x));
      float s0 = 0.f, s1 = 0.f, q0 = 0.f, q1 = 0.f;
#pragma unroll
      for (int i = 0; i < 4; ++i) {
        const float d0 = bflo(xc0[i]) - x0, d1 = bfhi(xc0[i]) - x0, d2 = bflo(xc1[i]) - x0, d3 = bfhi(xc1[i]) - x0;
        s0 += d0 + d1; s1 += d2 + d3;
        q0 = fmaf(d0, d0, q0); q0 = fmaf(d1, d1, q0); q1 = fmaf(d2, d2, q1); q1 = fmaf(d3, d3, q1);
      }
      const float dm = wave_sum(s0 + (lane < 8 ? s1 : 0.f)) * (1.0f / D);
      const float var = fmaxf(wave_sum(q0 + (lane < 8 ? q1 : 0.f)) * (1.0f / D) - dm * dm, 0.f);
      const float mb = rbf(x0 + dm), den = rbf(sqrtf(rbf(rbf(var) + 1e-5f)));
#pragma unroll
      for (int i = 0; i < 3; ++i) LF[L_XN + 192 * ta + 64 * i + lane] = rbf(rbf(bflo(xo[i]) - mb) / den);
      LGKM0();
    }
    {
      float acc[8];
#pragma unroll
      for (int e = 0; e < 8; ++e) acc[e] = 0.f;
#pragma unroll
      for (int i = 0; i < 6; ++i) {
        const float xn = LF[L_XN + 192 * ta + 32 * i + (lane >> 1)];
#pragma unroll
        for (int e = 0; e < 4; ++e) { acc[2 * e] = fmaf(xn, bflo(wqr[i][e]), acc[2 * e]); acc[2 * e + 1] = fmaf(xn, bfhi(wqr[i][e]), acc[2 * e + 1]); }
      }
      float u[4], v2[2];
#pragma unroll
      for (int e = 0; e < 4; ++e) u[e] = swap32_add(acc[e], acc[4 + e]);
#pragma unroll
      for (int e = 0; e < 2; ++e) v2[e] = swap16_add(u[e], u[2 + e]);
      const float t1 = v2[0] + dpp<ROR8>(v2[0]), t2 = v2[1] + dpp<ROR8>(v2[1]);
      float z = (lane & 8) ? t2 : t1;
      z += dpp<XOR2>(z);
      const float z1 = dpp<SHL4>(z), z2 = dpp<SHR4>(z);
      z += (lane & 4) ? z2 : z1;
      if ((lane & 6) == 0) LF[L_REDQ + ta * 16 + 8 * (lane & 1) + ((lane >> 3) & 7)] = z;
      LGKM0();
      if (lds_inc(L, L_CNTQ, lane) == 2u) {
        const float qv = (LF[L_REDQ + c16] + LF[L_REDQ + 16 + c16]) + LF[L_REDQ + 32 + c16];
        lds_wait<0>(L, L_TRDY, 1u);
        const unsigned tlo = lds_ld(L, L_TAG + 2), thi = lds_ld(L, L_TAG + 3);
        if (lane < 16) __builtin_amdgcn_raw_buffer_store_b64(v2u{(cvtpk(qv, qv) & 0xffffu) | (tlo << 16), thi}, rws, QG_OFF + (qs * 16 + lane) * 8, 0, AUX_SC1);
        CLOBBER();
        if (lane == 0) L[L_QPUB] = 1u;
        STAMP(1);
      }
    }
    lds_wait<1>(L, L_QPUB, 1u);
    const int vo2 = tt < NCG ? tt * 8 : OOB;
    const int o2 = tt < NCG ? (tt & 7) * 16 : OOB;
    v2u wr[16];
    {
      const __amdgpu_buffer_rsrc_t ro = __builtin_amdgcn_make_buffer_rsrc((void*)(a.wo + (size_t)b * 16 * D * 2), 0, DBG_NOW ? 0 : 16 * D * 2, RSRC3);
#pragma unroll
      for (int k = 0; k < 16; ++k) wr[k] = __builtin_amdgcn_raw_buffer_load_b64(ro, vo2, k * D * 2, AUXD);
      __builtin_amdgcn_sched_barrier(0);
    }
    STAMP(2);
    if (is_ffn) {
      STAMP(7);
      const __amdgpu_buffer_rsrc_t ra = __builtin_amdgcn_make_buffer_rsrc((void*)(a.wg + ft * 64), 0, D * FF * 2 - ft * 64, RSRC3);
      const __amdgpu_buffer_rsrc_t rb = __builtin_amdgcn_make_buffer_rsrc((void*)(a.wu + ft * 64), 0, D * FF * 2 - ft * 64, RSRC3);
      const __amdgpu_buffer_rsrc_t rd = __builtin_amdgcn_make_buffer_rsrc((void*)(a.wd + (size_t)ft * 32 * D * 2), 0, 32 * D * 2, RSRC3);
      const int vof = (192 * ta + rr) * (FF * 2) + cc * 16;
      constexpr int fstep = 16 * FF * 2;
      v4u wa[12], wb[12];
      v2u dn[32];
#pragma unroll
      for (int i = 0; i < 12; ++i) {
        wa[i] = __builtin_amdgcn_raw_buffer_load_b128(ra, vof, i * fstep, AUXG);
        wb[i] = __builtin_amdgcn_raw_buffer_load_b128(rb, vof, i * fstep, AUXG);
      }
#pragma unroll
      for (int k = 0; k < 32; ++k) dn[k] = __builtin_amdgcn_raw_buffer_load_b64(rd, vo2, k * D * 2, AUXD);
      __builtin_amdgcn_sched_barrier(0);
      float acc[16];
#pragma unroll
      for (int e = 0; e < 16; ++e) acc[e] = 0.f;
#pragma unroll
      for (int i = 0; i < 12; ++i) {
        const float xn = LF[L_XN + 192 * ta + 16 * i + rr];
#pragma unroll
        for (int e = 0; e < 4; ++e) {
          acc[2 * e] = fmaf(xn, bflo(wa[i][e]), acc[2 * e]); acc[2 * e + 1] = fmaf(xn, bfhi(wa[i][e]), acc[2 * e + 1]);
          acc[8 + 2 * e] = fmaf(xn, bflo(wb[i][e]), acc[8 + 2 * e]); acc[9 + 2 * e] = fmaf(xn, bfhi(wb[i][e]), acc[9 + 2 * e]);
        }
        asm volatile("" : "+v"(acc[0]), "+v"(acc[1]), "+v"(acc[2]), "+v"(acc[3]), "+v"(acc[4]), "+v"(acc[5]), "+v"(acc[6]), "+v"(acc[7]),
                          "+v"(acc[8]), "+v"(acc[9]), "+v"(acc[10]), "+v"(acc[11]), "+v"(acc[12]), "+v"(acc[13]), "+v"(acc[14]), "+v"(acc[15]));
      }
      float u[8], v4[4], z[2];
#pragma unroll
      for (int e = 0; e < 8; ++e) u[e] = swap32_add(acc[e], acc[8 + e]);
#pragma unroll
      for (int e = 0; e < 4; ++e) v4[e] = swap16_add(u[e], u[4 + e]);
#pragma unroll
      for (int e = 0; e < 2; ++e) { const float t1 = v4[e] + dpp<ROR8>(v4[e]), t2 = v4[2 + e] + dpp<ROR8>(v4[2 + e]); z[e] = (lane & 8) ? t2 : t1; }
      const float t1 = z[0] + dpp<SHL4>(z[0]), t2 = z[1] + dpp<SHR4>(z[1]);
      const float zz = (lane & 4) ? t2 : t1;
      LF[L_RED + ta * 64 + (lane & 32) + 8 * cc + ((lane >> 2) & 7)] = zz;
      LGKM0();
      lds_inc(L, L_CNTA, lane);
      lds_wait<0>(L, L_CNTA, 3u);
      const int u5 = lane & 31;
      const float gp = rbf((LF[L_RED + u5] + LF[L_RED + 64 + u5]) + LF[L_RED + 128 + u5]);
      const float up = rbf((LF[L_RED + 32 + u5] + LF[L_RED + 96 + u5]) + LF[L_RED + 160 + u5]);
      const float h = rbf(rbf(silu_f(gp)) * up);
      f32x4 y = {0.f, 0.f, 0.f, 0.f};
#pragma unroll
      for (int k = 0; k < 32; ++k) {
        const float hk = asf(__builtin_amdgcn_readlane(asu(h), k));
        y.x = fmaf(hk, bflo(dn[k].x), y.x); y.y = fmaf(hk, bfhi(dn[k].x), y.y); y.z = fmaf(hk, bflo(dn[k].y), y.z); y.w = fmaf(hk, bfhi(dn[k].y), y.w);
      }
      __builtin_amdgcn_raw_buffer_store_b128(__builtin_bit_cast(v4u, y), rws, tt < NCG ? FP_OFF + ft * (D * 4) + tt * 16 : OOB, 0, AUX_SC1);
      STAMP(6);
    }
    DRAIN();
    lds_inc(L, L_CNTD, lane);
    float wf[64];
#pragma unroll
    for (int k = 0; k < 16; ++k) { wf[4 * k] = bflo(wr[k].x); wf[4 * k + 1] = bfhi(wr[k].x); wf[4 * k + 2] = bflo(wr[k].y); wf[4 * k + 3] = bfhi(wr[k].y); }
#pragma unroll
    for (int k = 0; k < 64; ++k) asm volatile("" : "+v"(wf[k]));
    lds_wait<8>(L, L_SDONE, 1u);
    STAMP(3);
    lds_wait<0>(L, L_ORDY, 1u);
    STAMP(4);
    const unsigned opk = L[L_O + (lane & 7)];
    f32x4 y = {0.f, 0.f, 0.f, 0.f};
#pragma unroll
    for (int k = 0; k < 8; ++k) {
      const unsigned sp = __builtin_amdgcn_readlane(opk, k);
      const float e0 = bflo(sp), e1 = bfhi(sp);
      y.x = fmaf(e0, wf[8 * k], y.x); y.y = fmaf(e0, wf[8 * k + 1], y.y); y.z = fmaf(e0, wf[8 * k + 2], y.z); y.w = fmaf(e0, wf[8 * k + 3], y.w);
      y.x = fmaf(e1, wf[8 * k + 4], y.x); y.y = fmaf(e1, wf[8 * k + 5], y.y); y.z = fmaf(e1, wf[8 * k + 6], y.z); y.w = fmaf(e1, wf[8 * k + 7], y.w);
    }
    const int op = P2_OFF + ((tt >> 3) * NWG + b) * 128;
    __builtin_amdgcn_raw_buffer_store_b128(v4u{asu(y.x), 1u, asu(y.y), 1u}, rws, o2 + op, 0, AUX_SC1);
    __builtin_amdgcn_raw_buffer_store_b128(v4u{asu(y.z), 1u, asu(y.w), 1u}, rws, o2 + op, 18 * NWG * 128, AUX_SC1);
    lds_inc(L, L_YD, lane);
    STAMP(5);
  } else {
    __builtin_amdgcn_s_setprio(3);
    const v2u ev = __builtin_amdgcn_raw_buffer_load_b64(rws, E_OFF, 0, AUX_SC1);
    const unsigned cv = __builtin_amdgcn_raw_buffer_load_b32(rws, C_OFF + g * 256, 0, AUX_SC1);
    if (lane < L_NCTL) L[lane] = 0u;
    LGKM0();
    WG_BARRIER();
    const unsigned elo = __builtin_amdgcn_readfirstlane(ev.x), ehi = __builtin_amdgcn_readfirstlane(ev.y);
    const unsigned qlo = (elo ^ 0x7F4A7C15u) & 0xffffu, qhi = ((elo ^ 0x7F4A7C15u) >> 16) | ((ehi ^ 0x9E3779B9u) << 16);
    const unsigned rlo = elo ^ 0xC2B2AE35u, rhi = ehi ^ 0x27D4EB2Fu;
    if (lane == 0) { L[L_TAG + 1] = cv; L[L_TAG + 2] = qlo; L[L_TAG + 3] = qhi; L[L_TAG + 4] = elo; L[L_TAG + 5] = ehi; }
    LGKM0();
    if (lane == 0) L[L_TRDY] = 1u;
    STAMP(1);
    {
      const int oq = QG_OFF + g * 2048 + lane * 16;
      v4u q0 = {0u, ~qhi, 0u, 0u}, q1 = {0u, ~qhi, 0u, 0u};
#pragma nounroll
      for (unsigned sp = 0; sp < SPIN_CAP; ++sp) {
        CLOBBER();
        const bool ok0 = q0.y == qhi && q0.w == qhi && (q0.x >> 16) == qlo && (q0.z >> 16) == qlo;
        const bool ok1 = q1.y == qhi && q1.w == qhi && (q1.x >> 16) == qlo && (q1.z >> 16) == qlo;
        if (__all(ok0 && ok1)) break;
        const v4u n0 = __builtin_amdgcn_raw_buffer_load_b128(rws, ok0 ? OOB : oq, 0, AUX_SC1);
        const v4u n1 = __builtin_amdgcn_raw_buffer_load_b128(rws, ok1 ? OOB : oq + 1024, 0, AUX_SC1);
        q0 = ok0 ? q0 : n0; q1 = ok1 ? q1 : n1;
      }
      L[L_Q + lane] = (q0.x & 0xffffu) | (q0.z << 16);
      L[L_Q + 64 + lane] = (q1.x & 0xffffu) | (q1.z << 16);
      LGKM0();
      if (lane == 0) L[L_QRDY] = 1u;
    }
    STAMP(2);
    {
      const v4u zero = {0u, 0u, 0u, 0u};
      __builtin_amdgcn_raw_buffer_store_b128(zero, rws, p1_off, 0, AUX_SC1);
      __builtin_amdgcn_raw_buffer_store_b128(zero, rws, p1_off, P1_H1, AUX_SC1);
#pragma unroll
      for (int i = 0; i < 5; ++i) {
        const int e = i * 8 + (lane >> 3);
        __builtin_amdgcn_raw_buffer_store_b128(zero, rws, e < NFC ? P2_OFF + (e * NWG + b) * 128 + (lane & 7) * 16 : OOB, 0, AUX_SC1);
      }
    }
    lds_wait<8>(L, L_CNTD, 3u);
    DRAIN();
    if (lane == 0) __builtin_amdgcn_raw_buffer_store_b64(v2u{rlo, rhi}, rws, RDY_OFF + b * 8, 0, AUX_SC1);
    STAMP(3);
#pragma nounroll
    for (unsigned sp = 0; sp < SPIN_CAP; ++sp) {
      __builtin_amdgcn_s_sleep(100);
      CLOBBER();
      const v4u f0 = __builtin_amdgcn_raw_buffer_load_b128(rws, RDY_OFF + lane * 16, 0, AUX_SC1);
      const v4u f1 = __builtin_amdgcn_raw_buffer_load_b128(rws, RDY_OFF + 1024 + lane * 16, 0, AUX_SC1);
      const bool ok = f0.x == rlo && f0.y == rhi && f0.z == rlo && f0.w == rhi && f1.x == rlo && f1.y == rhi && f1.z == rlo && f1.w == rhi;
      if (__all(ok)) break;
    }
    CLOBBER();
    if (lane == 0) L[L_GO] = 1u;
    STAMP(4);
    if (b < NFC) {
      const int of = FP_OFF + (lane >> 3) * (D * 4) + fl * 128 + (lane & 7) * 16 + fh * 8;
      v2u f[18];
#pragma unroll
      for (int i = 0; i < 18; ++i) f[i] = __builtin_amdgcn_raw_buffer_load_b64(rws, of, i * 8 * D * 4, AUX_SC1);
      const unsigned xv = *(const unsigned*)(a.x + 32 * fl + 4 * (lane & 7) + 2 * fh);
      float y0 = 0.f, y1 = 0.f;
#pragma unroll
      for (int i = 0; i < 18; ++i) { y0 += asf(f[i].x); y1 += asf(f[i].y); }
      y0 += dpp<ROR8>(y0); y1 += dpp<ROR8>(y1);
      y0 = xrow_sum(y0); y1 = xrow_sum(y1);
      if (lane < 8) *(LAS f32x4*)(LF + L_G + 4 * lane) = f32x4{rbf(silu_f(rbf(y0))), rbf(silu_f(rbf(y1))), bflo(xv), bfhi(xv)};
      LGKM0();
      if (lane == 0) L[L_FFR] = 1u;
    }
    STAMP(5);
    lds_wait<4>(L, L_SDONE, 1u);
    STAMP(6);
    {
      v4u p[2];
      poll_lines<2>(p, rws, P1_OFF + (g * 16 + j) * 2048 + lane * 16);
      float M = fmaxf(asf(~p[0].w), asf(~p[1].w));
      M = fmaxf(M, dpp<ROR8>(M)); M = xrow_max(M);
      float Ls = 0.f, o0 = 0.f, o1 = 0.f;
#pragma unroll
      for (int i = 0; i < 2; ++i) {
        const float wt = __builtin_amdgcn_exp2f(asf(~p[i].w) - M);
        Ls = fmaf(wt, asf(p[i].y), Ls); o0 = fmaf(wt, asf(p[i].x), o0); o1 = fmaf(wt, asf(p[i].z), o1);
      }
      Ls += dpp<ROR8>(Ls); o0 += dpp<ROR8>(o0); o1 += dpp<ROR8>(o1);
      Ls = xrow_sum(Ls); o0 = xrow_sum(o0); o1 = xrow_sum(o1);
      const unsigned opk = cvtpk(o0 / Ls, o1 / Ls);
      if (lane < 8) L[L_O + lane] = opk;
      LGKM0();
      if (lane == 0) L[L_ORDY] = 1u;
    }
    STAMP(7);
  }
  if (w == 0 && b == 0) { while (__builtin_amdgcn_s_memrealtime() - tq0 < 12000) __builtin_amdgcn_s_sleep(1); }
}

}

extern "C" void kernel_launch(void* const* d_in, const int* in_sizes, int n_in, void* d_out, int out_size, void* d_ws, size_t ws_size,
                              hipStream_t stream) {
  Args a;
  a.x = (const unsigned short*)d_in[0];
  a.k = (const unsigned char*)d_in[1];
  a.v = (const unsigned char*)d_in[2];
  a.wq = (const unsigned char*)d_in[3];
  a.wo = (const unsigned char*)d_in[4];
  a.wg = (const unsigned char*)d_in[6];
  a.wu = (const unsigned char*)d_in[7];
  a.wd = (const unsigned char*)d_in[8];
  a.out = (unsigned short*)d_out;
  a.ws = (unsigned char*)d_ws;
  hipLaunchKernelGGL(fused_decode_kernel, dim3(NWG), dim3(NTHR), 0, stream, a);
}
```

```cpp
#include <hip/hip_runtime.h>
#include <hip/hip_bf16.h>
#include <stdint.h>

namespace {

typedef unsigned v4u __attribute__((ext_vector_type(4)));
typedef unsigned v2u __attribute__((ext_vector_type(2)));
typedef float f32x4 __attribute__((ext_vector_type(4)));
typedef float f32x2 __attribute__((ext_vector_type(2)));
typedef __bf16 bf16x8 __attribute__((ext_vector_type(8)));
typedef __bf16 bf16x2 __attribute__((ext_vector_type(2)));
typedef unsigned long long u64;
#define LAS __attribute__((address_space(3)))

#define NSU 9
#define EP1 640
#define EP2 EP1
#define WO_WHEN 0
#define FFN_WHEN 0
#define DBG_NC 0
#define IDLE_SLEEP 8
#define PACE_W 8
#define DBG_NOW 0
#define AUXQ 0
#define AUXG 0
#define AUXD 0

constexpr int D = 576, SK = 65536, DH = 128, FF = 4608, QN = 4096;
constexpr int NWG = 256, NTHR = 512;
constexpr int NU = SK / 64;
constexpr int DYN0 = NSU * 64;
constexpr unsigned TK_E1 = EP1 - DYN0, TK_E2 = EP2 - DYN0, TK_END = TK_E2 + 2 * (NU - EP2);
constexpr int NBMAX = 32;
static_assert(EP1 >= DYN0 && EP2 >= EP1 && EP2 <= NU && NSU < NBMAX && NSU >= 3, "");
constexpr unsigned HEAD_BYTES = (unsigned)SK * DH * 2;
constexpr int NCG = D / 4;
constexpr int NFC = 36;
constexpr int NFT = FF / 32;
constexpr int OOB = 0x7f000000;

constexpr int E_OFF = 0;
constexpr int C_OFF = 256;
constexpr int RDY_OFF = 8192;
constexpr int RDF_OFF = 12288;
constexpr int QG_OFF = 16384;
constexpr int P1_OFF = 65536;
constexpr int P2_OFF = P1_OFF + 16 * 16 * 16 * 128;
constexpr int FP_OFF = P2_OFF + NFC * NWG * 128;
constexpr int WS_BYTES = FP_OFF + NFT * D * 4;
constexpr int ST_OFF = 16 << 20;
static_assert(WS_BYTES <= ST_OFF, "");

constexpr int AUX_NT = 2, AUX_SC1 = 16;
constexpr unsigned RSRC3 = 0x00020000u;
constexpr unsigned SPIN_CAP = 1u << 17;

constexpr int L_QRDY = 0, L_TRDY = 1, L_CNTA = 2, L_CNTQ = 3, L_CNTS = 4, L_CNTF = 5, L_GO = 6, L_FFR = 7, L_SDONE = 8, L_YD = 9, L_QPUB = 10, L_ORDY = 11, L_CNTD = 12;
constexpr int L_NCTL = 16;
constexpr int L_TAG = 16;
constexpr int L_Q = 32;
constexpr int L_RED = 192;
constexpr int L_O = 384;
constexpr int L_REDQ = 400;
constexpr int L_G = 448;
constexpr int L_FIN = 512;
constexpr int L_XN = 576;
constexpr int L_ML = 1152;
constexpr int L_ACC = 1280;
constexpr int L_DUMMY = 3072;
constexpr int L_SC = 4096;
constexpr int L_TOTAL = 24 * 1024;

constexpr float SCALE = 0.08838834764831845f;
constexpr float LOG2E = 1.4426950408889634f;

__device__ __forceinline__ unsigned cvtpk(float lo, float hi) {
  f32x2 v = {lo, hi};
  return __builtin_bit_cast(unsigned, __builtin_convertvector(v, bf16x2));
}
__device__ __forceinline__ float bflo(unsigned u) { return __builtin_bit_cast(float, u << 16); }
__device__ __forceinline__ float bfhi(unsigned u) { return __builtin_bit_cast(float, u & 0xffff0000u); }
__device__ __forceinline__ float rbf(float x) { return bflo(cvtpk(x, x)); }
__device__ __forceinline__ float asf(unsigned u) { return __builtin_bit_cast(float, u); }
__device__ __forceinline__ unsigned asu(float f) { return __builtin_bit_cast(unsigned, f); }

template <int CTRL> __device__ __forceinline__ float dpp(float x) {
  return __builtin_bit_cast(float, __builtin_amdgcn_mov_dpp(__builtin_bit_cast(int, x), CTRL, 0xf, 0xf, true));
}
constexpr int XOR1 = 0xB1, XOR2 = 0x4E, HMIR = 0x141, ROR8 = 0x128, ROR4 = 0x124, SHL4 = 0x104, SHR4 = 0x114, QB0 = 0x00, QB2 = 0xAA;

__device__ __forceinline__ float xrow_max(float x) {
  auto s = __builtin_amdgcn_permlane16_swap(asu(x), asu(x), false, false);
  x = fmaxf(asf(s[0]), asf(s[1]));
  auto t = __builtin_amdgcn_permlane32_swap(asu(x), asu(x), false, false);
  return fmaxf(asf(t[0]), asf(t[1]));
}
__device__ __forceinline__ float xrow_sum(float x) {
  auto s = __builtin_amdgcn_permlane16_swap(asu(x), asu(x), false, false);
  x = asf(s[0]) + asf(s[1]);
  auto t = __builtin_amdgcn_permlane32_swap(asu(x), asu(x), false, false);
  return asf(t[0]) + asf(t[1]);
}
__device__ __forceinline__ float wave_sum(float x) {
  x += dpp<XOR1>(x); x += dpp<XOR2>(x); x += dpp<HMIR>(x); x += dpp<ROR8>(x);
  return xrow_sum(x);
}
__device__ __forceinline__ float swap32_add(float a, float b) {
  auto s = __builtin_amdgcn_permlane32_swap(asu(a), asu(b), false, false);
  return asf(s[0]) + asf(s[1]);
}
__device__ __forceinline__ float swap16_add(float a, float b) {
  auto s = __builtin_amdgcn_permlane16_swap(asu(a), asu(b), false, false);
  return asf(s[0]) + asf(s[1]);
}
__device__ __forceinline__ f32x4 mfma(v4u a, v4u b, f32x4 c) {
  return __builtin_amdgcn_mfma_f32_16x16x32_bf16(__builtin_bit_cast(bf16x8, a), __builtin_bit_cast(bf16x8, b), c, 0, 0, 0);
}
__device__ __forceinline__ float dot2(unsigned a, unsigned b, float c) {
  return __builtin_amdgcn_fdot2_f32_bf16(__builtin_bit_cast(bf16x2, a), __builtin_bit_cast(bf16x2, b), c, false);
}
__device__ __forceinline__ float silu_f(float x) { return x / (1.0f + __expf(-x)); }

#define WG_BARRIER() do { __builtin_amdgcn_sched_barrier(0); __builtin_amdgcn_s_barrier(); __builtin_amdgcn_sched_barrier(0); } while (0)
#define CLOBBER() asm volatile("" ::: "memory")
#define LGKM0() asm volatile("s_waitcnt lgkmcnt(0)" ::: "memory")
#define DRAIN() asm volatile("s_waitcnt vmcnt(0)" ::: "memory")

__device__ __forceinline__ unsigned lds_ld(volatile LAS unsigned* L, int i) { return __builtin_amdgcn_readfirstlane(L[i]); }
template <int SLEEP> __device__ __forceinline__ void lds_wait(volatile LAS unsigned* L, int i, unsigned want) {
#pragma nounroll
  for (unsigned s = 0; s < (SPIN_CAP << 3); ++s) {
    if (lds_ld(L, i) >= want) break;
    __builtin_amdgcn_s_sleep(SLEEP);
  }
  CLOBBER();
}
__device__ __forceinline__ unsigned lds_inc(volatile LAS unsigned* L, int i, int lane) {
  unsigned r = 0;
  if (lane == 0) r = __hip_atomic_fetch_add((LAS unsigned*)(L + i), 1u, __ATOMIC_RELAXED, __HIP_MEMORY_SCOPE_WORKGROUP);
  r = __builtin_amdgcn_readfirstlane(r);
  CLOBBER();
  return r;
}

struct Args {
  const unsigned short* x;
  const unsigned char* k;
  const unsigned char* v;
  const unsigned char* wq;
  const unsigned char* wo;
  const unsigned char* wg;
  const unsigned char* wu;
  const unsigned char* wd;
  unsigned short* out;
  unsigned char* ws;
};

#define STAMP(i) do { } while (0)

struct Next { __amdgpu_buffer_rsrc_t r0, r1; int vo0[4], vo1[4]; unsigned off0, off1; };
#define REFILL(i) ((i) < 8 ? ldkv(nx.r0, nx.vo0[(i) & 3], nx.off0 + ((i) >> 2) * 4096) : ldkv(nx.r1, nx.vo1[(i) & 3], nx.off1 + ((i) >> 2) * 4096))

#define THROTTLE()
struct SState {
  f32x4 acc;
  float m0, m1, l0, l1;
  v4u fa, fb;
};

template <int QD> __device__ __forceinline__ void build_frag(v4u& f, unsigned pb) {
  constexpr int r0 = (0 - QD) & 3, r1 = (1 - QD) & 3, r2 = (2 - QD) & 3, r3 = (3 - QD) & 3;
  f.x = __builtin_amdgcn_update_dpp(f.x, pb, r0 ? 0x120 + 4 * r0 : 0xE4, 0xf, 0x1, false);
  f.y = __builtin_amdgcn_update_dpp(f.y, pb, r1 ? 0x120 + 4 * r1 : 0xE4, 0xf, 0x2, false);
  f.z = __builtin_amdgcn_update_dpp(f.z, pb, r2 ? 0x120 + 4 * r2 : 0xE4, 0xf, 0x4, false);
  f.w = __builtin_amdgcn_update_dpp(f.w, pb, r3 ? 0x120 + 4 * r3 : 0xE4, 0xf, 0x8, false);
}

__device__ __forceinline__ v4u ldkv(__amdgpu_buffer_rsrc_t r, int voff, unsigned soff) {
  return __builtin_amdgcn_raw_buffer_load_b128(r, voff, soff, AUX_NT);
}

template <int NG>
__device__ __forceinline__ unsigned k_unit(v4u (&t)[16], const v4u (&qf)[4], const Next& nx, int lane) {
  float x[2 * NG][2];
#pragma unroll
  for (int u = 0; u < 2 * NG; ++u) {
    float a0 = 0.f, a1 = 0.f;
#pragma unroll
    for (int hf = 0; hf < 2; ++hf) {
#pragma unroll
      for (int e = 0; e < 4; ++e) { if (DBG_NC & 1) a0 = asf((asu(a0) ^ t[2 * u + hf][e]) & 0x3fffffffu); else { a0 = dot2(t[2 * u + hf][e], qf[hf][e], a0); a1 = dot2(t[2 * u + hf][e], qf[2 + hf][e], a1); } }
      asm volatile("" : "+v"(a0), "+v"(a1));
      THROTTLE();
      t[2 * u + hf] = REFILL(2 * u + hf);
      __builtin_amdgcn_sched_barrier(0);
    }
    x[u][0] = a0; x[u][1] = a1;
  }
  float sc[2];
#pragma unroll
  for (int h = 0; h < 2; ++h) {
    float y[NG], z[2];
#pragma unroll
    for (int v = 0; v < NG; ++v) {
      const float t1 = x[2 * v][h] + dpp<HMIR>(x[2 * v][h]), t2 = x[2 * v + 1][h] + dpp<HMIR>(x[2 * v + 1][h]);
      y[v] = asf(__builtin_amdgcn_update_dpp(asu(t1), asu(t2), 0xE4, 0xf, 0xA, false));
    }
#pragma unroll
    for (int v = 0; v < 2; ++v) {
      const float t1 = y[v] + dpp<XOR2>(y[v]);
      if (NG == 4) { const float t2 = y[NG - 2 + v] + dpp<XOR2>(y[NG - 2 + v]); z[v] = (lane & 2) ? t2 : t1; }
      else z[v] = t1;
    }
    const float t1 = z[0] + dpp<XOR1>(z[0]), t2 = z[1] + dpp<XOR1>(z[1]);
    sc[h] = (lane & 1) ? t2 : t1;
  }
  unsigned pk = cvtpk(sc[0], sc[1]);
  pk = cvtpk(bflo(pk) * SCALE, bfhi(pk) * SCALE);
  return (NG == 2 && (lane & 2)) ? 0xff80ff80u : pk;
}

__device__ __forceinline__ float wave_max(float x) {
  x = fmaxf(x, dpp<XOR1>(x)); x = fmaxf(x, dpp<XOR2>(x)); x = fmaxf(x, dpp<HMIR>(x)); x = fmaxf(x, dpp<ROR8>(x));
  return xrow_max(x);
}

template <int NG>
__device__ __forceinline__ void v_unit(v4u (&t)[16], SState& s, unsigned pk, const Next& nx, unsigned psel) {
  const float s0 = bflo(pk) * LOG2E, s1 = bfhi(pk) * LOG2E;
  if (!__all(s0 <= s.m0 && s1 <= s.m1)) {
    const float n0 = fmaxf(s.m0, ceilf(wave_max(s0))), n1 = fmaxf(s.m1, ceilf(wave_max(s1)));
    const float a0 = __builtin_amdgcn_exp2f(s.m0 - n0), a1 = __builtin_amdgcn_exp2f(s.m1 - n1);
    s.acc.x *= a0; s.acc.y *= a0; s.acc.z *= a1; s.acc.w *= a1;
    s.l0 *= a0; s.l1 *= a1;
    s.m0 = n0; s.m1 = n1;
  }
  const float e0 = __builtin_amdgcn_exp2f(s0 - s.m0), e1 = __builtin_amdgcn_exp2f(s1 - s.m1);
  s.l0 += e0; s.l1 += e1;
  const float pe = asf(cvtpk(e0, e1));
#define PV_STEP(IDX, F, QD) \
  if (!(DBG_NC & 2) || (IDX) == 15) build_frag<QD>(F, pb); \
  s.acc = mfma(F, t[IDX], s.acc); \
  THROTTLE(); \
  t[IDX] = REFILL(IDX); \
  __builtin_amdgcn_sched_barrier(0);
#define PV_GROUP(V) { \
    const unsigned pb = __builtin_amdgcn_perm(0u, asu(dpp<(V) * 0x55>(pe)), psel);       \
    constexpr int B0 = (16 - 4 * NG + 4 * (V)) & 15; \
    PV_STEP(B0, s.fa, 0) PV_STEP(B0 + 1, s.fb, 1) PV_STEP(B0 + 2, s.fa, 2) PV_STEP(B0 + 3, s.fb, 3) }
  PV_GROUP(0) PV_GROUP(1)
  if (NG == 4) { PV_GROUP(2) PV_GROUP(3) }
#undef PV_GROUP
#undef PV_STEP
}

template <int N, bool TAGGED> __device__ __forceinline__ void poll_lines(v4u (&u)[N], __amdgpu_buffer_rsrc_t rws, int off, unsigned tag) {
#pragma unroll
  for (int i = 0; i < N; ++i) u[i] = v4u{0u, 0u, 0u, 0u};
#pragma nounroll
  for (unsigned sp = 0; sp < SPIN_CAP; ++sp) {
    CLOBBER();
    bool ok[N], all = true;
#pragma unroll
    for (int i = 0; i < N; ++i) { ok[i] = TAGGED ? (u[i].y == tag && u[i].w == tag) : (u[i].y != 0u && u[i].w != 0u); all = all && ok[i]; }
    if (__all(all)) break;
    v4u nw[N];
#pragma unroll
    for (int i = 0; i < N; ++i) nw[i] = __builtin_amdgcn_raw_buffer_load_b128(rws, ok[i] ? OOB : off, i * 1024, AUX_SC1);
#pragma unroll
    for (int i = 0; i < N; ++i) u[i] = ok[i] ? u[i] : nw[i];
  }
}

__global__ void __launch_bounds__(NTHR, 2) fused_decode_kernel(Args a) {
  __shared__ __attribute__((aligned(16))) unsigned lds_raw[L_TOTAL];
  volatile LAS unsigned* L = (volatile LAS unsigned*)lds_raw;
  LAS float* LF = (LAS float*)lds_raw;
  const int tid = threadIdx.x, lane = tid & 63, w = __builtin_amdgcn_readfirstlane(tid >> 6);
  const int b = blockIdx.x, g = b >> 4, j = b & 15;
  const __amdgpu_buffer_rsrc_t rws = __builtin_amdgcn_make_buffer_rsrc((void*)a.ws, 0, WS_BYTES, RSRC3);
  const int r4 = lane >> 4, c16 = lane & 15;
  const int p1_off = P1_OFF + ((g * 16 + (c16 >> 1)) * 16 + j) * 128 + (8 * (c16 & 1) + 2 * r4) * 8;
  constexpr int P1_H1 = 8 * 16 * 128;
  const int slot = ((j & 7) - (g >> 1)) & 7, hi = j >> 3;
  const bool is_ffn = slot < 4 || (slot == 4 && hi == 0);
  const int ft = 18 * (g >> 1) + (slot < 4 ? (g & 1) * 8 + 2 * slot + hi : 16 + (g & 1));
  const int fh = b >= 18 ? 1 : 0, fl = b - 18 * fh;
  STAMP(0);

  if (w >= 4) {
    const int sw = w - 4;
    const unsigned char* kb = a.k + (size_t)g * HEAD_BYTES;
    const unsigned char* vb = a.v + (size_t)g * HEAD_BYTES;
    const __amdgpu_buffer_rsrc_t rc = __builtin_amdgcn_make_buffer_rsrc((void*)(a.ws + C_OFF + g * 256), 0, 4, RSRC3);
    const int voffk = (8 * (c16 >> 3) + r4) * 256 + (lane & 7) * 16, voffv = lane * 16;
    const unsigned sb = (unsigned)(j * NSU * 4 + sw) * 16384u;
    Next nx;
    auto set_type = [&](int ty) {
      const bool v0 = ty == 1, v1 = ty != 0;
      nx.r0 = __builtin_amdgcn_make_buffer_rsrc((void*)(v0 ? vb : kb), 0, HEAD_BYTES, RSRC3);
      nx.r1 = __builtin_amdgcn_make_buffer_rsrc((void*)(v1 ? vb : kb), 0, HEAD_BYTES, RSRC3);
      nx.vo0[0] = v0 ? voffv : voffk; nx.vo0[1] = v0 ? voffv + 1024 : voffk + 128;
      nx.vo0[2] = v0 ? voffv + 2048 : voffk + 1024; nx.vo0[3] = v0 ? voffv + 3072 : voffk + 1152;
      nx.vo1[0] = v1 ? voffv : voffk; nx.vo1[1] = v1 ? voffv + 1024 : voffk + 128;
      nx.vo1[2] = v1 ? voffv + 2048 : voffk + 1024; nx.vo1[3] = v1 ? voffv + 3072 : voffk + 1152;
    };
    set_type(0);
    nx.off0 = nx.off1 = sb;
    v4u t[16];
    WG_BARRIER();
    {
      unsigned qr = 0;
#pragma unroll
      for (int i = 0; i < 16; ++i) {
        if (i >= PACE_W && !qr) { asm volatile("s_waitcnt vmcnt(%0)" ::"n"(PACE_W - 1) : "memory"); qr = lds_ld(L, L_QRDY); }
        t[i] = REFILL(i);
        __builtin_amdgcn_sched_barrier(0);
      }
    }
    lds_wait<1>(L, L_QRDY, 1u);
    STAMP(1);
    v4u qf[4];
#pragma unroll
    for (int i = 0; i < 4; ++i) qf[i] = *(LAS v4u*)(lds_raw + L_Q + 32 * i + 4 * (lane & 7));
    unsigned T32 = lds_ld(L, L_TAG), c0 = lds_ld(L, L_TAG + 1);
    volatile LAS unsigned* LS = L + L_SC + sw * (NBMAX * 64) + lane;
    const unsigned psel = (lane & 3) == 0 ? 0x0c0c0100u : (lane & 3) == 1 ? 0x01000c0cu : (lane & 3) == 2 ? 0x0c0c0302u : 0x03020c0cu;
    SState s;
    s.acc = f32x4{0.f, 0.f, 0.f, 0.f};
    s.m0 = -INFINITY; s.m1 = -INFINITY; s.l0 = 0.f; s.l1 = 0.f;
    s.fa = v4u{0u, 0u, 0u, 0u}; s.fb = v4u{0u, 0u, 0u, 0u};
    {
      unsigned bl = lane == 0 ? sb : sb + 65536u;
      int nb = 2, vi = 0, ks = 2, ep = 0;
      bool flush = false, have = false;
      unsigned idx = 0;
      int cur_t = 0, nxt_t = 0, cur_slot = 0, nxt_slot = 1;
      nx.off0 = nx.off1 = sb + 65536u;
      bool pick, ask;
      unsigned tk;
      auto step_begin = [&]() {
        pick = flush ? vi >= nb : nb < NBMAX;
        ask = pick && !have && ks >= NSU;
        if (ask && ep == 0 && nb == NSU) { STAMP(7); lds_wait<1>(L, L_GO, 1u); T32 = lds_ld(L, L_TAG); c0 = lds_ld(L, L_TAG + 1); }
        tk = __builtin_amdgcn_raw_ptr_buffer_atomic_add_i32(1, rc, lane * 4, ask ? 0 : 4, 0);
        nx.off0 = __builtin_amdgcn_readfirstlane(nx.off0); nx.off1 = __builtin_amdgcn_readfirstlane(nx.off1);
      };
      auto step_end = [&]() {
        cur_t = nxt_t; cur_slot = nxt_slot;
        if (ask) { idx = __builtin_amdgcn_readfirstlane(tk) - c0; have = true; }
        if (flush && vi >= nb) { flush = false; nb = 0; vi = 0; STAMP(2); }
        if (!flush) {
          const bool stat = ks < NSU;
          const int e = !pick ? 3 : stat ? 0 : idx >= TK_END ? 3 : idx >= TK_E2 ? 2 : idx >= TK_E1 ? 1 : 0;
          if (nb > 0 && e > ep) flush = true;
          else if (e == 3) { nx.off0 = nx.off1 = OOB; }
          else if (e == 2) {
            if (nxt_t != 2) set_type(2);
            nx.off0 = (unsigned)EP2 * 16384u + (idx - TK_E2) * 8192u; nx.off1 = nx.off0 - 8192u;
            nxt_t = 2; have = false;
          } else {
            const unsigned off = stat ? sb + (unsigned)ks * 65536u : (DYN0 + idx) * 16384u;
            if (stat) ++ks; else have = false;
            ep = e;
            bl = lane == nb ? off : bl;
            if (nxt_t != 0) set_type(0);
            nx.off0 = nx.off1 = off;
            nxt_t = 0; nxt_slot = nb++;
          }
        }
        if (flush) {
          if (nxt_t != 1) set_type(1);
          nx.off0 = nx.off1 = __builtin_amdgcn_readlane(bl, vi);
          nxt_t = 1; nxt_slot = vi++;
        }
      };
      bool done = false;
#pragma nounroll
      while (cur_t == 0) {
#pragma nounroll
        do {
          step_begin();
          LS[cur_slot * 64] = k_unit<4>(t, qf, nx, lane);
          step_end();
        } while (cur_t == 0);
#pragma nounroll
        do {
          step_begin();
          v_unit<4>(t, s, LS[cur_slot * 64], nx, psel);
          done = nx.off0 >= HEAD_BYTES;
          if (done) break;
          step_end();
        } while (cur_t == 1);
      }
#pragma nounroll
      while (!done) {
        step_begin();
        const unsigned pk = k_unit<2>(t, qf, nx, lane);
        v_unit<2>(t, s, pk, nx, psel);
        done = nx.off0 >= HEAD_BYTES;
        if (done) break;
        step_end();
      }
    }
    STAMP(3);
    {
      const float lw0 = wave_sum(s.l0), lw1 = wave_sum(s.l1);
      *(LAS f32x4*)(LF + L_ACC + (sw * 64 + lane) * 4) = s.acc;
      if (lane == 0) { *(LAS f32x4*)(LF + L_ML + sw * 4) = f32x4{s.m0, lw0, s.m1, lw1}; L[L_SDONE] = 1u; }
      CLOBBER();
      const unsigned arr = __hip_atomic_fetch_add((LAS unsigned*)(lds_raw + (lane == 0 ? L_CNTS : L_DUMMY + lane)), 1u, __ATOMIC_RELAXED, __HIP_MEMORY_SCOPE_WORKGROUP);
      CLOBBER();
      f32x4 ml[4], ac[4];
#pragma unroll
      for (int i = 0; i < 4; ++i) { ml[i] = *(volatile LAS f32x4*)(LF + L_ML + i * 4); ac[i] = *(volatile LAS f32x4*)(LF + L_ACC + (i * 64 + lane) * 4); }
      if (__builtin_amdgcn_readfirstlane(arr) == 3u) {
        const float M0 = fmaxf(fmaxf(ml[0].x, ml[1].x), fmaxf(ml[2].x, ml[3].x)), M1 = fmaxf(fmaxf(ml[0].z, ml[1].z), fmaxf(ml[2].z, ml[3].z));
        float L0 = 0.f, L1 = 0.f;
        f32x4 o = {0.f, 0.f, 0.f, 0.f};
#pragma unroll
        for (int i = 0; i < 4; ++i) {
          const float w0 = __builtin_amdgcn_exp2f(ml[i].x - M0), w1 = __builtin_amdgcn_exp2f(ml[i].z - M1);
          L0 = fmaf(w0, ml[i].y, L0); L1 = fmaf(w1, ml[i].w, L1);
          o.x = fmaf(w0, ac[i].x, o.x); o.y = fmaf(w0, ac[i].y, o.y); o.z = fmaf(w1, ac[i].z, o.z); o.w = fmaf(w1, ac[i].w, o.w);
        }
        __builtin_amdgcn_raw_buffer_store_b128(v4u{asu(o.x), asu(L0), asu(o.y), ~asu(M0)}, rws, p1_off, 0, AUX_SC1);
        __builtin_amdgcn_raw_buffer_store_b128(v4u{asu(o.z), asu(L1), asu(o.w), ~asu(M1)}, rws, p1_off, P1_H1, AUX_SC1);
      }
    }
    STAMP(4);
    if (b < NFC) {
      lds_wait<1>(L, L_FFR, 1u);
      const f32x4 gx = *(LAS f32x4*)(LF + L_G + 4 * (lane & 7));
      lds_wait<1>(L, L_YD, 3u);
      v4u u[8];
      poll_lines<8, true>(u, rws, P2_OFF + (b * NWG + 64 * sw) * 128 + lane * 16, T32);
      STAMP(5);
      float y0 = 0.f, y1 = 0.f;
#pragma unroll
      for (int i = 0; i < 8; ++i) { y0 += asf(u[i].x); y1 += asf(u[i].z); }
      y0 += dpp<ROR8>(y0); y1 += dpp<ROR8>(y1);
      y0 = xrow_sum(y0); y1 = xrow_sum(y1);
      if (lane < 8) *(LAS f32x2*)(LF + L_FIN + sw * 16 + 2 * lane) = f32x2{y0, y1};
      CLOBBER();
      const unsigned arr = __hip_atomic_fetch_add((LAS unsigned*)(lds_raw + (lane == 0 ? L_CNTF : L_DUMMY + lane)), 1u, __ATOMIC_RELAXED, __HIP_MEMORY_SCOPE_WORKGROUP);
      CLOBBER();
      const int c = lane & 7;
      const f32x2 f0 = *(volatile LAS f32x2*)(LF + L_FIN + 2 * c), f1 = *(volatile LAS f32x2*)(LF + L_FIN + 16 + 2 * c),
                  f2 = *(volatile LAS f32x2*)(LF + L_FIN + 32 + 2 * c), f3 = *(volatile LAS f32x2*)(LF + L_FIN + 48 + 2 * c);
      if (__builtin_amdgcn_readfirstlane(arr) == 3u) {
        const float sv0 = rbf((f0.x + f1.x) + (f2.x + f3.x)), sv1 = rbf((f0.y + f1.y) + (f2.y + f3.y));
        const unsigned pk = cvtpk(gx.z + rbf(sv0 * gx.x), gx.w + rbf(sv1 * gx.y));
        if (lane < 8) *(unsigned*)(a.out + 32 * fl + 4 * lane + 2 * fh) = pk;
        if (b == 0 && lane == 0) {
          const u64 e = (((u64)L[L_TAG + 5] << 32) | L[L_TAG + 4]) * 6364136223846793005ull + 1442695040888963407ull;
          __builtin_amdgcn_raw_buffer_store_b64(v2u{(unsigned)e, (unsigned)(e >> 32)}, rws, E_OFF, 0, AUX_SC1);
        }
        STAMP(6);
      }
    }
  } else if (w >= 1) {
    const int ta = w - 1, tt = tid - 64;
    __builtin_amdgcn_s_setprio(3);
    const int qs = ((b & 7) * 8 + (b >> 5)) * 4 + ((b >> 3) & 3);
    const __amdgpu_buffer_rsrc_t rx = __builtin_amdgcn_make_buffer_rsrc((void*)a.x, 0, D * 2, RSRC3);
    const __amdgpu_buffer_rsrc_t rq = __builtin_amdgcn_make_buffer_rsrc((void*)(a.wq + qs * 32), 0, DBG_NOW ? 0 : (D - 1) * QN * 2 + 32, RSRC3);
    const int rr = lane >> 2, cc = lane & 3;
    v4u wqr[6];
    const v4u xc0 = __builtin_amdgcn_raw_buffer_load_b128(rx, lane * 16, 0, 0);
    const v4u xc1 = __builtin_amdgcn_raw_buffer_load_b128(rx, 1024 + lane * 16, 0, 0);
    unsigned xo[3];
#pragma unroll
    for (int i = 0; i < 3; ++i) xo[i] = __builtin_amdgcn_raw_buffer_load_b16(rx, (192 * ta + 64 * i + lane) * 2, 0, 0);
    {
      const int vo = (192 * ta + (lane >> 1)) * (QN * 2) + (lane & 1) * 16;
#pragma unroll
      for (int i = 0; i < 6; ++i) wqr[i] = __builtin_amdgcn_raw_buffer_load_b128(rq, vo, i * 32 * QN * 2, AUXQ);
    }
    WG_BARRIER();
    {
      const float x0 = bflo(__builtin_amdgcn_readfirstlane(xc0.x));
      float s0 = 0.f, s1 = 0.f, q0 = 0.f, q1 = 0.f;
#pragma unroll
      for (int i = 0; i < 4; ++i) {
        const float d0 = bflo(xc0[i]) - x0, d1 = bfhi(xc0[i]) - x0, d2 = bflo(xc1[i]) - x0, d3 = bfhi(xc1[i]) - x0;
        s0 += d0 + d1; s1 += d2 + d3;
        q0 = fmaf(d0, d0, q0); q0 = fmaf(d1, d1, q0); q1 = fmaf(d2, d2, q1); q1 = fmaf(d3, d3, q1);
      }
      const float dm = wave_sum(s0 + (lane < 8 ? s1 : 0.f)) * (1.0f / D);
      const float var = fmaxf(wave_sum(q0 + (lane < 8 ? q1 : 0.f)) * (1.0f / D) - dm * dm, 0.f);
      const float mb = rbf(x0 + dm), den = rbf(sqrtf(rbf(rbf(var) + 1e-5f)));
#pragma unroll
      for (int i = 0; i < 3; ++i) LF[L_XN + 192 * ta + 64 * i + lane] = rbf(rbf(bflo(xo[i]) - mb) / den);
      LGKM0();
    }
    {
      float acc[8];
#pragma unroll
      for (int e = 0; e < 8; ++e) acc[e] = 0.f;
#pragma unroll
      for (int i = 0; i < 6; ++i) {
        const float xn = LF[L_XN + 192 * ta + 32 * i + (lane >> 1)];
#pragma unroll
        for (int e = 0; e < 4; ++e) { acc[2 * e] = fmaf(xn, bflo(wqr[i][e]), acc[2 * e]); acc[2 * e + 1] = fmaf(xn, bfhi(wqr[i][e]), acc[2 * e + 1]); }
      }
      float u[4], v2[2];
#pragma unroll
      for (int e = 0; e < 4; ++e) u[e] = swap32_add(acc[e], acc[4 + e]);
#pragma unroll
      for (int e = 0; e < 2; ++e) v2[e] = swap16_add(u[e], u[2 + e]);
      const float t1 = v2[0] + dpp<ROR8>(v2[0]), t2 = v2[1] + dpp<ROR8>(v2[1]);
      float z = (lane & 8) ? t2 : t1;
      z += dpp<XOR2>(z);
      const float z1 = dpp<SHL4>(z), z2 = dpp<SHR4>(z);
      z += (lane & 4) ? z2 : z1;
      if ((lane & 6) == 0) LF[L_REDQ + ta * 16 + 8 * (lane & 1) + ((lane >> 3) & 7)] = z;
      LGKM0();
      if (lds_inc(L, L_CNTQ, lane) == 2u) {
        const float qv = (LF[L_REDQ + c16] + LF[L_REDQ + 16 + c16]) + LF[L_REDQ + 32 + c16];
        lds_wait<0>(L, L_TRDY, 1u);
        const unsigned tlo = lds_ld(L, L_TAG + 2), thi = lds_ld(L, L_TAG + 3);
        if (lane < 16) __builtin_amdgcn_raw_buffer_store_b64(v2u{(cvtpk(qv, qv) & 0xffffu) | (tlo << 16), thi}, rws, QG_OFF + (qs * 16 + lane) * 8, 0, AUX_SC1);
        CLOBBER();
        if (lane == 0) L[L_QPUB] = 1u;
        STAMP(1);
      }
    }
    lds_wait<1>(L, WO_WHEN == 1 ? L_QRDY : L_QPUB, 1u);
    const int vo2 = tt < NCG ? tt * 8 : OOB;
    const int o2 = tt < NCG ? (tt & 7) * 16 : OOB;
    v2u wr[16];
    {
      const __amdgpu_buffer_rsrc_t ro = __builtin_amdgcn_make_buffer_rsrc((void*)(a.wo + (size_t)b * 16 * D * 2), 0, DBG_NOW ? 0 : 16 * D * 2, RSRC3);
#pragma unroll
      for (int k = 0; k < 16; ++k) wr[k] = __builtin_amdgcn_raw_buffer_load_b64(ro, vo2, k * D * 2, AUXD);
      __builtin_amdgcn_sched_barrier(0);
    }
    STAMP(2);
    if (is_ffn) {
      if (FFN_WHEN == 1) lds_wait<1>(L, L_QRDY, 1u);
      if (FFN_WHEN == 2) { lds_wait<1>(L, L_QRDY, 1u); __builtin_amdgcn_s_sleep(127); __builtin_amdgcn_s_sleep(127); }
      STAMP(7);
      const __amdgpu_buffer_rsrc_t ra = __builtin_amdgcn_make_buffer_rsrc((void*)(a.wg + ft * 64), 0, D * FF * 2 - ft * 64, RSRC3);
      const __amdgpu_buffer_rsrc_t rb = __builtin_amdgcn_make_buffer_rsrc((void*)(a.wu + ft * 64), 0, D * FF * 2 - ft * 64, RSRC3);
      const __amdgpu_buffer_rsrc_t rd = __builtin_amdgcn_make_buffer_rsrc((void*)(a.wd + (size_t)ft * 32 * D * 2), 0, 32 * D * 2, RSRC3);
      const int vof = (192 * ta + rr) * (FF * 2) + cc * 16;
      constexpr int fstep = 16 * FF * 2;
      v4u wa[12], wb[12];
      v2u dn[32];
#pragma unroll
      for (int i = 0; i < 12; ++i) {
        wa[i] = __builtin_amdgcn_raw_buffer_load_b128(ra, vof, i * fstep, AUXG);
        wb[i] = __builtin_amdgcn_raw_buffer_load_b128(rb, vof, i * fstep, AUXG);
      }
#pragma unroll
      for (int k = 0; k < 32; ++k) dn[k] = __builtin_amdgcn_raw_buffer_load_b64(rd, vo2, k * D * 2, AUXD);
      __builtin_amdgcn_sched_barrier(0);
      float acc[16];
#pragma unroll
      for (int e = 0; e < 16; ++e) acc[e] = 0.f;
#pragma unroll
      for (int i = 0; i < 12; ++i) {
        const float xn = LF[L_XN + 192 * ta + 16 * i + rr];
#pragma unroll
        for (int e = 0; e < 4; ++e) {
          acc[2 * e] = fmaf(xn, bflo(wa[i][e]), acc[2 * e]); acc[2 * e + 1] = fmaf(xn, bfhi(wa[i][e]), acc[2 * e + 1]);
          acc[8 + 2 * e] = fmaf(xn, bflo(wb[i][e]), acc[8 + 2 * e]); acc[9 + 2 * e] = fmaf(xn, bfhi(wb[i][e]), acc[9 + 2 * e]);
        }
        asm volatile("" : "+v"(acc[0]), "+v"(acc[1]), "+v"(acc[2]), "+v"(acc[3]), "+v"(acc[4]), "+v"(acc[5]), "+v"(acc[6]), "+v"(acc[7]),
                          "+v"(acc[8]), "+v"(acc[9]), "+v"(acc[10]), "+v"(acc[11]), "+v"(acc[12]), "+v"(acc[13]), "+v"(acc[14]), "+v"(acc[15]));
      }
      float u[8], v4[4], z[2];
#pragma unroll
      for (int e = 0; e < 8; ++e) u[e] = swap32_add(acc[e], acc[8 + e]);
#pragma unroll
      for (int e = 0; e < 4; ++e) v4[e] = swap16_add(u[e], u[4 + e]);
#pragma unroll
      for (int e = 0; e < 2; ++e) { const float t1 = v4[e] + dpp<ROR8>(v4[e]), t2 = v4[2 + e] + dpp<ROR8>(v4[2 + e]); z[e] = (lane & 8) ? t2 : t1; }
      const float t1 = z[0] + dpp<SHL4>(z[0]), t2 = z[1] + dpp<SHR4>(z[1]);
      const float zz = (lane & 4) ? t2 : t1;
      LF[L_RED + ta * 64 + (lane & 32) + 8 * cc + ((lane >> 2) & 7)] = zz;
      LGKM0();
      lds_inc(L, L_CNTA, lane);
      lds_wait<0>(L, L_CNTA, 3u);
      const int u5 = lane & 31;
      const float gp = rbf((LF[L_RED + u5] + LF[L_RED + 64 + u5]) + LF[L_RED + 128 + u5]);
      const float up = rbf((LF[L_RED + 32 + u5] + LF[L_RED + 96 + u5]) + LF[L_RED + 160 + u5]);
      const float h = rbf(rbf(silu_f(gp)) * up);
      f32x4 y = {0.f, 0.f, 0.f, 0.f};
#pragma unroll
      for (int k = 0; k < 32; ++k) {
        const float hk = asf(__builtin_amdgcn_readlane(asu(h), k));
        y.x = fmaf(hk, bflo(dn[k].x), y.x); y.y = fmaf(hk, bfhi(dn[k].x), y.y); y.z = fmaf(hk, bflo(dn[k].y), y.z); y.w = fmaf(hk, bfhi(dn[k].y), y.w);
      }
      __builtin_amdgcn_raw_buffer_store_b128(__builtin_bit_cast(v4u, y), rws, tt < NCG ? FP_OFF + ft * (D * 4) + tt * 16 : OOB, 0, AUX_SC1);
      STAMP(6);
    }
    DRAIN();
    lds_inc(L, L_CNTD, lane);
    float wf[64];
#pragma unroll
    for (int k = 0; k < 16; ++k) { wf[4 * k] = bflo(wr[k].x); wf[4 * k + 1] = bfhi(wr[k].x); wf[4 * k + 2] = bflo(wr[k].y); wf[4 * k + 3] = bfhi(wr[k].y); }
#pragma unroll
    for (int k = 0; k < 64; ++k) asm volatile("" : "+v"(wf[k]));
    lds_wait<IDLE_SLEEP>(L, L_SDONE, 1u);
    STAMP(3);
    const unsigned T32 = lds_ld(L, L_TAG);
    unsigned opk = 0;
#pragma nounroll
    for (unsigned sp = 0; sp < (SPIN_CAP << 4); ++sp) {
      const unsigned f = L[L_ORDY];
      opk = L[L_O + (lane & 7)];
      if (__builtin_amdgcn_readfirstlane(f)) break;
    }
    STAMP(4);
    f32x4 y = {0.f, 0.f, 0.f, 0.f};
#pragma unroll
    for (int k = 0; k < 8; ++k) {
      const unsigned sp = __builtin_amdgcn_readlane(opk, k);
      const float e0 = bflo(sp), e1 = bfhi(sp);
      y.x = fmaf(e0, wf[8 * k], y.x); y.y = fmaf(e0, wf[8 * k + 1], y.y); y.z = fmaf(e0, wf[8 * k + 2], y.z); y.w = fmaf(e0, wf[8 * k + 3], y.w);
      y.x = fmaf(e1, wf[8 * k + 4], y.x); y.y = fmaf(e1, wf[8 * k + 5], y.y); y.z = fmaf(e1, wf[8 * k + 6], y.z); y.w = fmaf(e1, wf[8 * k + 7], y.w);
    }
    const int op = P2_OFF + ((tt >> 3) * NWG + b) * 128;
    __builtin_amdgcn_raw_buffer_store_b128(v4u{asu(y.x), T32, asu(y.y), T32}, rws, o2 + op, 0, AUX_SC1);
    __builtin_amdgcn_raw_buffer_store_b128(v4u{asu(y.z), T32, asu(y.w), T32}, rws, o2 + op, 18 * NWG * 128, AUX_SC1);
    lds_inc(L, L_YD, lane);
    STAMP(5);
  } else {
    __builtin_amdgcn_s_setprio(3);
    const v2u ev = __builtin_amdgcn_raw_buffer_load_b64(rws, E_OFF, 0, AUX_SC1);
    const unsigned cv = __builtin_amdgcn_raw_buffer_load_b32(rws, C_OFF + g * 256, 0, AUX_SC1);
    if (lane < L_NCTL) L[lane] = 0u;
    LGKM0();
    WG_BARRIER();
    const unsigned elo = __builtin_amdgcn_readfirstlane(ev.x), ehi = __builtin_amdgcn_readfirstlane(ev.y);
    const unsigned qlo = (elo ^ 0x7F4A7C15u) & 0xffffu, qhi = ((elo ^ 0x7F4A7C15u) >> 16) | ((ehi ^ 0x9E3779B9u) << 16);
    const unsigned rlo = elo ^ 0xC2B2AE35u, rhi = ehi ^ 0x27D4EB2Fu;
    if (lane == 0) { L[L_TAG] = (elo ^ 0x85EBCA6Bu) | 1u; L[L_TAG + 1] = cv; L[L_TAG + 2] = qlo; L[L_TAG + 3] = qhi; L[L_TAG + 4] = elo; L[L_TAG + 5] = ehi; }
    LGKM0();
    if (lane == 0) L[L_TRDY] = 1u;
    STAMP(1);
    {
      const int oq = QG_OFF + g * 2048 + lane * 16;
      v4u q0 = {0u, ~qhi, 0u, 0u}, q1 = {0u, ~qhi, 0u, 0u};
#pragma nounroll
      for (unsigned sp = 0; sp < SPIN_CAP; ++sp) {
        CLOBBER();
        const bool ok0 = q0.y == qhi && q0.w == qhi && (q0.x >> 16) == qlo && (q0.z >> 16) == qlo;
        const bool ok1 = q1.y == qhi && q1.w == qhi && (q1.x >> 16) == qlo && (q1.z >> 16) == qlo;
        if (__all(ok0 && ok1)) break;
        const v4u n0 = __builtin_amdgcn_raw_buffer_load_b128(rws, ok0 ? OOB : oq, 0, AUX_SC1);
        const v4u n1 = __builtin_amdgcn_raw_buffer_load_b128(rws, ok1 ? OOB : oq + 1024, 0, AUX_SC1);
        q0 = ok0 ? q0 : n0; q1 = ok1 ? q1 : n1;
      }
      L[L_Q + lane] = (q0.x & 0xffffu) | (q0.z << 16);
      L[L_Q + 64 + lane] = (q1.x & 0xffffu) | (q1.z << 16);
      LGKM0();
      if (lane == 0) L[L_QRDY] = 1u;
    }
    STAMP(2);
    {
      const v4u zero = {0u, 0u, 0u, 0u};
      __builtin_amdgcn_raw_buffer_store_b128(zero, rws, p1_off, 0, AUX_SC1);
      __builtin_amdgcn_raw_buffer_store_b128(zero, rws, p1_off, P1_H1, AUX_SC1);
#pragma unroll
      for (int i = 0; i < 5; ++i) {
        const int e = i * 8 + (lane >> 3);
        __builtin_amdgcn_raw_buffer_store_b128(zero, rws, e < NFC ? P2_OFF + (e * NWG + b) * 128 + (lane & 7) * 16 : OOB, 0, AUX_SC1);
      }
    }
    DRAIN();
    if (lane == 0) __builtin_amdgcn_raw_buffer_store_b64(v2u{rlo, rhi}, rws, RDY_OFF + b * 8, 0, AUX_SC1);
    STAMP(3);
#pragma nounroll
    for (unsigned sp = 0; sp < SPIN_CAP; ++sp) {
      __builtin_amdgcn_s_sleep(30);
      CLOBBER();
      const v4u f0 = __builtin_amdgcn_raw_buffer_load_b128(rws, RDY_OFF + g * 128 + (lane & 7) * 16, 0, AUX_SC1);
      const bool ok = f0.x == rlo && f0.y == rhi && f0.z == rlo && f0.w == rhi;
      if (__all(ok)) break;
    }
    CLOBBER();
    if (lane == 0) L[L_GO] = 1u;
    STAMP(4);
    if (is_ffn) {
      lds_wait<8>(L, L_CNTD, 3u);
      if (lane == 0) __builtin_amdgcn_raw_buffer_store_b64(v2u{rlo, rhi}, rws, RDF_OFF + ft * 8, 0, AUX_SC1);
    }
    if (b < NFC) {
#pragma nounroll
      for (unsigned sp = 0; sp < SPIN_CAP; ++sp) {
        __builtin_amdgcn_s_sleep(127);
        CLOBBER();
        const v4u f0 = __builtin_amdgcn_raw_buffer_load_b128(rws, RDF_OFF + lane * 16, 0, AUX_SC1);
        const v4u f1 = __builtin_amdgcn_raw_buffer_load_b128(rws, RDF_OFF + 1024 + (lane & 7) * 16, 0, AUX_SC1);
        const bool ok = f0.x == rlo && f0.y == rhi && f0.z == rlo && f0.w == rhi && f1.x == rlo && f1.y == rhi && f1.z == rlo && f1.w == rhi;
        if (__all(ok)) break;
      }
      CLOBBER();
      const int of = FP_OFF + (lane >> 3) * (D * 4) + fl * 128 + (lane & 7) * 16 + fh * 8;
      v2u f[18];
#pragma unroll
      for (int i = 0; i < 18; ++i) f[i] = __builtin_amdgcn_raw_buffer_load_b64(rws, of, i * 8 * D * 4, AUX_SC1);
      const unsigned xv = *(const unsigned*)(a.x + 32 * fl + 4 * (lane & 7) + 2 * fh);
      float y0 = 0.f, y1 = 0.f;
#pragma unroll
      for (int i = 0; i < 18; ++i) { y0 += asf(f[i].x); y1 += asf(f[i].y); }
      y0 += dpp<ROR8>(y0); y1 += dpp<ROR8>(y1);
      y0 = xrow_sum(y0); y1 = xrow_sum(y1);
      if (lane < 8) *(LAS f32x4*)(LF + L_G + 4 * lane) = f32x4{rbf(silu_f(rbf(y0))), rbf(silu_f(rbf(y1))), bflo(xv), bfhi(xv)};
      LGKM0();
      if (lane == 0) L[L_FFR] = 1u;
    }
    STAMP(5);
    lds_wait<IDLE_SLEEP / 2>(L, L_SDONE, 1u);
    lds_wait<1>(L, L_CNTS, 4u);
    STAMP(6);
    {
      v4u p[2];
      poll_lines<2, false>(p, rws, P1_OFF + (g * 16 + j) * 2048 + lane * 16, 0u);
      float M = fmaxf(asf(~p[0].w), asf(~p[1].w));
      M = fmaxf(M, dpp<ROR8>(M)); M = xrow_max(M);
      float Ls = 0.f, o0 = 0.f, o1 = 0.f;
#pragma unroll
      for (int i = 0; i < 2; ++i) {
        const float wt = __builtin_amdgcn_exp2f(asf(~p[i].w) - M);
        Ls = fmaf(wt, asf(p[i].y), Ls); o0 = fmaf(wt, asf(p[i].x), o0); o1 = fmaf(wt, asf(p[i].z), o1);
      }
      Ls += dpp<ROR8>(Ls); o0 += dpp<ROR8>(o0); o1 += dpp<ROR8>(o1);
      Ls = xrow_sum(Ls); o0 = xrow_sum(o0); o1 = xrow_sum(o1);
      const unsigned opk = cvtpk(o0 / Ls, o1 / Ls);
      if (lane < 8) L[L_O + lane] = opk;
      CLOBBER();
      if (lane == 0) L[L_ORDY] = 1u;
    }
    STAMP(7);
  }
}

}

extern "C" void kernel_launch(void* const* d_in, const int* in_sizes, int n_in, void* d_out, int out_size, void* d_ws, size_t ws_size,
                              hipStream_t stream) {
  Args a;
  a.x = (const unsigned short*)d_in[0];
  a.k = (const unsigned char*)d_in[1];
  a.v = (const unsigned char*)d_in[2];
  a.wq = (const unsigned char*)d_in[3];
  a.wo = (const unsigned char*)d_in[4];
  a.wg = (const unsigned char*)d_in[6];
  a.wu = (const unsigned char*)d_in[7];
  a.wd = (const unsigned char*)d_in[8];
  a.out = (unsigned short*)d_out;
  a.ws = (unsigned char*)d_ws;
  hipLaunchKernelGGL(fused_decode_kernel, dim3(NWG), dim3(NTHR), 0, stream, a);
}
```

```cpp
#include <hip/hip_runtime.h>
#include <hip/hip_bf16.h>
#include <stdint.h>

namespace {

typedef unsigned v4u __attribute__((ext_vector_type(4)));
typedef unsigned v2u __attribute__((ext_vector_type(2)));
typedef float f32x4 __attribute__((ext_vector_type(4)));
typedef float f32x2 __attribute__((ext_vector_type(2)));
typedef __bf16 bf16x8 __attribute__((ext_vector_type(8)));
typedef __bf16 bf16x2 __attribute__((ext_vector_type(2)));
typedef unsigned long long u64;
#define LAS __attribute__((address_space(3)))

#define NSU 10
#define PACE_W 16
#define DFW 8
#define AUXQ 0
#define AUXG 0
#define AUXD 0

constexpr int D = 576, SK = 65536, DH = 128, FF = 4608, QN = 4096;
constexpr int NWG = 256, NTHR = 512;
constexpr int NU = SK / 64;
constexpr unsigned NDYN = NU - NSU * 64;
static_assert(NSU >= 4 && NSU < 16 && (64 - 4 * NSU) % DFW == 0, "");
constexpr unsigned HEAD_BYTES = (unsigned)SK * DH * 2;
constexpr int NCG = D / 4;
constexpr int NFC = 36;
constexpr int NFT = FF / 32;
constexpr int OOB = 0x7f000000;

constexpr int E_OFF = 0;
constexpr int C_OFF = 256;
constexpr int RDY_OFF = 8192;
constexpr int RDK_OFF = 10240;
constexpr int RDF_OFF = 12288;
constexpr int QG_OFF = 16384;
constexpr int P1_OFF = 65536;
constexpr int P2_OFF = P1_OFF + 16 * 16 * 16 * 128;
constexpr int FP_OFF = P2_OFF + NFC * NWG * 128;
constexpr int SC_OFF = FP_OFF + NFT * D * 4;
constexpr int WS_BYTES = SC_OFF + 16 * NDYN * 256;
constexpr int ST_OFF = 16 << 20;
static_assert(WS_BYTES <= ST_OFF, "");

constexpr int AUX_NT = 2, AUX_SC1 = 16;
constexpr unsigned RSRC3 = 0x00020000u;
constexpr unsigned SPIN_CAP = 1u << 17;

constexpr int L_QRDY = 0, L_TRDY = 1, L_CNTA = 2, L_CNTQ = 3, L_CNTS = 4, L_CNTF = 5, L_GO = 6, L_FFR = 7, L_SDONE = 8, L_YD = 9, L_QPUB = 10, L_ORDY = 11, L_CNTD = 12, L_CNTK = 13, L_VGO = 14;
constexpr int L_NCTL = 16;
constexpr int L_TAG = 16;
constexpr int L_Q = 32;
constexpr int L_RED = 192;
constexpr int L_O = 384;
constexpr int L_REDQ = 400;
constexpr int L_G = 448;
constexpr int L_FIN = 512;
constexpr int L_XN = 576;
constexpr int L_ML = 1152;
constexpr int L_ACC = 1280;
constexpr int L_DUMMY = 3072;
constexpr int L_SC = 4096;
constexpr int L_TOTAL = 24 * 1024;

constexpr float SCALE = 0.08838834764831845f;
constexpr float LOG2E = 1.4426950408889634f;

__device__ __forceinline__ unsigned cvtpk(float lo, float hi) {
  f32x2 v = {lo, hi};
  return __builtin_bit_cast(unsigned, __builtin_convertvector(v, bf16x2));
}
__device__ __forceinline__ float bflo(unsigned u) { return __builtin_bit_cast(float, u << 16); }
__device__ __forceinline__ float bfhi(unsigned u) { return __builtin_bit_cast(float, u & 0xffff0000u); }
__device__ __forceinline__ float rbf(float x) { return bflo(cvtpk(x, x)); }
__device__ __forceinline__ float asf(unsigned u) { return __builtin_bit_cast(float, u); }
__device__ __forceinline__ unsigned asu(float f) { return __builtin_bit_cast(unsigned, f); }

template <int CTRL> __device__ __forceinline__ float dpp(float x) {
  return __builtin_bit_cast(float, __builtin_amdgcn_mov_dpp(__builtin_bit_cast(int, x), CTRL, 0xf, 0xf, true));
}
constexpr int XOR1 = 0xB1, XOR2 = 0x4E, HMIR = 0x141, ROR8 = 0x128, ROR4 = 0x124, SHL4 = 0x104, SHR4 = 0x114, QB0 = 0x00, QB2 = 0xAA;

__device__ __forceinline__ float xrow_max(float x) {
  auto s = __builtin_amdgcn_permlane16_swap(asu(x), asu(x), false, false);
  x = fmaxf(asf(s[0]), asf(s[1]));
  auto t = __builtin_amdgcn_permlane32_swap(asu(x), asu(x), false, false);
  return fmaxf(asf(t[0]), asf(t[1]));
}
__device__ __forceinline__ float xrow_sum(float x) {
  auto s = __builtin_amdgcn_permlane16_swap(asu(x), asu(x), false, false);
  x = asf(s[0]) + asf(s[1]);
  auto t = __builtin_amdgcn_permlane32_swap(asu(x), asu(x), false, false);
  return asf(t[0]) + asf(t[1]);
}
__device__ __forceinline__ float wave_sum(float x) {
  x += dpp<XOR1>(x); x += dpp<XOR2>(x); x += dpp<HMIR>(x); x += dpp<ROR8>(x);
  return xrow_sum(x);
}
__device__ __forceinline__ float swap32_add(float a, float b) {
  auto s = __builtin_amdgcn_permlane32_swap(asu(a), asu(b), false, false);
  return asf(s[0]) + asf(s[1]);
}
__device__ __forceinline__ float swap16_add(float a, float b) {
  auto s = __builtin_amdgcn_permlane16_swap(asu(a), asu(b), false, false);
  return asf(s[0]) + asf(s[1]);
}
__device__ __forceinline__ f32x4 mfma(v4u a, v4u b, f32x4 c) {
  return __builtin_amdgcn_mfma_f32_16x16x32_bf16(__builtin_bit_cast(bf16x8, a), __builtin_bit_cast(bf16x8, b), c, 0, 0, 0);
}
__device__ __forceinline__ float dot2(unsigned a, unsigned b, float c) {
  return __builtin_amdgcn_fdot2_f32_bf16(__builtin_bit_cast(bf16x2, a), __builtin_bit_cast(bf16x2, b), c, false);
}
__device__ __forceinline__ float silu_f(float x) { return x / (1.0f + __expf(-x)); }

#define WG_BARRIER() do { __builtin_amdgcn_sched_barrier(0); __builtin_amdgcn_s_barrier(); __builtin_amdgcn_sched_barrier(0); } while (0)
#define CLOBBER() asm volatile("" ::: "memory")
#define LGKM0() asm volatile("s_waitcnt lgkmcnt(0)" ::: "memory")
#define DRAIN() asm volatile("s_waitcnt vmcnt(0)" ::: "memory")

__device__ __forceinline__ unsigned lds_ld(volatile LAS unsigned* L, int i) { return __builtin_amdgcn_readfirstlane(L[i]); }
template <int SLEEP> __device__ __forceinline__ void lds_wait(volatile LAS unsigned* L, int i, unsigned want) {
#pragma nounroll
  for (unsigned s = 0; s < (SPIN_CAP << 3); ++s) {
    if (lds_ld(L, i) >= want) break;
    __builtin_amdgcn_s_sleep(SLEEP);
  }
  CLOBBER();
}
__device__ __forceinline__ unsigned lds_inc(volatile LAS unsigned* L, int i, int lane) {
  unsigned r = 0;
  if (lane == 0) r = __hip_atomic_fetch_add((LAS unsigned*)(L + i), 1u, __ATOMIC_RELAXED, __HIP_MEMORY_SCOPE_WORKGROUP);
  r = __builtin_amdgcn_readfirstlane(r);
  CLOBBER();
  return r;
}

struct Args {
  const unsigned short* x;
  const unsigned char* k;
  const unsigned char* v;
  const unsigned char* wq;
  const unsigned char* wo;
  const unsigned char* wg;
  const unsigned char* wu;
  const unsigned char* wd;
  unsigned short* out;
  unsigned char* ws;
};

#define STAMP(i) do { } while (0)

#define KOFF(i) ((((i) >> 1) & 1) * 1024 + ((i) & 1) * 128)
#define LOAD_K(i, off) ldkv(rk, voffk + KOFF((i) & 3), (off) + ((i) >> 2) * 4096)
#define LOAD_V(i, off) ldkv(rv, voffv + ((i) & 3) * 1024, (off) + ((i) >> 2) * 4096)
struct Src { __amdgpu_buffer_rsrc_t rk, rv; int voffk, voffv; };

struct SState {
  f32x4 acc;
  float m0, m1, l0, l1;
  v4u fa, fb;
};

template <int QD> __device__ __forceinline__ void build_frag(v4u& f, unsigned pb) {
  constexpr int r0 = (0 - QD) & 3, r1 = (1 - QD) & 3, r2 = (2 - QD) & 3, r3 = (3 - QD) & 3;
  f.x = __builtin_amdgcn_update_dpp(f.x, pb, r0 ? 0x120 + 4 * r0 : 0xE4, 0xf, 0x1, false);
  f.y = __builtin_amdgcn_update_dpp(f.y, pb, r1 ? 0x120 + 4 * r1 : 0xE4, 0xf, 0x2, false);
  f.z = __builtin_amdgcn_update_dpp(f.z, pb, r2 ? 0x120 + 4 * r2 : 0xE4, 0xf, 0x4, false);
  f.w = __builtin_amdgcn_update_dpp(f.w, pb, r3 ? 0x120 + 4 * r3 : 0xE4, 0xf, 0x8, false);
}

__device__ __forceinline__ v4u ldkv(__amdgpu_buffer_rsrc_t r, int voff, unsigned soff) {
  return __builtin_amdgcn_raw_buffer_load_b128(r, voff, soff, AUX_NT);
}

template <bool NEXT_V>
__device__ __forceinline__ unsigned k_unit(v4u (&t)[16], const v4u (&qf)[4], const Src& sr, unsigned offn, int lane) {
  const __amdgpu_buffer_rsrc_t rk = sr.rk, rv = sr.rv;
  const int voffk = sr.voffk, voffv = sr.voffv;
  float x[8][2];
#pragma unroll
  for (int u = 0; u < 8; ++u) {
    float a0 = 0.f, a1 = 0.f;
#pragma unroll
    for (int hf = 0; hf < 2; ++hf) {
#pragma unroll
      for (int e = 0; e < 4; ++e) { a0 = dot2(t[2 * u + hf][e], qf[hf][e], a0); a1 = dot2(t[2 * u + hf][e], qf[2 + hf][e], a1); }
      asm volatile("" : "+v"(a0), "+v"(a1));
      t[2 * u + hf] = NEXT_V ? LOAD_V(2 * u + hf, offn) : LOAD_K(2 * u + hf, offn);
      __builtin_amdgcn_sched_barrier(0);
    }
    x[u][0] = a0; x[u][1] = a1;
  }
  float sc[2];
#pragma unroll
  for (int h = 0; h < 2; ++h) {
    float y[4], z[2];
#pragma unroll
    for (int v = 0; v < 4; ++v) {
      const float t1 = x[2 * v][h] + dpp<HMIR>(x[2 * v][h]), t2 = x[2 * v + 1][h] + dpp<HMIR>(x[2 * v + 1][h]);
      y[v] = asf(__builtin_amdgcn_update_dpp(asu(t1), asu(t2), 0xE4, 0xf, 0xA, false));
    }
#pragma unroll
    for (int v = 0; v < 2; ++v) { const float t1 = y[v] + dpp<XOR2>(y[v]), t2 = y[2 + v] + dpp<XOR2>(y[2 + v]); z[v] = (lane & 2) ? t2 : t1; }
    const float t1 = z[0] + dpp<XOR1>(z[0]), t2 = z[1] + dpp<XOR1>(z[1]);
    sc[h] = (lane & 1) ? t2 : t1;
  }
  const unsigned pk = cvtpk(sc[0], sc[1]);
  return cvtpk(bflo(pk) * SCALE, bfhi(pk) * SCALE);
}

__device__ __forceinline__ float wave_max(float x) {
  x = fmaxf(x, dpp<XOR1>(x)); x = fmaxf(x, dpp<XOR2>(x)); x = fmaxf(x, dpp<HMIR>(x)); x = fmaxf(x, dpp<ROR8>(x));
  return xrow_max(x);
}

__device__ __forceinline__ void v_unit(v4u (&t)[16], SState& s, unsigned pk, const Src& sr, unsigned offn, unsigned psel) {
  const __amdgpu_buffer_rsrc_t rv = sr.rv;
  const int voffv = sr.voffv;
  const float s0 = bflo(pk) * LOG2E, s1 = bfhi(pk) * LOG2E;
  if (!__all(s0 <= s.m0 && s1 <= s.m1)) {
    const float n0 = fmaxf(s.m0, ceilf(wave_max(s0))), n1 = fmaxf(s.m1, ceilf(wave_max(s1)));
    const float a0 = __builtin_amdgcn_exp2f(s.m0 - n0), a1 = __builtin_amdgcn_exp2f(s.m1 - n1);
    s.acc.x *= a0; s.acc.y *= a0; s.acc.z *= a1; s.acc.w *= a1;
    s.l0 *= a0; s.l1 *= a1;
    s.m0 = n0; s.m1 = n1;
  }
  const float e0 = __builtin_amdgcn_exp2f(s0 - s.m0), e1 = __builtin_amdgcn_exp2f(s1 - s.m1);
  s.l0 += e0; s.l1 += e1;
  const float pe = asf(cvtpk(e0, e1));
#define PV_STEP(IDX, F, QD) \
  build_frag<QD>(F, pb); \
  s.acc = mfma(F, t[IDX], s.acc); \
  t[IDX] = LOAD_V(IDX, offn); \
  __builtin_amdgcn_sched_barrier(0);
#define PV_GROUP(V) { \
    const unsigned pb = __builtin_amdgcn_perm(0u, asu(dpp<(V) * 0x55>(pe)), psel);       \
    PV_STEP(4 * (V), s.fa, 0) PV_STEP(4 * (V) + 1, s.fb, 1) PV_STEP(4 * (V) + 2, s.fa, 2) PV_STEP(4 * (V) + 3, s.fb, 3) }
  PV_GROUP(0) PV_GROUP(1) PV_GROUP(2) PV_GROUP(3)
#undef PV_GROUP
#undef PV_STEP
}

template <int N, bool TAGGED> __device__ __forceinline__ void poll_lines(v4u (&u)[N], __amdgpu_buffer_rsrc_t rws, int off, unsigned tag) {
#pragma unroll
  for (int i = 0; i < N; ++i) u[i] = v4u{0u, 0u, 0u, 0u};
#pragma nounroll
  for (unsigned sp = 0; sp < SPIN_CAP; ++sp) {
    CLOBBER();
    bool ok[N], all = true;
#pragma unroll
    for (int i = 0; i < N; ++i) { ok[i] = TAGGED ? (u[i].y == tag && u[i].w == tag) : (u[i].y != 0u && u[i].w != 0u); all = all && ok[i]; }
    if (__all(all)) break;
    v4u nw[N];
#pragma unroll
    for (int i = 0; i < N; ++i) nw[i] = __builtin_amdgcn_raw_buffer_load_b128(rws, ok[i] ? OOB : off, i * 1024, AUX_SC1);
#pragma unroll
    for (int i = 0; i < N; ++i) u[i] = ok[i] ? u[i] : nw[i];
  }
}

__global__ void __launch_bounds__(NTHR, 2) fused_decode_kernel(Args a) {
  __shared__ __attribute__((aligned(16))) unsigned lds_raw[L_TOTAL];
  volatile LAS unsigned* L = (volatile LAS unsigned*)lds_raw;
  LAS float* LF = (LAS float*)lds_raw;
  const int tid = threadIdx.x, lane = tid & 63, w = __builtin_amdgcn_readfirstlane(tid >> 6);
  const int b = blockIdx.x, g = b >> 4, j = b & 15;
  const __amdgpu_buffer_rsrc_t rws = __builtin_amdgcn_make_buffer_rsrc((void*)a.ws, 0, WS_BYTES, RSRC3);
  const int r4 = lane >> 4, c16 = lane & 15;
  const int p1_off = P1_OFF + ((g * 16 + (c16 >> 1)) * 16 + j) * 128 + (8 * (c16 & 1) + 2 * r4) * 8;
  constexpr int P1_H1 = 8 * 16 * 128;
  const int slot = ((j & 7) - (g >> 1)) & 7, hi = j >> 3;
  const bool is_ffn = slot < 4 || (slot == 4 && hi == 0);
  const int ft = 18 * (g >> 1) + (slot < 4 ? (g & 1) * 8 + 2 * slot + hi : 16 + (g & 1));
  const int fh = b >= 18 ? 1 : 0, fl = b - 18 * fh;
  STAMP(0);

  if (w >= 4) {
    const int sw = w - 4;
    Src sr;
    sr.rk = __builtin_amdgcn_make_buffer_rsrc((void*)(a.k + (size_t)g * HEAD_BYTES), 0, HEAD_BYTES, RSRC3);
    sr.rv = __builtin_amdgcn_make_buffer_rsrc((void*)(a.v + (size_t)g * HEAD_BYTES), 0, HEAD_BYTES, RSRC3);
    sr.voffk = (8 * (c16 >> 3) + r4) * 256 + (lane & 7) * 16;
    sr.voffv = lane * 16;
    const __amdgpu_buffer_rsrc_t rck = __builtin_amdgcn_make_buffer_rsrc((void*)(a.ws + C_OFF + g * 256), 0, 4, RSRC3);
    const __amdgpu_buffer_rsrc_t rcv = __builtin_amdgcn_make_buffer_rsrc((void*)(a.ws + C_OFF + g * 256 + 128), 0, 4, RSRC3);
    const __amdgpu_buffer_rsrc_t rsc = __builtin_amdgcn_make_buffer_rsrc((void*)(a.ws + SC_OFF + g * (NDYN * 256)), 0, NDYN * 256, RSRC3);
    const unsigned sb = (unsigned)(j * 64 + sw) * 16384u;
    auto pool_off = [](unsigned i) { return ((i / DFW) & 15u) * 1048576u + (4 * NSU + (i / (16 * DFW)) * DFW + i % DFW) * 16384u; };
    v4u t[16];
    WG_BARRIER();
    {
      const __amdgpu_buffer_rsrc_t rk = sr.rk;
      const int voffk = sr.voffk;
      unsigned qr = 0;
#pragma unroll
      for (int i = 0; i < 16; ++i) {
        if (i >= PACE_W && !qr) { asm volatile("s_waitcnt vmcnt(%0)" ::"n"(PACE_W - 1) : "memory"); qr = lds_ld(L, L_QRDY); }
        t[i] = LOAD_K(i, sb);
        __builtin_amdgcn_sched_barrier(0);
      }
    }
    lds_wait<1>(L, L_QRDY, 1u);
    STAMP(1);
    v4u qf[4];
#pragma unroll
    for (int i = 0; i < 4; ++i) qf[i] = *(LAS v4u*)(lds_raw + L_Q + 32 * i + 4 * (lane & 7));
    const unsigned T32 = lds_ld(L, L_TAG), c0k = lds_ld(L, L_TAG + 1), c0v = lds_ld(L, L_TAG + 6);
    volatile LAS unsigned* LS = L + L_SC + sw * (NSU * 64) + lane;
    {
      unsigned cur = 0, nxt = 0;
      unsigned offn = sb + 65536u;
#pragma nounroll
      for (int n = 2;; ++n) {
        if (n == NSU) { STAMP(7); lds_wait<1>(L, L_GO, 1u); }
        const unsigned tk = __builtin_amdgcn_raw_ptr_buffer_atomic_add_i32(1, rck, lane * 4, n >= NSU ? 0 : 4, 0);
        const unsigned pk = k_unit<false>(t, qf, sr, offn, lane);
        if (n - 2 < NSU) LS[(n - 2) * 64] = pk;
        else __builtin_amdgcn_raw_buffer_store_b32(pk, rsc, lane * 4, cur * 256u, AUX_SC1);
        cur = nxt;
        if (n >= NSU) {
          nxt = __builtin_amdgcn_readfirstlane(tk) - c0k;
          if (nxt >= NDYN) {
            const unsigned pl = k_unit<true>(t, qf, sr, sb, lane);
            if (n - 1 < NSU) LS[(n - 1) * 64] = pl;
            else __builtin_amdgcn_raw_buffer_store_b32(pl, rsc, lane * 4, cur * 256u, AUX_SC1);
            break;
          }
          offn = pool_off(nxt);
        } else {
          offn = sb + (unsigned)n * 65536u;
        }
      }
    }
    STAMP(2);
    SState s;
    s.acc = f32x4{0.f, 0.f, 0.f, 0.f};
    s.m0 = -INFINITY; s.m1 = -INFINITY; s.l0 = 0.f; s.l1 = 0.f;
    s.fa = v4u{0u, 0u, 0u, 0u}; s.fb = v4u{0u, 0u, 0u, 0u};
    {
      const unsigned psel = (lane & 3) == 0 ? 0x0c0c0100u : (lane & 3) == 1 ? 0x01000c0cu : (lane & 3) == 2 ? 0x0c0c0302u : 0x03020c0cu;
      unsigned offn = sb + 65536u, nxt = 0, scur = 0;
#pragma nounroll
      for (int n = 2;; ++n) {
        if (n == 3) {
          asm volatile("s_waitcnt vmcnt(16)" ::: "memory");
          lds_inc(L, L_CNTK, lane);
        }
        if (n == NSU + 1) lds_wait<1>(L, L_VGO, 1u);
        const unsigned tk = __builtin_amdgcn_raw_ptr_buffer_atomic_add_i32(1, rcv, lane * 4, n >= NSU ? 0 : 4, 0);
        const unsigned snx = __builtin_amdgcn_raw_buffer_load_b32(rsc, lane * 4, (n - 1 >= NSU && offn < HEAD_BYTES) ? nxt * 256u : (unsigned)OOB, AUX_SC1);
        unsigned pk = scur;
        if (n - 2 < NSU) pk = LS[(n - 2) * 64];
        v_unit(t, s, pk, sr, offn, psel);
        if (offn >= HEAD_BYTES) break;
        scur = snx;
        if (n >= NSU) {
          nxt = __builtin_amdgcn_readfirstlane(tk) - c0v;
          offn = nxt < NDYN ? pool_off(nxt) : (unsigned)OOB;
        } else {
          offn = sb + (unsigned)n * 65536u;
        }
      }
    }
    STAMP(3);
    {
      const float lw0 = wave_sum(s.l0), lw1 = wave_sum(s.l1);
      *(LAS f32x4*)(LF + L_ACC + (sw * 64 + lane) * 4) = s.acc;
      if (lane == 0) { *(LAS f32x4*)(LF + L_ML + sw * 4) = f32x4{s.m0, lw0, s.m1, lw1}; L[L_SDONE] = 1u; }
      CLOBBER();
      const unsigned arr = __hip_atomic_fetch_add((LAS unsigned*)(lds_raw + (lane == 0 ? L_CNTS : L_DUMMY + lane)), 1u, __ATOMIC_RELAXED, __HIP_MEMORY_SCOPE_WORKGROUP);
      CLOBBER();
      f32x4 ml[4], ac[4];
#pragma unroll
      for (int i = 0; i < 4; ++i) { ml[i] = *(volatile LAS f32x4*)(LF + L_ML + i * 4); ac[i] = *(volatile LAS f32x4*)(LF + L_ACC + (i * 64 + lane) * 4); }
      if (__builtin_amdgcn_readfirstlane(arr) == 3u) {
        const float M0 = fmaxf(fmaxf(ml[0].x, ml[1].x), fmaxf(ml[2].x, ml[3].x)), M1 = fmaxf(fmaxf(ml[0].z, ml[1].z), fmaxf(ml[2].z, ml[3].z));
        float L0 = 0.f, L1 = 0.f;
        f32x4 o = {0.f, 0.f, 0.f, 0.f};
#pragma unroll
        for (int i = 0; i < 4; ++i) {
          const float w0 = __builtin_amdgcn_exp2f(ml[i].x - M0), w1 = __builtin_amdgcn_exp2f(ml[i].z - M1);
          L0 = fmaf(w0, ml[i].y, L0); L1 = fmaf(w1, ml[i].w, L1);
          o.x = fmaf(w0, ac[i].x, o.x); o.y = fmaf(w0, ac[i].y, o.y); o.z = fmaf(w1, ac[i].z, o.z); o.w = fmaf(w1, ac[i].w, o.w);
        }
        __builtin_amdgcn_raw_buffer_store_b128(v4u{asu(o.x), asu(L0), asu(o.y), ~asu(M0)}, rws, p1_off, 0, AUX_SC1);
        __builtin_amdgcn_raw_buffer_store_b128(v4u{asu(o.z), asu(L1), asu(o.w), ~asu(M1)}, rws, p1_off, P1_H1, AUX_SC1);
      }
    }
    STAMP(4);
    if (b < NFC) {
      lds_wait<1>(L, L_FFR, 1u);
      const f32x4 gx = *(LAS f32x4*)(LF + L_G + 4 * (lane & 7));
      lds_wait<1>(L, L_YD, 3u);
      v4u u[8];
      poll_lines<8, true>(u, rws, P2_OFF + (b * NWG + 64 * sw) * 128 + lane * 16, T32);
      STAMP(5);
      float y0 = 0.f, y1 = 0.f;
#pragma unroll
      for (int i = 0; i < 8; ++i) { y0 += asf(u[i].x); y1 += asf(u[i].z); }
      y0 += dpp<ROR8>(y0); y1 += dpp<ROR8>(y1);
      y0 = xrow_sum(y0); y1 = xrow_sum(y1);
      if (lane < 8) *(LAS f32x2*)(LF + L_FIN + sw * 16 + 2 * lane) = f32x2{y0, y1};
      CLOBBER();
      const unsigned arr = __hip_atomic_fetch_add((LAS unsigned*)(lds_raw + (lane == 0 ? L_CNTF : L_DUMMY + lane)), 1u, __ATOMIC_RELAXED, __HIP_MEMORY_SCOPE_WORKGROUP);
      CLOBBER();
      const int c = lane & 7;
      const f32x2 f0 = *(volatile LAS f32x2*)(LF + L_FIN + 2 * c), f1 = *(volatile LAS f32x2*)(LF + L_FIN + 16 + 2 * c),
                  f2 = *(volatile LAS f32x2*)(LF + L_FIN + 32 + 2 * c), f3 = *(volatile LAS f32x2*)(LF + L_FIN + 48 + 2 * c);
      if (__builtin_amdgcn_readfirstlane(arr) == 3u) {
        const float sv0 = rbf((f0.x + f1.x) + (f2.x + f3.x)), sv1 = rbf((f0.y + f1.y) + (f2.y + f3.y));
        const unsigned pk = cvtpk(gx.z + rbf(sv0 * gx.x), gx.w + rbf(sv1 * gx.y));
        if (lane < 8) *(unsigned*)(a.out + 32 * fl + 4 * lane + 2 * fh) = pk;
        if (b == 0 && lane == 0) {
          const u64 e = (((u64)L[L_TAG + 5] << 32) | L[L_TAG + 4]) * 6364136223846793005ull + 1442695040888963407ull;
          __builtin_amdgcn_raw_buffer_store_b64(v2u{(unsigned)e, (unsigned)(e >> 32)}, rws, E_OFF, 0, AUX_SC1);
        }
        STAMP(6);
      }
    }
  } else if (w >= 1) {
    const int ta = w - 1, tt = tid - 64;
    __builtin_amdgcn_s_setprio(3);
    const int qs = ((b & 7) * 8 + (b >> 5)) * 4 + ((b >> 3) & 3);
    const __amdgpu_buffer_rsrc_t rx = __builtin_amdgcn_make_buffer_rsrc((void*)a.x, 0, D * 2, RSRC3);
    const __amdgpu_buffer_rsrc_t rq = __builtin_amdgcn_make_buffer_rsrc((void*)(a.wq + qs * 32), 0, (D - 1) * QN * 2 + 32, RSRC3);
    const int rr = lane >> 2, cc = lane & 3;
    v4u wqr[6];
    const v4u xc0 = __builtin_amdgcn_raw_buffer_load_b128(rx, lane * 16, 0, 0);
    const v4u xc1 = __builtin_amdgcn_raw_buffer_load_b128(rx, 1024 + lane * 16, 0, 0);
    unsigned xo[3];
#pragma unroll
    for (int i = 0; i < 3; ++i) xo[i] = __builtin_amdgcn_raw_buffer_load_b16(rx, (192 * ta + 64 * i + lane) * 2, 0, 0);
    {
      const int vo = (192 * ta + (lane >> 1)) * (QN * 2) + (lane & 1) * 16;
#pragma unroll
      for (int i = 0; i < 6; ++i) wqr[i] = __builtin_amdgcn_raw_buffer_load_b128(rq, vo, i * 32 * QN * 2, AUXQ);
    }
    WG_BARRIER();
    {
      const float x0 = bflo(__builtin_amdgcn_readfirstlane(xc0.x));
      float s0 = 0.f, s1 = 0.f, q0 = 0.f, q1 = 0.f;
#pragma unroll
      for (int i = 0; i < 4; ++i) {
        const float d0 = bflo(xc0[i]) - x0, d1 = bfhi(xc0[i]) - x0, d2 = bflo(xc1[i]) - x0, d3 = bfhi(xc1[i]) - x0;
        s0 += d0 + d1; s1 += d2 + d3;
        q0 = fmaf(d0, d0, q0); q0 = fmaf(d1, d1, q0); q1 = fmaf(d2, d2, q1); q1 = fmaf(d3, d3, q1);
      }
      const float dm = wave_sum(s0 + (lane < 8 ? s1 : 0.f)) * (1.0f / D);
      const float var = fmaxf(wave_sum(q0 + (lane < 8 ? q1 : 0.f)) * (1.0f / D) - dm * dm, 0.f);
      const float mb = rbf(x0 + dm), den = rbf(sqrtf(rbf(rbf(var) + 1e-5f)));
#pragma unroll
      for (int i = 0; i < 3; ++i) LF[L_XN + 192 * ta + 64 * i + lane] = rbf(rbf(bflo(xo[i]) - mb) / den);
      LGKM0();
    }
    {
      float acc[8];
#pragma unroll
      for (int e = 0; e < 8; ++e) acc[e] = 0.f;
#pragma unroll
      for (int i = 0; i < 6; ++i) {
        const float xn = LF[L_XN + 192 * ta + 32 * i + (lane >> 1)];
#pragma unroll
        for (int e = 0; e < 4; ++e) { acc[2 * e] = fmaf(xn, bflo(wqr[i][e]), acc[2 * e]); acc[2 * e + 1] = fmaf(xn, bfhi(wqr[i][e]), acc[2 * e + 1]); }
      }
      float u[4], v2[2];
#pragma unroll
      for (int e = 0; e < 4; ++e) u[e] = swap32_add(acc[e], acc[4 + e]);
#pragma unroll
      for (int e = 0; e < 2; ++e) v2[e] = swap16_add(u[e], u[2 + e]);
      const float t1 = v2[0] + dpp<ROR8>(v2[0]), t2 = v2[1] + dpp<ROR8>(v2[1]);
      float z = (lane & 8) ? t2 : t1;
      z += dpp<XOR2>(z);
      const float z1 = dpp<SHL4>(z), z2 = dpp<SHR4>(z);
      z += (lane & 4) ? z2 : z1;
      if ((lane & 6) == 0) LF[L_REDQ + ta * 16 + 8 * (lane & 1) + ((lane >> 3) & 7)] = z;
      LGKM0();
      if (lds_inc(L, L_CNTQ, lane) == 2u) {
        const float qv = (LF[L_REDQ + c16] + LF[L_REDQ + 16 + c16]) + LF[L_REDQ + 32 + c16];
        lds_wait<0>(L, L_TRDY, 1u);
        const unsigned tlo = lds_ld(L, L_TAG + 2), thi = lds_ld(L, L_TAG + 3);
        if (lane < 16) __builtin_amdgcn_raw_buffer_store_b64(v2u{(cvtpk(qv, qv) & 0xffffu) | (tlo << 16), thi}, rws, QG_OFF + (qs * 16 + lane) * 8, 0, AUX_SC1);
        CLOBBER();
        if (lane == 0) L[L_QPUB] = 1u;
        STAMP(1);
      }
    }
    lds_wait<1>(L, L_QPUB, 1u);
    const int vo2 = tt < NCG ? tt * 8 : OOB;
    const int o2 = tt < NCG ? (tt & 7) * 16 : OOB;
    v2u wr[16];
    {
      const __amdgpu_buffer_rsrc_t ro = __builtin_amdgcn_make_buffer_rsrc((void*)(a.wo + (size_t)b * 16 * D * 2), 0, 16 * D * 2, RSRC3);
#pragma unroll
      for (int k = 0; k < 16; ++k) wr[k] = __builtin_amdgcn_raw_buffer_load_b64(ro, vo2, k * D * 2, AUXD);
      __builtin_amdgcn_sched_barrier(0);
    }
    STAMP(2);
    if (is_ffn) {
      STAMP(7);
      const __amdgpu_buffer_rsrc_t ra = __builtin_amdgcn_make_buffer_rsrc((void*)(a.wg + ft * 64), 0, D * FF * 2 - ft * 64, RSRC3);
      const __amdgpu_buffer_rsrc_t rb = __builtin_amdgcn_make_buffer_rsrc((void*)(a.wu + ft * 64), 0, D * FF * 2 - ft * 64, RSRC3);
      const __amdgpu_buffer_rsrc_t rd = __builtin_amdgcn_make_buffer_rsrc((void*)(a.wd + (size_t)ft * 32 * D * 2), 0, 32 * D * 2, RSRC3);
      const int vof = (192 * ta + rr) * (FF * 2) + cc * 16;
      constexpr int fstep = 16 * FF * 2;
      v4u wa[12], wb[12];
      v2u dn[32];
#pragma unroll
      for (int i = 0; i < 12; ++i) {
        wa[i] = __builtin_amdgcn_raw_buffer_load_b128(ra, vof, i * fstep, AUXG);
        wb[i] = __builtin_amdgcn_raw_buffer_load_b128(rb, vof, i * fstep, AUXG);
      }
#pragma unroll
      for (int k = 0; k < 32; ++k) dn[k] = __builtin_amdgcn_raw_buffer_load_b64(rd, vo2, k * D * 2, AUXD);
      __builtin_amdgcn_sched_barrier(0);
      float acc[16];
#pragma unroll
      for (int e = 0; e < 16; ++e) acc[e] = 0.f;
#pragma unroll
      for (int i = 0; i < 12; ++i) {
        const float xn = LF[L_XN + 192 * ta + 16 * i + rr];
#pragma unroll
        for (int e = 0; e < 4; ++e) {
          acc[2 * e] = fmaf(xn, bflo(wa[i][e]), acc[2 * e]); acc[2 * e + 1] = fmaf(xn, bfhi(wa[i][e]), acc[2 * e + 1]);
          acc[8 + 2 * e] = fmaf(xn, bflo(wb[i][e]), acc[8 + 2 * e]); acc[9 + 2 * e] = fmaf(xn, bfhi(wb[i][e]), acc[9 + 2 * e]);
        }
        asm volatile("" : "+v"(acc[0]), "+v"(acc[1]), "+v"(acc[2]), "+v"(acc[3]), "+v"(acc[4]), "+v"(acc[5]), "+v"(acc[6]), "+v"(acc[7]),
                          "+v"(acc[8]), "+v"(acc[9]), "+v"(acc[10]), "+v"(acc[11]), "+v"(acc[12]), "+v"(acc[13]), "+v"(acc[14]), "+v"(acc[15]));
      }
      float u[8], v4[4], z[2];
#pragma unroll
      for (int e = 0; e < 8; ++e) u[e] = swap32_add(acc[e], acc[8 + e]);
#pragma unroll
      for (int e = 0; e < 4; ++e) v4[e] = swap16_add(u[e], u[4 + e]);
#pragma unroll
      for (int e = 0; e < 2; ++e) { const float t1 = v4[e] + dpp<ROR8>(v4[e]), t2 = v4[2 + e] + dpp<ROR8>(v4[2 + e]); z[e] = (lane & 8) ? t2 : t1; }
      const float t1 = z[0] + dpp<SHL4>(z[0]), t2 = z[1] + dpp<SHR4>(z[1]);
      const float zz = (lane & 4) ? t2 : t1;
      LF[L_RED + ta * 64 + (lane & 32) + 8 * cc + ((lane >> 2) & 7)] = zz;
      LGKM0();
      lds_inc(L, L_CNTA, lane);
      lds_wait<0>(L, L_CNTA, 3u);
      const int u5 = lane & 31;
      const float gp = rbf((LF[L_RED + u5] + LF[L_RED + 64 + u5]) + LF[L_RED + 128 + u5]);
      const float up = rbf((LF[L_RED + 32 + u5] + LF[L_RED + 96 + u5]) + LF[L_RED + 160 + u5]);
      const float h = rbf(rbf(silu_f(gp)) * up);
      f32x4 y = {0.f, 0.f, 0.f, 0.f};
#pragma unroll
      for (int k = 0; k < 32; ++k) {
        const float hk = asf(__builtin_amdgcn_readlane(asu(h), k));
        y.x = fmaf(hk, bflo(dn[k].x), y.x); y.y = fmaf(hk, bfhi(dn[k].x), y.y); y.z = fmaf(hk, bflo(dn[k].y), y.z); y.w = fmaf(hk, bfhi(dn[k].y), y.w);
      }
      __builtin_amdgcn_raw_buffer_store_b128(__builtin_bit_cast(v4u, y), rws, tt < NCG ? FP_OFF + ft * (D * 4) + tt * 16 : OOB, 0, AUX_SC1);
      STAMP(6);
    }
    DRAIN();
    lds_inc(L, L_CNTD, lane);
    float wf[64];
#pragma unroll
    for (int k = 0; k < 16; ++k) { wf[4 * k] = bflo(wr[k].x); wf[4 * k + 1] = bfhi(wr[k].x); wf[4 * k + 2] = bflo(wr[k].y); wf[4 * k + 3] = bfhi(wr[k].y); }
#pragma unroll
    for (int k = 0; k < 64; ++k) asm volatile("" : "+v"(wf[k]));
    lds_wait<8>(L, L_SDONE, 1u);
    STAMP(3);
    const unsigned T32 = lds_ld(L, L_TAG);
    unsigned opk = 0;
#pragma nounroll
    for (unsigned sp = 0; sp < (SPIN_CAP << 4); ++sp) {
      const unsigned f = L[L_ORDY];
      opk = L[L_O + (lane & 7)];
      if (__builtin_amdgcn_readfirstlane(f)) break;
    }
    STAMP(4);
    f32x4 y = {0.f, 0.f, 0.f, 0.f};
#pragma unroll
    for (int k = 0; k < 8; ++k) {
      const unsigned sp = __builtin_amdgcn_readlane(opk, k);
      const float e0 = bflo(sp), e1 = bfhi(sp);
      y.x = fmaf(e0, wf[8 * k], y.x); y.y = fmaf(e0, wf[8 * k + 1], y.y); y.z = fmaf(e0, wf[8 * k + 2], y.z); y.w = fmaf(e0, wf[8 * k + 3], y.w);
      y.x = fmaf(e1, wf[8 * k + 4], y.x); y.y = fmaf(e1, wf[8 * k + 5], y.y); y.z = fmaf(e1, wf[8 * k + 6], y.z); y.w = fmaf(e1, wf[8 * k + 7], y.w);
    }
    const int op = P2_OFF + ((tt >> 3) * NWG + b) * 128;
    __builtin_amdgcn_raw_buffer_store_b128(v4u{asu(y.x), T32, asu(y.y), T32}, rws, o2 + op, 0, AUX_SC1);
    __builtin_amdgcn_raw_buffer_store_b128(v4u{asu(y.z), T32, asu(y.w), T32}, rws, o2 + op, 18 * NWG * 128, AUX_SC1);
    lds_inc(L, L_YD, lane);
    STAMP(5);
  } else {
    __builtin_amdgcn_s_setprio(3);
    const v2u ev = __builtin_amdgcn_raw_buffer_load_b64(rws, E_OFF, 0, AUX_SC1);
    const unsigned cv = __builtin_amdgcn_raw_buffer_load_b32(rws, C_OFF + g * 256, 0, AUX_SC1);
    const unsigned cv2 = __builtin_amdgcn_raw_buffer_load_b32(rws, C_OFF + g * 256 + 128, 0, AUX_SC1);
    if (lane < L_NCTL) L[lane] = 0u;
    LGKM0();
    WG_BARRIER();
    const unsigned elo = __builtin_amdgcn_readfirstlane(ev.x), ehi = __builtin_amdgcn_readfirstlane(ev.y);
    const unsigned qlo = (elo ^ 0x7F4A7C15u) & 0xffffu, qhi = ((elo ^ 0x7F4A7C15u) >> 16) | ((ehi ^ 0x9E3779B9u) << 16);
    const unsigned rlo = elo ^ 0xC2B2AE35u, rhi = ehi ^ 0x27D4EB2Fu;
    if (lane == 0) { L[L_TAG] = (elo ^ 0x85EBCA6Bu) | 1u; L[L_TAG + 1] = cv; L[L_TAG + 6] = cv2; L[L_TAG + 2] = qlo; L[L_TAG + 3] = qhi; L[L_TAG + 4] = elo; L[L_TAG + 5] = ehi; }
    LGKM0();
    if (lane == 0) L[L_TRDY] = 1u;
    STAMP(1);
    {
      const int oq = QG_OFF + g * 2048 + lane * 16;
      v4u q0 = {0u, ~qhi, 0u, 0u}, q1 = {0u, ~qhi, 0u, 0u};
#pragma nounroll
      for (unsigned sp = 0; sp < SPIN_CAP; ++sp) {
        CLOBBER();
        const bool ok0 = q0.y == qhi && q0.w == qhi && (q0.x >> 16) == qlo && (q0.z >> 16) == qlo;
        const bool ok1 = q1.y == qhi && q1.w == qhi && (q1.x >> 16) == qlo && (q1.z >> 16) == qlo;
        if (__all(ok0 && ok1)) break;
        const v4u n0 = __builtin_amdgcn_raw_buffer_load_b128(rws, ok0 ? OOB : oq, 0, AUX_SC1);
        const v4u n1 = __builtin_amdgcn_raw_buffer_load_b128(rws, ok1 ? OOB : oq + 1024, 0, AUX_SC1);
        q0 = ok0 ? q0 : n0; q1 = ok1 ? q1 : n1;
      }
      L[L_Q + lane] = (q0.x & 0xffffu) | (q0.z << 16);
      L[L_Q + 64 + lane] = (q1.x & 0xffffu) | (q1.z << 16);
      LGKM0();
      if (lane == 0) L[L_QRDY] = 1u;
    }
    STAMP(2);
    {
      const v4u zero = {0u, 0u, 0u, 0u};
      __builtin_amdgcn_raw_buffer_store_b128(zero, rws, p1_off, 0, AUX_SC1);
      __builtin_amdgcn_raw_buffer_store_b128(zero, rws, p1_off, P1_H1, AUX_SC1);
#pragma unroll
      for (int i = 0; i < 5; ++i) {
        const int e = i * 8 + (lane >> 3);
        __builtin_amdgcn_raw_buffer_store_b128(zero, rws, e < NFC ? P2_OFF + (e * NWG + b) * 128 + (lane & 7) * 16 : OOB, 0, AUX_SC1);
      }
    }
    DRAIN();
    if (lane == 0) __builtin_amdgcn_raw_buffer_store_b64(v2u{rlo, rhi}, rws, RDY_OFF + b * 8, 0, AUX_SC1);
    STAMP(3);
#pragma nounroll
    for (unsigned sp = 0; sp < SPIN_CAP; ++sp) {
      __builtin_amdgcn_s_sleep(30);
      CLOBBER();
      const v4u f0 = __builtin_amdgcn_raw_buffer_load_b128(rws, RDY_OFF + g * 128 + (lane & 7) * 16, 0, AUX_SC1);
      const bool ok = f0.x == rlo && f0.y == rhi && f0.z == rlo && f0.w == rhi;
      if (__all(ok)) break;
    }
    CLOBBER();
    if (lane == 0) L[L_GO] = 1u;
    STAMP(4);
    if (is_ffn) {
      lds_wait<8>(L, L_CNTD, 3u);
      if (lane == 0) __builtin_amdgcn_raw_buffer_store_b64(v2u{rlo, rhi}, rws, RDF_OFF + ft * 8, 0, AUX_SC1);
    }
    if (b < NFC) {
#pragma nounroll
      for (unsigned sp = 0; sp < SPIN_CAP; ++sp) {
        __builtin_amdgcn_s_sleep(127);
        CLOBBER();
        const v4u f0 = __builtin_amdgcn_raw_buffer_load_b128(rws, RDF_OFF + lane * 16, 0, AUX_SC1);
        const v4u f1 = __builtin_amdgcn_raw_buffer_load_b128(rws, RDF_OFF + 1024 + (lane & 7) * 16, 0, AUX_SC1);
        const bool ok = f0.x == rlo && f0.y == rhi && f0.z == rlo && f0.w == rhi && f1.x == rlo && f1.y == rhi && f1.z == rlo && f1.w == rhi;
        if (__all(ok)) break;
      }
      CLOBBER();
      const int of = FP_OFF + (lane >> 3) * (D * 4) + fl * 128 + (lane & 7) * 16 + fh * 8;
      v2u f[18];
#pragma unroll
      for (int i = 0; i < 18; ++i) f[i] = __builtin_amdgcn_raw_buffer_load_b64(rws, of, i * 8 * D * 4, AUX_SC1);
      const unsigned xv = *(const unsigned*)(a.x + 32 * fl + 4 * (lane & 7) + 2 * fh);
      float y0 = 0.f, y1 = 0.f;
#pragma unroll
      for (int i = 0; i < 18; ++i) { y0 += asf(f[i].x); y1 += asf(f[i].y); }
      y0 += dpp<ROR8>(y0); y1 += dpp<ROR8>(y1);
      y0 = xrow_sum(y0); y1 = xrow_sum(y1);
      if (lane < 8) *(LAS f32x4*)(LF + L_G + 4 * lane) = f32x4{rbf(silu_f(rbf(y0))), rbf(silu_f(rbf(y1))), bflo(xv), bfhi(xv)};
      LGKM0();
      if (lane == 0) L[L_FFR] = 1u;
    }
    STAMP(5);
    lds_wait<16>(L, L_CNTK, 4u);
    if (lane == 0) __builtin_amdgcn_raw_buffer_store_b64(v2u{rlo, rhi}, rws, RDK_OFF + b * 8, 0, AUX_SC1);
#pragma nounroll
    for (unsigned sp = 0; sp < SPIN_CAP; ++sp) {
      __builtin_amdgcn_s_sleep(60);
      CLOBBER();
      const v4u f0 = __builtin_amdgcn_raw_buffer_load_b128(rws, RDK_OFF + g * 128 + (lane & 7) * 16, 0, AUX_SC1);
      const bool ok = f0.x == rlo && f0.y == rhi && f0.z == rlo && f0.w == rhi;
      if (__all(ok)) break;
    }
    CLOBBER();
    if (lane == 0) L[L_VGO] = 1u;
    lds_wait<4>(L, L_SDONE, 1u);
    lds_wait<1>(L, L_CNTS, 4u);
    STAMP(6);
    {
      v4u p[2];
      poll_lines<2, false>(p, rws, P1_OFF + (g * 16 + j) * 2048 + lane * 16, 0u);
      float M = fmaxf(asf(~p[0].w), asf(~p[1].w));
      M = fmaxf(M, dpp<ROR8>(M)); M = xrow_max(M);
      float Ls = 0.f, o0 = 0.f, o1 = 0.f;
#pragma unroll
      for (int i = 0; i < 2; ++i) {
        const float wt = __builtin_amdgcn_exp2f(asf(~p[i].w) - M);
        Ls = fmaf(wt, asf(p[i].y), Ls); o0 = fmaf(wt, asf(p[i].x), o0); o1 = fmaf(wt, asf(p[i].z), o1);
      }
      Ls += dpp<ROR8>(Ls); o0 += dpp<ROR8>(o0); o1 += dpp<ROR8>(o1);
      Ls = xrow_sum(Ls); o0 = xrow_sum(o0); o1 = xrow_sum(o1);
      const unsigned opk = cvtpk(o0 / Ls, o1 / Ls);
      if (lane < 8) L[L_O + lane] = opk;
      CLOBBER();
      if (lane == 0) L[L_ORDY] = 1u;
    }
    STAMP(7);
  }
}

}

extern "C" void kernel_launch(void* const* d_in, const int* in_sizes, int n_in, void* d_out, int out_size, void* d_ws, size_t ws_size,
                              hipStream_t stream) {
  Args a;
  a.x = (const unsigned short*)d_in[0];
  a.k = (const unsigned char*)d_in[1];
  a.v = (const unsigned char*)d_in[2];
  a.wq = (const unsigned char*)d_in[3];
  a.wo = (const unsigned char*)d_in[4];
  a.wg = (const unsigned char*)d_in[6];
  a.wu = (const unsigned char*)d_in[7];
  a.wd = (const unsigned char*)d_in[8];
  a.out = (unsigned short*)d_out;
  a.ws = (unsigned char*)d_ws;
  hipLaunchKernelGGL(fused_decode_kernel, dim3(NWG), dim3(NTHR), 0, stream, a);
}
```

```cpp
#include <hip/hip_runtime.h>
#include <hip/hip_bf16.h>
#include <stdint.h>

namespace {

typedef unsigned v4u __attribute__((ext_vector_type(4)));
typedef unsigned v2u __attribute__((ext_vector_type(2)));
typedef float f32x4 __attribute__((ext_vector_type(4)));
typedef float f32x2 __attribute__((ext_vector_type(2)));
typedef __bf16 bf16x8 __attribute__((ext_vector_type(8)));
typedef __bf16 bf16x2 __attribute__((ext_vector_type(2)));
typedef unsigned long long u64;
#define LAS __attribute__((address_space(3)))

constexpr int D = 576, SK = 65536, DH = 128, FF = 4608, QN = 4096;
constexpr int NWG = 256, NTHR = 512;
constexpr int NU = SK / 64;
constexpr int NSU = 12;
constexpr int DFW = 8;
constexpr unsigned NDYN = NU - NSU * 64;
static_assert(NSU >= 4 && NSU < 16 && (64 - 4 * NSU) % DFW == 0, "");
constexpr unsigned HEAD_BYTES = (unsigned)SK * DH * 2;
constexpr int NCG = D / 4;
constexpr int NFC = 36;
constexpr int NFT = FF / 32;
constexpr int OOB = 0x7f000000;

constexpr int E_OFF = 0;
constexpr int C_OFF = 256;
constexpr int RDY_OFF = 8192;
constexpr int RDK_OFF = 10240;
constexpr int RDF_OFF = 12288;
constexpr int QG_OFF = 16384;
constexpr int P1_OFF = 65536;
constexpr int P2_OFF = P1_OFF + 16 * 16 * 16 * 128;
constexpr int FP_OFF = P2_OFF + NFC * NWG * 128;
constexpr int SC_OFF = FP_OFF + NFT * D * 4;
constexpr int WS_BYTES = SC_OFF + 16 * NDYN * 256;

constexpr int AUX_NT = 2, AUX_SC1 = 16;
constexpr int AUXQ = 0, AUXG = 0, AUXD = AUX_NT;
constexpr unsigned RSRC3 = 0x00020000u;
constexpr unsigned SPIN_CAP = 1u << 17;

constexpr int L_QRDY = 0, L_TRDY = 1, L_CNTA = 2, L_CNTQ = 3, L_CNTS = 4, L_CNTF = 5, L_GO = 6, L_FFR = 7, L_SDONE = 8, L_YD = 9, L_QPUB = 10, L_ORDY = 11, L_CNTD = 12, L_CNTK = 13, L_VGO = 14;
constexpr int L_NCTL = 16;
constexpr int L_TAG = 16;
constexpr int L_Q = 32;
constexpr int L_RED = 192;
constexpr int L_O = 384;
constexpr int L_REDQ = 400;
constexpr int L_G = 448;
constexpr int L_FIN = 512;
constexpr int L_XN = 576;
constexpr int L_ML = 1152;
constexpr int L_ACC = 1280;
constexpr int L_DUMMY = 3072;
constexpr int L_SC = 4096;
constexpr int L_TOTAL = 24 * 1024;

constexpr float SCALE = 0.08838834764831845f;
constexpr float LOG2E = 1.4426950408889634f;

__device__ __forceinline__ unsigned cvtpk(float lo, float hi) {
  f32x2 v = {lo, hi};
  return __builtin_bit_cast(unsigned, __builtin_convertvector(v, bf16x2));
}
__device__ __forceinline__ float bflo(unsigned u) { return __builtin_bit_cast(float, u << 16); }
__device__ __forceinline__ float bfhi(unsigned u) { return __builtin_bit_cast(float, u & 0xffff0000u); }
__device__ __forceinline__ float rbf(float x) { return bflo(cvtpk(x, x)); }
__device__ __forceinline__ float asf(unsigned u) { return __builtin_bit_cast(float, u); }
__device__ __forceinline__ unsigned asu(float f) { return __builtin_bit_cast(unsigned, f); }

template <int CTRL> __device__ __forceinline__ float dpp(float x) {
  return __builtin_bit_cast(float, __builtin_amdgcn_mov_dpp(__builtin_bit_cast(int, x), CTRL, 0xf, 0xf, true));
}
constexpr int XOR1 = 0xB1, XOR2 = 0x4E, HMIR = 0x141, ROR8 = 0x128, SHL4 = 0x104, SHR4 = 0x114;

__device__ __forceinline__ float xrow_max(float x) {
  auto s = __builtin_amdgcn_permlane16_swap(asu(x), asu(x), false, false);
  x = fmaxf(asf(s[0]), asf(s[1]));
  auto t = __builtin_amdgcn_permlane32_swap(asu(x), asu(x), false, false);
  return fmaxf(asf(t[0]), asf(t[1]));
}
__device__ __forceinline__ float xrow_sum(float x) {
  auto s = __builtin_amdgcn_permlane16_swap(asu(x), asu(x), false, false);
  x = asf(s[0]) + asf(s[1]);
  auto t = __builtin_amdgcn_permlane32_swap(asu(x), asu(x), false, false);
  return asf(t[0]) + asf(t[1]);
}
__device__ __forceinline__ float wave_sum(float x) {
  x += dpp<XOR1>(x); x += dpp<XOR2>(x); x += dpp<HMIR>(x); x += dpp<ROR8>(x);
  return xrow_sum(x);
}
__device__ __forceinline__ float swap32_add(float a, float b) {
  auto s = __builtin_amdgcn_permlane32_swap(asu(a), asu(b), false, false);
  return asf(s[0]) + asf(s[1]);
}
__device__ __forceinline__ float swap16_add(float a, float b) {
  auto s = __builtin_amdgcn_permlane16_swap(asu(a), asu(b), false, false);
  return asf(s[0]) + asf(s[1]);
}
__device__ __forceinline__ f32x4 mfma(v4u a, v4u b, f32x4 c) {
  return __builtin_amdgcn_mfma_f32_16x16x32_bf16(__builtin_bit_cast(bf16x8, a), __builtin_bit_cast(bf16x8, b), c, 0, 0, 0);
}
__device__ __forceinline__ float dot2(unsigned a, unsigned b, float c) {
  return __builtin_amdgcn_fdot2_f32_bf16(__builtin_bit_cast(bf16x2, a), __builtin_bit_cast(bf16x2, b), c, false);
}
__device__ __forceinline__ float silu_f(float x) { return x / (1.0f + __expf(-x)); }

#define WG_BARRIER() do { __builtin_amdgcn_sched_barrier(0); __builtin_amdgcn_s_barrier(); __builtin_amdgcn_sched_barrier(0); } while (0)
#define CLOBBER() asm volatile("" ::: "memory")
#define LGKM0() asm volatile("s_waitcnt lgkmcnt(0)" ::: "memory")
#define DRAIN() asm volatile("s_waitcnt vmcnt(0)" ::: "memory")

__device__ __forceinline__ unsigned lds_ld(volatile LAS unsigned* L, int i) { return __builtin_amdgcn_readfirstlane(L[i]); }
template <int SLEEP> __device__ __forceinline__ void lds_wait(volatile LAS unsigned* L, int i, unsigned want) {
#pragma nounroll
  for (unsigned s = 0; s < (SPIN_CAP << 3); ++s) {
    if (lds_ld(L, i) >= want) break;
    __builtin_amdgcn_s_sleep(SLEEP);
  }
  CLOBBER();
}
__device__ __forceinline__ unsigned lds_inc(volatile LAS unsigned* L, int i, int lane) {
  unsigned r = 0;
  if (lane == 0) r = __hip_atomic_fetch_add((LAS unsigned*)(L + i), 1u, __ATOMIC_RELAXED, __HIP_MEMORY_SCOPE_WORKGROUP);
  r = __builtin_amdgcn_readfirstlane(r);
  CLOBBER();
  return r;
}

struct Args {
  const unsigned short* x;
  const unsigned char* k;
  const unsigned char* v;
  const unsigned char* wq;
  const unsigned char* wo;
  const unsigned char* wg;
  const unsigned char* wu;
  const unsigned char* wd;
  unsigned short* out;
  unsigned char* ws;
};

#define KOFF(i) ((((i) >> 1) & 1) * 1024 + ((i) & 1) * 128)
#define LOAD_K(i, off) ldkv(rk, voffk + KOFF((i) & 3), (off) + ((i) >> 2) * 4096)
#define LOAD_V(i, off) ldkv(rv, voffv + ((i) & 3) * 1024, (off) + ((i) >> 2) * 4096)
struct Src { __amdgpu_buffer_rsrc_t rk, rv; int voffk, voffv; };

struct SState {
  f32x4 acc;
  float m0, m1, l0, l1;
  v4u fa, fb;
};

template <int QD> __device__ __forceinline__ void build_frag(v4u& f, unsigned pb) {
  constexpr int r0 = (0 - QD) & 3, r1 = (1 - QD) & 3, r2 = (2 - QD) & 3, r3 = (3 - QD) & 3;
  f.x = __builtin_amdgcn_update_dpp(f.x, pb, r0 ? 0x120 + 4 * r0 : 0xE4, 0xf, 0x1, false);
  f.y = __builtin_amdgcn_update_dpp(f.y, pb, r1 ? 0x120 + 4 * r1 : 0xE4, 0xf, 0x2, false);
  f.z = __builtin_amdgcn_update_dpp(f.z, pb, r2 ? 0x120 + 4 * r2 : 0xE4, 0xf, 0x4, false);
  f.w = __builtin_amdgcn_update_dpp(f.w, pb, r3 ? 0x120 + 4 * r3 : 0xE4, 0xf, 0x8, false);
}

__device__ __forceinline__ v4u ldkv(__amdgpu_buffer_rsrc_t r, int voff, unsigned soff) {
  return __builtin_amdgcn_raw_buffer_load_b128(r, voff, soff, AUX_NT);
}

template <bool NEXT_V>
__device__ __forceinline__ unsigned k_unit(v4u (&t)[16], const v4u (&qf)[4], const Src& sr, unsigned offn, int lane) {
  const __amdgpu_buffer_rsrc_t rk = sr.rk, rv = sr.rv;
  const int voffk = sr.voffk, voffv = sr.voffv;
  float x[8][2];
#pragma unroll
  for (int u = 0; u < 8; ++u) {
    float a0 = 0.f, a1 = 0.f;
#pragma unroll
    for (int hf = 0; hf < 2; ++hf) {
#pragma unroll
      for (int e = 0; e < 4; ++e) { a0 = dot2(t[2 * u + hf][e], qf[hf][e], a0); a1 = dot2(t[2 * u + hf][e], qf[2 + hf][e], a1); }
      asm volatile("" : "+v"(a0), "+v"(a1));
      t[2 * u + hf] = NEXT_V ? LOAD_V(2 * u + hf, offn) : LOAD_K(2 * u + hf, offn);
      __builtin_amdgcn_sched_barrier(0);
    }
    x[u][0] = a0; x[u][1] = a1;
  }
  float sc[2];
#pragma unroll
  for (int h = 0; h < 2; ++h) {
    float y[4], z[2];
#pragma unroll
    for (int v = 0; v < 4; ++v) {
      const float t1 = x[2 * v][h] + dpp<HMIR>(x[2 * v][h]), t2 = x[2 * v + 1][h] + dpp<HMIR>(x[2 * v + 1][h]);
      y[v] = asf(__builtin_amdgcn_update_dpp(asu(t1), asu(t2), 0xE4, 0xf, 0xA, false));
    }
#pragma unroll
    for (int v = 0; v < 2; ++v) { const float t1 = y[v] + dpp<XOR2>(y[v]), t2 = y[2 + v] + dpp<XOR2>(y[2 + v]); z[v] = (lane & 2) ? t2 : t1; }
    const float t1 = z[0] + dpp<XOR1>(z[0]), t2 = z[1] + dpp<XOR1>(z[1]);
    sc[h] = (lane & 1) ? t2 : t1;
  }
  const unsigned pk = cvtpk(sc[0], sc[1]);
  return cvtpk(bflo(pk) * SCALE, bfhi(pk) * SCALE);
}

__device__ __forceinline__ float wave_max(float x) {
  x = fmaxf(x, dpp<XOR1>(x)); x = fmaxf(x, dpp<XOR2>(x)); x = fmaxf(x, dpp<HMIR>(x)); x = fmaxf(x, dpp<ROR8>(x));
  return xrow_max(x);
}

__device__ __forceinline__ void v_unit(v4u (&t)[16], SState& s, unsigned pk, const Src& sr, unsigned offn, unsigned psel) {
  const __amdgpu_buffer_rsrc_t rv = sr.rv;
  const int voffv = sr.voffv;
  const float s0 = bflo(pk) * LOG2E, s1 = bfhi(pk) * LOG2E;
  if (!__all(s0 <= s.m0 && s1 <= s.m1)) {
    const float n0 = fmaxf(s.m0, ceilf(wave_max(s0))), n1 = fmaxf(s.m1, ceilf(wave_max(s1)));
    const float a0 = __builtin_amdgcn_exp2f(s.m0 - n0), a1 = __builtin_amdgcn_exp2f(s.m1 - n1);
    s.acc.x *= a0; s.acc.y *= a0; s.acc.z *= a1; s.acc.w *= a1;
    s.l0 *= a0; s.l1 *= a1;
    s.m0 = n0; s.m1 = n1;
  }
  const float e0 = __builtin_amdgcn_exp2f(s0 - s.m0), e1 = __builtin_amdgcn_exp2f(s1 - s.m1);
  s.l0 += e0; s.l1 += e1;
  const float pe = asf(cvtpk(e0, e1));
#define PV_STEP(IDX, F, QD) \
  build_frag<QD>(F, pb); \
  s.acc = mfma(F, t[IDX], s.acc); \
  t[IDX] = LOAD_V(IDX, offn); \
  __builtin_amdgcn_sched_barrier(0);
#define PV_GROUP(V) { \
    const unsigned pb = __builtin_amdgcn_perm(0u, asu(dpp<(V) * 0x55>(pe)), psel);       \
    PV_STEP(4 * (V), s.fa, 0) PV_STEP(4 * (V) + 1, s.fb, 1) PV_STEP(4 * (V) + 2, s.fa, 2) PV_STEP(4 * (V) + 3, s.fb, 3) }
  PV_GROUP(0) PV_GROUP(1) PV_GROUP(2) PV_GROUP(3)
#undef PV_GROUP
#undef PV_STEP
}

template <int N, bool TAGGED> __device__ __forceinline__ void poll_lines(v4u (&u)[N], __amdgpu_buffer_rsrc_t rws, int off, unsigned tag) {
#pragma unroll
  for (int i = 0; i < N; ++i) u[i] = v4u{0u, 0u, 0u, 0u};
#pragma nounroll
  for (unsigned sp = 0; sp < SPIN_CAP; ++sp) {
    CLOBBER();
    bool ok[N], all = true;
#pragma unroll
    for (int i = 0; i < N; ++i) { ok[i] = TAGGED ? (u[i].y == tag && u[i].w == tag) : (u[i].y != 0u && u[i].w != 0u); all = all && ok[i]; }
    if (__all(all)) break;
    v4u nw[N];
#pragma unroll
    for (int i = 0; i < N; ++i) nw[i] = __builtin_amdgcn_raw_buffer_load_b128(rws, ok[i] ? OOB : off, i * 1024, AUX_SC1);
#pragma unroll
    for (int i = 0; i < N; ++i) u[i] = ok[i] ? u[i] : nw[i];
  }
}

__global__ void __launch_bounds__(NTHR, 2) fused_decode_kernel(Args a) {
  __shared__ __attribute__((aligned(16))) unsigned lds_raw[L_TOTAL];
  volatile LAS unsigned* L = (volatile LAS unsigned*)lds_raw;
  LAS float* LF = (LAS float*)lds_raw;
  const int tid = threadIdx.x, lane = tid & 63, w = __builtin_amdgcn_readfirstlane(tid >> 6);
  const int b = blockIdx.x, g = b >> 4, j = b & 15;
  const __amdgpu_buffer_rsrc_t rws = __builtin_amdgcn_make_buffer_rsrc((void*)a.ws, 0, WS_BYTES, RSRC3);
  const int r4 = lane >> 4, c16 = lane & 15;
  const int p1_off = P1_OFF + ((g * 16 + (c16 >> 1)) * 16 + j) * 128 + (8 * (c16 & 1) + 2 * r4) * 8;
  constexpr int P1_H1 = 8 * 16 * 128;
  const int slot = ((j & 7) - (g >> 1)) & 7, hi = j >> 3;
  const bool is_ffn = slot < 4 || (slot == 4 && hi == 0);
  const int ft = 18 * (g >> 1) + (slot < 4 ? (g & 1) * 8 + 2 * slot + hi : 16 + (g & 1));
  const int fh = b >= 18 ? 1 : 0, fl = b - 18 * fh;

  if (w >= 4) {
    const int sw = w - 4;
    Src sr;
    sr.rk = __builtin_amdgcn_make_buffer_rsrc((void*)(a.k + (size_t)g * HEAD_BYTES), 0, HEAD_BYTES, RSRC3);
    sr.rv = __builtin_amdgcn_make_buffer_rsrc((void*)(a.v + (size_t)g * HEAD_BYTES), 0, HEAD_BYTES, RSRC3);
    sr.voffk = (8 * (c16 >> 3) + r4) * 256 + (lane & 7) * 16;
    sr.voffv = lane * 16;
    const __amdgpu_buffer_rsrc_t rck = __builtin_amdgcn_make_buffer_rsrc((void*)(a.ws + C_OFF + g * 256), 0, 4, RSRC3);
    const __amdgpu_buffer_rsrc_t rcv = __builtin_amdgcn_make_buffer_rsrc((void*)(a.ws + C_OFF + g * 256 + 128), 0, 4, RSRC3);
    const __amdgpu_buffer_rsrc_t rsc = __builtin_amdgcn_make_buffer_rsrc((void*)(a.ws + SC_OFF + g * (NDYN * 256)), 0, NDYN * 256, RSRC3);
    const unsigned sb = (unsigned)(j * 64 + sw) * 16384u;
    auto pool_off = [](unsigned i) { return ((i / DFW) & 15u) * 1048576u + (4 * NSU + (i / (16 * DFW)) * DFW + i % DFW) * 16384u; };
    v4u t[16];
    WG_BARRIER();
    {
      const __amdgpu_buffer_rsrc_t rk = sr.rk;
      const int voffk = sr.voffk;
#pragma unroll
      for (int i = 0; i < 16; ++i) t[i] = LOAD_K(i, sb);
      __builtin_amdgcn_sched_barrier(0);
    }
    lds_wait<1>(L, L_QRDY, 1u);
    v4u qf[4];
#pragma unroll
    for (int i = 0; i < 4; ++i) qf[i] = *(LAS v4u*)(lds_raw + L_Q + 32 * i + 4 * (lane & 7));
    const unsigned T32 = lds_ld(L, L_TAG), c0k = lds_ld(L, L_TAG + 1), c0v = lds_ld(L, L_TAG + 6);
    volatile LAS unsigned* LS = L + L_SC + sw * (NSU * 64) + lane;
    {
      unsigned cur = 0, nxt = 0;
      unsigned offn = sb + 65536u;
#pragma nounroll
      for (int n = 2;; ++n) {
        if (n == NSU) lds_wait<1>(L, L_GO, 1u);
        const unsigned tk = __builtin_amdgcn_raw_ptr_buffer_atomic_add_i32(1, rck, lane * 4, n >= NSU ? 0 : 4, 0);
        const unsigned pk = k_unit<false>(t, qf, sr, offn, lane);
        if (n - 2 < NSU) LS[(n - 2) * 64] = pk;
        else __builtin_amdgcn_raw_buffer_store_b32(pk, rsc, lane * 4, cur * 256u, AUX_SC1);
        cur = nxt;
        if (n >= NSU) {
          nxt = __builtin_amdgcn_readfirstlane(tk) - c0k;
          if (nxt >= NDYN) {
            const unsigned pl = k_unit<true>(t, qf, sr, sb, lane);
            if (n - 1 < NSU) LS[(n - 1) * 64] = pl;
            else __builtin_amdgcn_raw_buffer_store_b32(pl, rsc, lane * 4, cur * 256u, AUX_SC1);
            break;
          }
          offn = pool_off(nxt);
        } else {
          offn = sb + (unsigned)n * 65536u;
        }
      }
    }
    SState s;
    s.acc = f32x4{0.f, 0.f, 0.f, 0.f};
    s.m0 = -INFINITY; s.m1 = -INFINITY; s.l0 = 0.f; s.l1 = 0.f;
    s.fa = v4u{0u, 0u, 0u, 0u}; s.fb = v4u{0u, 0u, 0u, 0u};
    {
      const unsigned psel = (lane & 3) == 0 ? 0x0c0c0100u : (lane & 3) == 1 ? 0x01000c0cu : (lane & 3) == 2 ? 0x0c0c0302u : 0x03020c0cu;
      unsigned offn = sb + 65536u, nxt = 0, scur = 0;
#pragma nounroll
      for (int n = 2;; ++n) {
        if (n == 3) {
          asm volatile("s_waitcnt vmcnt(16)" ::: "memory");
          lds_inc(L, L_CNTK, lane);
        }
        if (n == NSU + 1) lds_wait<1>(L, L_VGO, 1u);
        const unsigned tk = __builtin_amdgcn_raw_ptr_buffer_atomic_add_i32(1, rcv, lane * 4, (n >= NSU && offn < HEAD_BYTES) ? 0 : 4, 0);
        const unsigned snx = __builtin_amdgcn_raw_buffer_load_b32(rsc, lane * 4, (n - 1 >= NSU && offn < HEAD_BYTES) ? nxt * 256u : (unsigned)OOB, AUX_SC1);
        unsigned pk = scur;
        if (n - 2 < NSU) pk = LS[(n - 2) * 64];
        v_unit(t, s, pk, sr, offn, psel);
        if (offn >= HEAD_BYTES) break;
        scur = snx;
        if (n >= NSU) {
          nxt = __builtin_amdgcn_readfirstlane(tk) - c0v;
          offn = nxt < NDYN ? pool_off(nxt) : (unsigned)OOB;
        } else {
          offn = sb + (unsigned)n * 65536u;
        }
      }
    }
    {
      const float lw0 = wave_sum(s.l0), lw1 = wave_sum(s.l1);
      *(LAS f32x4*)(LF + L_ACC + (sw * 64 + lane) * 4) = s.acc;
      if (lane == 0) { *(LAS f32x4*)(LF + L_ML + sw * 4) = f32x4{s.m0, lw0, s.m1, lw1}; L[L_SDONE] = 1u; }
      CLOBBER();
      const unsigned arr = __hip_atomic_fetch_add((LAS unsigned*)(lds_raw + (lane == 0 ? L_CNTS : L_DUMMY + lane)), 1u, __ATOMIC_RELAXED, __HIP_MEMORY_SCOPE_WORKGROUP);
      CLOBBER();
      f32x4 ml[4], ac[4];
#pragma unroll
      for (int i = 0; i < 4; ++i) { ml[i] = *(volatile LAS f32x4*)(LF + L_ML + i * 4); ac[i] = *(volatile LAS f32x4*)(LF + L_ACC + (i * 64 + lane) * 4); }
      if (__builtin_amdgcn_readfirstlane(arr) == 3u) {
        const float M0 = fmaxf(fmaxf(ml[0].x, ml[1].x), fmaxf(ml[2].x, ml[3].x)), M1 = fmaxf(fmaxf(ml[0].z, ml[1].z), fmaxf(ml[2].z, ml[3].z));
        float L0 = 0.f, L1 = 0.f;
        f32x4 o = {0.f, 0.f, 0.f, 0.f};
#pragma unroll
        for (int i = 0; i < 4; ++i) {
          const float w0 = __builtin_amdgcn_exp2f(ml[i].x - M0), w1 = __builtin_amdgcn_exp2f(ml[i].z - M1);
          L0 = fmaf(w0, ml[i].y, L0); L1 = fmaf(w1, ml[i].w, L1);
          o.x = fmaf(w0, ac[i].x, o.x); o.y = fmaf(w0, ac[i].y, o.y); o.z = fmaf(w1, ac[i].z, o.z); o.w = fmaf(w1, ac[i].w, o.w);
        }
        __builtin_amdgcn_raw_buffer_store_b128(v4u{asu(o.x), asu(L0), asu(o.y), ~asu(M0)}, rws, p1_off, 0, AUX_SC1);
        __builtin_amdgcn_raw_buffer_store_b128(v4u{asu(o.z), asu(L1), asu(o.w), ~asu(M1)}, rws, p1_off, P1_H1, AUX_SC1);
      }
    }
    if (b < NFC) {
      lds_wait<1>(L, L_FFR, 1u);
      const f32x4 gx = *(LAS f32x4*)(LF + L_G + 4 * (lane & 7));
      lds_wait<1>(L, L_YD, 3u);
      v4u u[8];
      poll_lines<8, true>(u, rws, P2_OFF + (b * NWG + 64 * sw) * 128 + lane * 16, T32);
      float y0 = 0.f, y1 = 0.f;
#pragma unroll
      for (int i = 0; i < 8; ++i) { y0 += asf(u[i].x); y1 += asf(u[i].z); }
      y0 += dpp<ROR8>(y0); y1 += dpp<ROR8>(y1);
      y0 = xrow_sum(y0); y1 = xrow_sum(y1);
      if (lane < 8) *(LAS f32x2*)(LF + L_FIN + sw * 16 + 2 * lane) = f32x2{y0, y1};
      CLOBBER();
      const unsigned arr = __hip_atomic_fetch_add((LAS unsigned*)(lds_raw + (lane == 0 ? L_CNTF : L_DUMMY + lane)), 1u, __ATOMIC_RELAXED, __HIP_MEMORY_SCOPE_WORKGROUP);
      CLOBBER();
      const int c = lane & 7;
      const f32x2 f0 = *(volatile LAS f32x2*)(LF + L_FIN + 2 * c), f1 = *(volatile LAS f32x2*)(LF + L_FIN + 16 + 2 * c),
                  f2 = *(volatile LAS f32x2*)(LF + L_FIN + 32 + 2 * c), f3 = *(volatile LAS f32x2*)(LF + L_FIN + 48 + 2 * c);
      if (__builtin_amdgcn_readfirstlane(arr) == 3u) {
        const float sv0 = rbf((f0.x + f1.x) + (f2.x + f3.x)), sv1 = rbf((f0.y + f1.y) + (f2.y + f3.y));
        const unsigned pk = cvtpk(gx.z + rbf(sv0 * gx.x), gx.w + rbf(sv1 * gx.y));
        if (lane < 8) *(unsigned*)(a.out + 32 * fl + 4 * lane + 2 * fh) = pk;
        if (b == 0 && lane == 0) {
          const u64 e = (((u64)L[L_TAG + 5] << 32) | L[L_TAG + 4]) * 6364136223846793005ull + 1442695040888963407ull;
          __builtin_amdgcn_raw_buffer_store_b64(v2u{(unsigned)e, (unsigned)(e >> 32)}, rws, E_OFF, 0, AUX_SC1);
        }
      }
    }
  } else if (w >= 1) {
    const int ta = w - 1, tt = tid - 64;
    __builtin_amdgcn_s_setprio(3);
    const int qs = ((b & 7) * 8 + (b >> 5)) * 4 + ((b >> 3) & 3);
    const __amdgpu_buffer_rsrc_t rx = __builtin_amdgcn_make_buffer_rsrc((void*)a.x, 0, D * 2, RSRC3);
    const __amdgpu_buffer_rsrc_t rq = __builtin_amdgcn_make_buffer_rsrc((void*)(a.wq + qs * 32), 0, (D - 1) * QN * 2 + 32, RSRC3);
    const int rr = lane >> 2, cc = lane & 3;
    v4u wqr[6];
    const v4u xc0 = __builtin_amdgcn_raw_buffer_load_b128(rx, lane * 16, 0, 0);
    const v4u xc1 = __builtin_amdgcn_raw_buffer_load_b128(rx, 1024 + lane * 16, 0, 0);
    unsigned xo[3];
#pragma unroll
    for (int i = 0; i < 3; ++i) xo[i] = __builtin_amdgcn_raw_buffer_load_b16(rx, (192 * ta + 64 * i + lane) * 2, 0, 0);
    {
      const int vo = (192 * ta + (lane >> 1)) * (QN * 2) + (lane & 1) * 16;
#pragma unroll
      for (int i = 0; i < 6; ++i) wqr[i] = __builtin_amdgcn_raw_buffer_load_b128(rq, vo, i * 32 * QN * 2, AUXQ);
    }
    WG_BARRIER();
    {
      const float x0 = bflo(__builtin_amdgcn_readfirstlane(xc0.x));
      float s0 = 0.f, s1 = 0.f, q0 = 0.f, q1 = 0.f;
#pragma unroll
      for (int i = 0; i < 4; ++i) {
        const float d0 = bflo(xc0[i]) - x0, d1 = bfhi(xc0[i]) - x0, d2 = bflo(xc1[i]) - x0, d3 = bfhi(xc1[i]) - x0;
        s0 += d0 + d1; s1 += d2 + d3;
        q0 = fmaf(d0, d0, q0); q0 = fmaf(d1, d1, q0); q1 = fmaf(d2, d2, q1); q1 = fmaf(d3, d3, q1);
      }
      const float dm = wave_sum(s0 + (lane < 8 ? s1 : 0.f)) * (1.0f / D);
      const float var = fmaxf(wave_sum(q0 + (lane < 8 ? q1 : 0.f)) * (1.0f / D) - dm * dm, 0.f);
      const float mb = rbf(x0 + dm), den = rbf(sqrtf(rbf(rbf(var) + 1e-5f)));
#pragma unroll
      for (int i = 0; i < 3; ++i) LF[L_XN + 192 * ta + 64 * i + lane] = rbf(rbf(bflo(xo[i]) - mb) / den);
      LGKM0();
    }
    {
      float acc[8];
#pragma unroll
      for (int e = 0; e < 8; ++e) acc[e] = 0.f;
#pragma unroll
      for (int i = 0; i < 6; ++i) {
        const float xn = LF[L_XN + 192 * ta + 32 * i + (lane >> 1)];
#pragma unroll
        for (int e = 0; e < 4; ++e) { acc[2 * e] = fmaf(xn, bflo(wqr[i][e]), acc[2 * e]); acc[2 * e + 1] = fmaf(xn, bfhi(wqr[i][e]), acc[2 * e + 1]); }
      }
      float u[4], v2[2];
#pragma unroll
      for (int e = 0; e < 4; ++e) u[e] = swap32_add(acc[e], acc[4 + e]);
#pragma unroll
      for (int e = 0; e < 2; ++e) v2[e] = swap16_add(u[e], u[2 + e]);
      const float t1 = v2[0] + dpp<ROR8>(v2[0]), t2 = v2[1] + dpp<ROR8>(v2[1]);
      float z = (lane & 8) ? t2 : t1;
      z += dpp<XOR2>(z);
      const float z1 = dpp<SHL4>(z), z2 = dpp<SHR4>(z);
      z += (lane & 4) ? z2 : z1;
      if ((lane & 6) == 0) LF[L_REDQ + ta * 16 + 8 * (lane & 1) + ((lane >> 3) & 7)] = z;
      LGKM0();
      if (lds_inc(L, L_CNTQ, lane) == 2u) {
        const float qv = (LF[L_REDQ + c16] + LF[L_REDQ + 16 + c16]) + LF[L_REDQ + 32 + c16];
        lds_wait<0>(L, L_TRDY, 1u);
        const unsigned tlo = lds_ld(L, L_TAG + 2), thi = lds_ld(L, L_TAG + 3);
        if (lane < 16) __builtin_amdgcn_raw_buffer_store_b64(v2u{(cvtpk(qv, qv) & 0xffffu) | (tlo << 16), thi}, rws, QG_OFF + (qs * 16 + lane) * 8, 0, AUX_SC1);
        CLOBBER();
        if (lane == 0) L[L_QPUB] = 1u;
      }
    }
    lds_wait<1>(L, L_QPUB, 1u);
    const int vo2 = tt < NCG ? tt * 8 : OOB;
    const int o2 = tt < NCG ? (tt & 7) * 16 : OOB;
    v2u wr[16];
    {
      const __amdgpu_buffer_rsrc_t ro = __builtin_amdgcn_make_buffer_rsrc((void*)(a.wo + (size_t)b * 16 * D * 2), 0, 16 * D * 2, RSRC3);
#pragma unroll
      for (int k = 0; k < 16; ++k) wr[k] = __builtin_amdgcn_raw_buffer_load_b64(ro, vo2, k * D * 2, AUXD);
      __builtin_amdgcn_sched_barrier(0);
    }
    if (is_ffn) {
      const __amdgpu_buffer_rsrc_t ra = __builtin_amdgcn_make_buffer_rsrc((void*)(a.wg + ft * 64), 0, D * FF * 2 - ft * 64, RSRC3);
      const __amdgpu_buffer_rsrc_t rb = __builtin_amdgcn_make_buffer_rsrc((void*)(a.wu + ft * 64), 0, D * FF * 2 - ft * 64, RSRC3);
      const __amdgpu_buffer_rsrc_t rd = __builtin_amdgcn_make_buffer_rsrc((void*)(a.wd + (size_t)ft * 32 * D * 2), 0, 32 * D * 2, RSRC3);
      const int vof = (192 * ta + rr) * (FF * 2) + cc * 16;
      constexpr int fstep = 16 * FF * 2;
      v4u wa[12], wb[12];
      v2u dn[32];
#pragma unroll
      for (int i = 0; i < 12; ++i) {
        wa[i] = __builtin_amdgcn_raw_buffer_load_b128(ra, vof, i * fstep, AUXG);
        wb[i] = __builtin_amdgcn_raw_buffer_load_b128(rb, vof, i * fstep, AUXG);
      }
#pragma unroll
      for (int k = 0; k < 32; ++k) dn[k] = __builtin_amdgcn_raw_buffer_load_b64(rd, vo2, k * D * 2, AUXD);
      __builtin_amdgcn_sched_barrier(0);
      float acc[16];
#pragma unroll
      for (int e = 0; e < 16; ++e) acc[e] = 0.f;
#pragma unroll
      for (int i = 0; i < 12; ++i) {
        const float xn = LF[L_XN + 192 * ta + 16 * i + rr];
#pragma unroll
        for (int e = 0; e < 4; ++e) {
          acc[2 * e] = fmaf(xn, bflo(wa[i][e]), acc[2 * e]); acc[2 * e + 1] = fmaf(xn, bfhi(wa[i][e]), acc[2 * e + 1]);
          acc[8 + 2 * e] = fmaf(xn, bflo(wb[i][e]), acc[8 + 2 * e]); acc[9 + 2 * e] = fmaf(xn, bfhi(wb[i][e]), acc[9 + 2 * e]);
        }
        asm volatile("" : "+v"(acc[0]), "+v"(acc[1]), "+v"(acc[2]), "+v"(acc[3]), "+v"(acc[4]), "+v"(acc[5]), "+v"(acc[6]), "+v"(acc[7]),
                          "+v"(acc[8]), "+v"(acc[9]), "+v"(acc[10]), "+v"(acc[11]), "+v"(acc[12]), "+v"(acc[13]), "+v"(acc[14]), "+v"(acc[15]));
      }
      float u[8], v4[4], z[2];
#pragma unroll
      for (int e = 0; e < 8; ++e) u[e] = swap32_add(acc[e], acc[8 + e]);
#pragma unroll
      for (int e = 0; e < 4; ++e) v4[e] = swap16_add(u[e], u[4 + e]);
#pragma unroll
      for (int e = 0; e < 2; ++e) { const float t1 = v4[e] + dpp<ROR8>(v4[e]), t2 = v4[2 + e] + dpp<ROR8>(v4[2 + e]); z[e] = (lane & 8) ? t2 : t1; }
      const float t1 = z[0] + dpp<SHL4>(z[0]), t2 = z[1] + dpp<SHR4>(z[1]);
      const float zz = (lane & 4) ? t2 : t1;
      LF[L_RED + ta * 64 + (lane & 32) + 8 * cc + ((lane >> 2) & 7)] = zz;
      LGKM0();
      lds_inc(L, L_CNTA, lane);
      lds_wait<0>(L, L_CNTA, 3u);
      const int u5 = lane & 31;
      const float gp = rbf((LF[L_RED + u5] + LF[L_RED + 64 + u5]) + LF[L_RED + 128 + u5]);
      const float up = rbf((LF[L_RED + 32 + u5] + LF[L_RED + 96 + u5]) + LF[L_RED + 160 + u5]);
      const float h = rbf(rbf(silu_f(gp)) * up);
      f32x4 y = {0.f, 0.f, 0.f, 0.f};
#pragma unroll
      for (int k = 0; k < 32; ++k) {
        const float hk = asf(__builtin_amdgcn_readlane(asu(h), k));
        y.x = fmaf(hk, bflo(dn[k].x), y.x); y.y = fmaf(hk, bfhi(dn[k].x), y.y); y.z = fmaf(hk, bflo(dn[k].y), y.z); y.w = fmaf(hk, bfhi(dn[k].y), y.w);
      }
      __builtin_amdgcn_raw_buffer_store_b128(__builtin_bit_cast(v4u, y), rws, tt < NCG ? FP_OFF + ft * (D * 4) + tt * 16 : OOB, 0, AUX_SC1);
    }
    DRAIN();
    lds_inc(L, L_CNTD, lane);
    float wf[64];
#pragma unroll
    for (int k = 0; k < 16; ++k) { wf[4 * k] = bflo(wr[k].x); wf[4 * k + 1] = bfhi(wr[k].x); wf[4 * k + 2] = bflo(wr[k].y); wf[4 * k + 3] = bfhi(wr[k].y); }
#pragma unroll
    for (int k = 0; k < 64; ++k) asm volatile("" : "+v"(wf[k]));
    lds_wait<8>(L, L_SDONE, 1u);
    const unsigned T32 = lds_ld(L, L_TAG);
    unsigned opk = 0;
#pragma nounroll
    for (unsigned sp = 0; sp < (SPIN_CAP << 4); ++sp) {
      const unsigned f = L[L_ORDY];
      opk = L[L_O + (lane & 7)];
      if (__builtin_amdgcn_readfirstlane(f)) break;
    }
    f32x4 y = {0.f, 0.f, 0.f, 0.f};
#pragma unroll
    for (int k = 0; k < 8; ++k) {
      const unsigned sp = __builtin_amdgcn_readlane(opk, k);
      const float e0 = bflo(sp), e1 = bfhi(sp);
      y.x = fmaf(e0, wf[8 * k], y.x); y.y = fmaf(e0, wf[8 * k + 1], y.y); y.z = fmaf(e0, wf[8 * k + 2], y.z); y.w = fmaf(e0, wf[8 * k + 3], y.w);
      y.x = fmaf(e1, wf[8 * k + 4], y.x); y.y = fmaf(e1, wf[8 * k + 5], y.y); y.z = fmaf(e1, wf[8 * k + 6], y.z); y.w = fmaf(e1, wf[8 * k + 7], y.w);
    }
    const int op = P2_OFF + ((tt >> 3) * NWG + b) * 128;
    __builtin_amdgcn_raw_buffer_store_b128(v4u{asu(y.x), T32, asu(y.y), T32}, rws, o2 + op, 0, AUX_SC1);
    __builtin_amdgcn_raw_buffer_store_b128(v4u{asu(y.z), T32, asu(y.w), T32}, rws, o2 + op, 18 * NWG * 128, AUX_SC1);
    lds_inc(L, L_YD, lane);
  } else {
    __builtin_amdgcn_s_setprio(3);
    const v2u ev = __builtin_amdgcn_raw_buffer_load_b64(rws, E_OFF, 0, AUX_SC1);
    const unsigned cv = __builtin_amdgcn_raw_buffer_load_b32(rws, C_OFF + g * 256, 0, AUX_SC1);
    const unsigned cv2 = __builtin_amdgcn_raw_buffer_load_b32(rws, C_OFF + g * 256 + 128, 0, AUX_SC1);
    if (lane < L_NCTL) L[lane] = 0u;
    LGKM0();
    WG_BARRIER();
    const unsigned elo = __builtin_amdgcn_readfirstlane(ev.x), ehi = __builtin_amdgcn_readfirstlane(ev.y);
    const unsigned qlo = (elo ^ 0x7F4A7C15u) & 0xffffu, qhi = ((elo ^ 0x7F4A7C15u) >> 16) | ((ehi ^ 0x9E3779B9u) << 16);
    const unsigned rlo = elo ^ 0xC2B2AE35u, rhi = ehi ^ 0x27D4EB2Fu;
    auto flags_ok = [&](const v4u& f) { return f.x == rlo && f.y == rhi && f.z == rlo && f.w == rhi; };
    if (lane == 0) { L[L_TAG] = (elo ^ 0x85EBCA6Bu) | 1u; L[L_TAG + 1] = cv; L[L_TAG + 6] = cv2; L[L_TAG + 2] = qlo; L[L_TAG + 3] = qhi; L[L_TAG + 4] = elo; L[L_TAG + 5] = ehi; }
    LGKM0();
    if (lane == 0) L[L_TRDY] = 1u;
    {
      const int oq = QG_OFF + g * 2048 + lane * 16;
      v4u q0 = {0u, ~qhi, 0u, 0u}, q1 = {0u, ~qhi, 0u, 0u};
#pragma nounroll
      for (unsigned sp = 0; sp < SPIN_CAP; ++sp) {
        CLOBBER();
        const bool ok0 = q0.y == qhi && q0.w == qhi && (q0.x >> 16) == qlo && (q0.z >> 16) == qlo;
        const bool ok1 = q1.y == qhi && q1.w == qhi && (q1.x >> 16) == qlo && (q1.z >> 16) == qlo;
        if (__all(ok0 && ok1)) break;
        const v4u n0 = __builtin_amdgcn_raw_buffer_load_b128(rws, ok0 ? OOB : oq, 0, AUX_SC1);
        const v4u n1 = __builtin_amdgcn_raw_buffer_load_b128(rws, ok1 ? OOB : oq + 1024, 0, AUX_SC1);
        q0 = ok0 ? q0 : n0; q1 = ok1 ? q1 : n1;
      }
      L[L_Q + lane] = (q0.x & 0xffffu) | (q0.z << 16);
      L[L_Q + 64 + lane] = (q1.x & 0xffffu) | (q1.z << 16);
      LGKM0();
      if (lane == 0) L[L_QRDY] = 1u;
    }
    {
      const v4u zero = {0u, 0u, 0u, 0u};
      __builtin_amdgcn_raw_buffer_store_b128(zero, rws, p1_off, 0, AUX_SC1);
      __builtin_amdgcn_raw_buffer_store_b128(zero, rws, p1_off, P1_H1, AUX_SC1);
#pragma unroll
      for (int i = 0; i < 5; ++i) {
        const int e = i * 8 + (lane >> 3);
        __builtin_amdgcn_raw_buffer_store_b128(zero, rws, e < NFC ? P2_OFF + (e * NWG + b) * 128 + (lane & 7) * 16 : OOB, 0, AUX_SC1);
      }
    }
    DRAIN();
    if (lane == 0) __builtin_amdgcn_raw_buffer_store_b64(v2u{rlo, rhi}, rws, RDY_OFF + b * 8, 0, AUX_SC1);
#pragma nounroll
    for (unsigned sp = 0; sp < SPIN_CAP; ++sp) {
      __builtin_amdgcn_s_sleep(30);
      CLOBBER();
      const v4u f0 = __builtin_amdgcn_raw_buffer_load_b128(rws, RDY_OFF + g * 128 + (lane & 7) * 16, 0, AUX_SC1);
      if (__all(flags_ok(f0))) break;
    }
    CLOBBER();
    if (lane == 0) L[L_GO] = 1u;
    if (is_ffn) {
      lds_wait<8>(L, L_CNTD, 3u);
      if (lane == 0) __builtin_amdgcn_raw_buffer_store_b64(v2u{rlo, rhi}, rws, RDF_OFF + ft * 8, 0, AUX_SC1);
    }
    if (b < NFC) {
#pragma nounroll
      for (unsigned sp = 0; sp < SPIN_CAP; ++sp) {
        __builtin_amdgcn_s_sleep(127);
        CLOBBER();
        const v4u f0 = __builtin_amdgcn_raw_buffer_load_b128(rws, RDF_OFF + lane * 16, 0, AUX_SC1);
        const v4u f1 = __builtin_amdgcn_raw_buffer_load_b128(rws, RDF_OFF + 1024 + (lane & 7) * 16, 0, AUX_SC1);
        const v4u f2 = __builtin_amdgcn_raw_buffer_load_b128(rws, RDY_OFF + lane * 16, 0, AUX_SC1);
        const v4u f3 = __builtin_amdgcn_raw_buffer_load_b128(rws, RDY_OFF + 1024 + lane * 16, 0, AUX_SC1);
        if (__all(flags_ok(f0) && flags_ok(f1) && flags_ok(f2) && flags_ok(f3))) break;
      }
      CLOBBER();
      const int of = FP_OFF + (lane >> 3) * (D * 4) + fl * 128 + (lane & 7) * 16 + fh * 8;
      v2u f[18];
#pragma unroll
      for (int i = 0; i < 18; ++i) f[i] = __builtin_amdgcn_raw_buffer_load_b64(rws, of, i * 8 * D * 4, AUX_SC1);
      const unsigned xv = *(const unsigned*)(a.x + 32 * fl + 4 * (lane & 7) + 2 * fh);
      float y0 = 0.f, y1 = 0.f;
#pragma unroll
      for (int i = 0; i < 18; ++i) { y0 += asf(f[i].x); y1 += asf(f[i].y); }
      y0 += dpp<ROR8>(y0); y1 += dpp<ROR8>(y1);
      y0 = xrow_sum(y0); y1 = xrow_sum(y1);
      if (lane < 8) *(LAS f32x4*)(LF + L_G + 4 * lane) = f32x4{rbf(silu_f(rbf(y0))), rbf(silu_f(rbf(y1))), bflo(xv), bfhi(xv)};
      LGKM0();
      if (lane == 0) L[L_FFR] = 1u;
    }
    lds_wait<16>(L, L_CNTK, 4u);
    if (lane == 0) __builtin_amdgcn_raw_buffer_store_b64(v2u{rlo, rhi}, rws, RDK_OFF + b * 8, 0, AUX_SC1);
#pragma nounroll
    for (unsigned sp = 0; sp < SPIN_CAP; ++sp) {
      __builtin_amdgcn_s_sleep(60);
      CLOBBER();
      const v4u f0 = __builtin_amdgcn_raw_buffer_load_b128(rws, RDK_OFF + g * 128 + (lane & 7) * 16, 0, AUX_SC1);
      if (__all(flags_ok(f0))) break;
    }
    CLOBBER();
    if (lane == 0) L[L_VGO] = 1u;
    lds_wait<4>(L, L_SDONE, 1u);
    lds_wait<1>(L, L_CNTS, 4u);
    {
      v4u p[2];
      poll_lines<2, false>(p, rws, P1_OFF + (g * 16 + j) * 2048 + lane * 16, 0u);
      float M = fmaxf(asf(~p[0].w), asf(~p[1].w));
      M = fmaxf(M, dpp<ROR8>(M)); M = xrow_max(M);
      float Ls = 0.f, o0 = 0.f, o1 = 0.f;
#pragma unroll
      for (int i = 0; i < 2; ++i) {
        const float wt = __builtin_amdgcn_exp2f(asf(~p[i].w) - M);
        Ls = fmaf(wt, asf(p[i].y), Ls); o0 = fmaf(wt, asf(p[i].x), o0); o1 = fmaf(wt, asf(p[i].z), o1);
      }
      Ls += dpp<ROR8>(Ls); o0 += dpp<ROR8>(o0); o1 += dpp<ROR8>(o1);
      Ls = xrow_sum(Ls); o0 = xrow_sum(o0); o1 = xrow_sum(o1);
      const unsigned opk = cvtpk(o0 / Ls, o1 / Ls);
      if (lane < 8) L[L_O + lane] = opk;
      CLOBBER();
      if (lane == 0) L[L_ORDY] = 1u;
    }
  }
}

}

extern "C" void kernel_launch(void* const* d_in, const int* in_sizes, int n_in, void* d_out, int out_size, void* d_ws, size_t ws_size,
                              hipStream_t stream) {
  Args a;
  a.x = (const unsigned short*)d_in[0];
  a.k = (const unsigned char*)d_in[1];
  a.v = (const unsigned char*)d_in[2];
  a.wq = (const unsigned char*)d_in[3];
  a.wo = (const unsigned char*)d_in[4];
  a.wg = (const unsigned char*)d_in[6];
  a.wu = (const unsigned char*)d_in[7];
  a.wd = (const unsigned char*)d_in[8];
  a.out = (unsigned short*)d_out;
  a.ws = (unsigned char*)d_ws;
  hipLaunchKernelGGL(fused_decode_kernel, dim3(NWG), dim3(NTHR), 0, stream, a);
}
```

```cpp
#include <hip/hip_runtime.h>
#include <hip/hip_bf16.h>
#include <stdint.h>

namespace {

typedef unsigned v4u __attribute__((ext_vector_type(4)));
typedef unsigned v2u __attribute__((ext_vector_type(2)));
typedef float f32x4 __attribute__((ext_vector_type(4)));
typedef float f32x2 __attribute__((ext_vector_type(2)));
typedef __bf16 bf16x8 __attribute__((ext_vector_type(8)));
typedef __bf16 bf16x2 __attribute__((ext_vector_type(2)));
typedef unsigned long long u64;
#define LAS __attribute__((address_space(3)))

constexpr int D = 576, SK = 65536, DH = 128, FF = 4608, QN = 4096;
constexpr int NWG = 256, NTHR = 512;
constexpr int NU = SK / 64;
constexpr int NSU = 12;
constexpr int DFW = 8;
constexpr unsigned NDYN = NU - NSU * 64;
static_assert(NSU >= 4 && NSU < 16 && (64 - 4 * NSU) % DFW == 0, "");
constexpr unsigned HEAD_BYTES = (unsigned)SK * DH * 2;
constexpr int NCG = D / 4;
constexpr int NFC = 36;
constexpr int NFT = FF / 32;
constexpr int OOB = 0x7f000000;

constexpr int E_OFF = 0;
constexpr int C_OFF = 256;
constexpr int RDY_OFF = 8192;
constexpr int RDK_OFF = 10240;
constexpr int RDF_OFF = 12288;
constexpr int QG_OFF = 16384;
constexpr int P1_OFF = 65536;
constexpr int P2_OFF = P1_OFF + 16 * 16 * 16 * 128;
constexpr int FP_OFF = P2_OFF + NFC * NWG * 128;
constexpr int SC_OFF = FP_OFF + NFT * D * 4;
constexpr int WS_BYTES = SC_OFF + 16 * NDYN * 256;

constexpr int AUX_NT = 2, AUX_SC1 = 16;
constexpr int AUXQ = 0, AUXG = 0, AUXD = AUX_NT;
constexpr unsigned RSRC3 = 0x00020000u;
constexpr unsigned SPIN_CAP = 1u << 17;

constexpr int L_QRDY = 0, L_TRDY = 1, L_CNTA = 2, L_CNTQ = 3, L_CNTS = 4, L_CNTF = 5, L_GO = 6, L_FFR = 7, L_YD = 9, L_QPUB = 10, L_ORDY = 11, L_CNTD = 12, L_CNTK = 13, L_VGO = 14;
constexpr int L_NCTL = 16;
constexpr int L_TAG = 16;
constexpr int L_Q = 32;
constexpr int L_RED = 192;
constexpr int L_O = 384;
constexpr int L_REDQ = 400;
constexpr int L_G = 448;
constexpr int L_FIN = 512;
constexpr int L_XN = 576;
constexpr int L_ML = 1152;
constexpr int L_ACC = 1280;
constexpr int L_DUMMY = 3072;
constexpr int L_SC = 4096;
constexpr int L_TOTAL = 24 * 1024;

constexpr float SCALE = 0.08838834764831845f;
constexpr float LOG2E = 1.4426950408889634f;

__device__ __forceinline__ unsigned cvtpk(float lo, float hi) {
  f32x2 v = {lo, hi};
  return __builtin_bit_cast(unsigned, __builtin_convertvector(v, bf16x2));
}
__device__ __forceinline__ float bflo(unsigned u) { return __builtin_bit_cast(float, u << 16); }
__device__ __forceinline__ float bfhi(unsigned u) { return __builtin_bit_cast(float, u & 0xffff0000u); }
__device__ __forceinline__ float rbf(float x) { return bflo(cvtpk(x, x)); }
__device__ __forceinline__ float asf(unsigned u) { return __builtin_bit_cast(float, u); }
__device__ __forceinline__ unsigned asu(float f) { return __builtin_bit_cast(unsigned, f); }

template <int CTRL> __device__ __forceinline__ float dpp(float x) {
  return __builtin_bit_cast(float, __builtin_amdgcn_mov_dpp(__builtin_bit_cast(int, x), CTRL, 0xf, 0xf, true));
}
constexpr int XOR1 = 0xB1, XOR2 = 0x4E, HMIR = 0x141, ROR8 = 0x128, SHL4 = 0x104, SHR4 = 0x114;

__device__ __forceinline__ float xrow_max(float x) {
  auto s = __builtin_amdgcn_permlane16_swap(asu(x), asu(x), false, false);
  x = fmaxf(asf(s[0]), asf(s[1]));
  auto t = __builtin_amdgcn_permlane32_swap(asu(x), asu(x), false, false);
  return fmaxf(asf(t[0]), asf(t[1]));
}
__device__ __forceinline__ float xrow_sum(float x) {
  auto s = __builtin_amdgcn_permlane16_swap(asu(x), asu(x), false, false);
  x = asf(s[0]) + asf(s[1]);
  auto t = __builtin_amdgcn_permlane32_swap(asu(x), asu(x), false, false);
  return asf(t[0]) + asf(t[1]);
}
__device__ __forceinline__ float wave_sum(float x) {
  x += dpp<XOR1>(x); x += dpp<XOR2>(x); x += dpp<HMIR>(x); x += dpp<ROR8>(x);
  return xrow_sum(x);
}
__device__ __forceinline__ float swap32_add(float a, float b) {
  auto s = __builtin_amdgcn_permlane32_swap(asu(a), asu(b), false, false);
  return asf(s[0]) + asf(s[1]);
}
__device__ __forceinline__ float swap16_add(float a, float b) {
  auto s = __builtin_amdgcn_permlane16_swap(asu(a), asu(b), false, false);
  return asf(s[0]) + asf(s[1]);
}
__device__ __forceinline__ f32x4 mfma(v4u a, v4u b, f32x4 c) {
  return __builtin_amdgcn_mfma_f32_16x16x32_bf16(__builtin_bit_cast(bf16x8, a), __builtin_bit_cast(bf16x8, b), c, 0, 0, 0);
}
__device__ __forceinline__ float dot2(unsigned a, unsigned b, float c) {
  return __builtin_amdgcn_fdot2_f32_bf16(__builtin_bit_cast(bf16x2, a), __builtin_bit_cast(bf16x2, b), c, false);
}
__device__ __forceinline__ float silu_f(float x) { return x / (1.0f + __expf(-x)); }

#define WG_BARRIER() do { __builtin_amdgcn_sched_barrier(0); __builtin_amdgcn_s_barrier(); __builtin_amdgcn_sched_barrier(0); } while (0)
#define CLOBBER() asm volatile("" ::: "memory")
#define LGKM0() asm volatile("s_waitcnt lgkmcnt(0)" ::: "memory")
#define DRAIN() asm volatile("s_waitcnt vmcnt(0)" ::: "memory")

__device__ __forceinline__ unsigned lds_ld(volatile LAS unsigned* L, int i) { return __builtin_amdgcn_readfirstlane(L[i]); }
template <int SLEEP> __device__ __forceinline__ void lds_wait(volatile LAS unsigned* L, int i, unsigned want) {
#pragma nounroll
  for (unsigned s = 0; s < (SPIN_CAP << 3); ++s) {
    if (lds_ld(L, i) >= want) break;
    __builtin_amdgcn_s_sleep(SLEEP);
  }
  CLOBBER();
}
__device__ __forceinline__ unsigned lds_inc(volatile LAS unsigned* L, int i, int lane) {
  unsigned r = 0;
  if (lane == 0) r = __hip_atomic_fetch_add((LAS unsigned*)(L + i), 1u, __ATOMIC_RELAXED, __HIP_MEMORY_SCOPE_WORKGROUP);
  r = __builtin_amdgcn_readfirstlane(r);
  CLOBBER();
  return r;
}

struct Args {
  const unsigned short* x;
  const unsigned char* k;
  const unsigned char* v;
  const unsigned char* wq;
  const unsigned char* wo;
  const unsigned char* wg;
  const unsigned char* wu;
  const unsigned char* wd;
  unsigned short* out;
  unsigned char* ws;
};

#define KOFF(i) ((((i) >> 1) & 1) * 1024 + ((i) & 1) * 128)
#define LOAD_K(i, off) ldkv(rk, voffk + KOFF((i) & 3), (off) + ((i) >> 2) * 4096)
#define LOAD_V(i, off) ldkv(rv, voffv + ((i) & 3) * 1024, (off) + ((i) >> 2) * 4096)
struct Src { __amdgpu_buffer_rsrc_t rk, rv; int voffk, voffv; };

struct SState {
  f32x4 acc;
  float m0, m1, l0, l1;
  v4u fa, fb;
};

template <int QD> __device__ __forceinline__ void build_frag(v4u& f, unsigned pb) {
  constexpr int r0 = (0 - QD) & 3, r1 = (1 - QD) & 3, r2 = (2 - QD) & 3, r3 = (3 - QD) & 3;
  f.x = __builtin_amdgcn_update_dpp(f.x, pb, r0 ? 0x120 + 4 * r0 : 0xE4, 0xf, 0x1, false);
  f.y = __builtin_amdgcn_update_dpp(f.y, pb, r1 ? 0x120 + 4 * r1 : 0xE4, 0xf, 0x2, false);
  f.z = __builtin_amdgcn_update_dpp(f.z, pb, r2 ? 0x120 + 4 * r2 : 0xE4, 0xf, 0x4, false);
  f.w = __builtin_amdgcn_update_dpp(f.w, pb, r3 ? 0x120 + 4 * r3 : 0xE4, 0xf, 0x8, false);
}

__device__ __forceinline__ v4u ldkv(__amdgpu_buffer_rsrc_t r, int voff, unsigned soff) {
  return __builtin_amdgcn_raw_buffer_load_b128(r, voff, soff, AUX_NT);
}

template <bool NEXT_V>
__device__ __forceinline__ unsigned k_unit(v4u (&t)[16], const v4u (&qf)[4], const Src& sr, unsigned offn, int lane) {
  const __amdgpu_buffer_rsrc_t rk = sr.rk, rv = sr.rv;
  const int voffk = sr.voffk, voffv = sr.voffv;
  float x[8][2];
#pragma unroll
  for (int u = 0; u < 8; ++u) {
    float a0 = 0.f, a1 = 0.f;
#pragma unroll
    for (int hf = 0; hf < 2; ++hf) {
#pragma unroll
      for (int e = 0; e < 4; ++e) { a0 = dot2(t[2 * u + hf][e], qf[hf][e], a0); a1 = dot2(t[2 * u + hf][e], qf[2 + hf][e], a1); }
      asm volatile("" : "+v"(a0), "+v"(a1));
      t[2 * u + hf] = NEXT_V ? LOAD_V(2 * u + hf, offn) : LOAD_K(2 * u + hf, offn);
      __builtin_amdgcn_sched_barrier(0);
    }
    x[u][0] = a0; x[u][1] = a1;
  }
  float sc[2];
#pragma unroll
  for (int h = 0; h < 2; ++h) {
    float y[4], z[2];
#pragma unroll
    for (int v = 0; v < 4; ++v) {
      const float t1 = x[2 * v][h] + dpp<HMIR>(x[2 * v][h]), t2 = x[2 * v + 1][h] + dpp<HMIR>(x[2 * v + 1][h]);
      y[v] = asf(__builtin_amdgcn_update_dpp(asu(t1), asu(t2), 0xE4, 0xf, 0xA, false));
    }
#pragma unroll
    for (int v = 0; v < 2; ++v) { const float t1 = y[v] + dpp<XOR2>(y[v]), t2 = y[2 + v] + dpp<XOR2>(y[2 + v]); z[v] = (lane & 2) ? t2 : t1; }
    const float t1 = z[0] + dpp<XOR1>(z[0]), t2 = z[1] + dpp<XOR1>(z[1]);
    sc[h] = (lane & 1) ? t2 : t1;
  }
  const unsigned pk = cvtpk(sc[0], sc[1]);
  return cvtpk(bflo(pk) * SCALE, bfhi(pk) * SCALE);
}

__device__ __forceinline__ float wave_max(float x) {
  x = fmaxf(x, dpp<XOR1>(x)); x = fmaxf(x, dpp<XOR2>(x)); x = fmaxf(x, dpp<HMIR>(x)); x = fmaxf(x, dpp<ROR8>(x));
  return xrow_max(x);
}

__device__ __forceinline__ void v_unit(v4u (&t)[16], SState& s, unsigned pk, const Src& sr, unsigned offn, unsigned psel) {
  const __amdgpu_buffer_rsrc_t rv = sr.rv;
  const int voffv = sr.voffv;
  const float s0 = bflo(pk) * LOG2E, s1 = bfhi(pk) * LOG2E;
  if (!__all(s0 <= s.m0 && s1 <= s.m1)) {
    const float n0 = fmaxf(s.m0, ceilf(wave_max(s0))), n1 = fmaxf(s.m1, ceilf(wave_max(s1)));
    const float a0 = __builtin_amdgcn_exp2f(s.m0 - n0), a1 = __builtin_amdgcn_exp2f(s.m1 - n1);
    s.acc.x *= a0; s.acc.y *= a0; s.acc.z *= a1; s.acc.w *= a1;
    s.l0 *= a0; s.l1 *= a1;
    s.m0 = n0; s.m1 = n1;
  }
  const float e0 = __builtin_amdgcn_exp2f(s0 - s.m0), e1 = __builtin_amdgcn_exp2f(s1 - s.m1);
  s.l0 += e0; s.l1 += e1;
  const float pe = asf(cvtpk(e0, e1));
#define PV_STEP(IDX, F, QD) \
  build_frag<QD>(F, pb); \
  s.acc = mfma(F, t[IDX], s.acc); \
  t[IDX] = LOAD_V(IDX, offn); \
  __builtin_amdgcn_sched_barrier(0);
#define PV_GROUP(V) { \
    const unsigned pb = __builtin_amdgcn_perm(0u, asu(dpp<(V) * 0x55>(pe)), psel);       \
    PV_STEP(4 * (V), s.fa, 0) PV_STEP(4 * (V) + 1, s.fb, 1) PV_STEP(4 * (V) + 2, s.fa, 2) PV_STEP(4 * (V) + 3, s.fb, 3) }
  PV_GROUP(0) PV_GROUP(1) PV_GROUP(2) PV_GROUP(3)
#undef PV_GROUP
#undef PV_STEP
}

template <int N, bool TAGGED> __device__ __forceinline__ void poll_lines(v4u (&u)[N], __amdgpu_buffer_rsrc_t rws, int off, unsigned tag) {
#pragma unroll
  for (int i = 0; i < N; ++i) u[i] = v4u{0u, 0u, 0u, 0u};
#pragma nounroll
  for (unsigned sp = 0; sp < SPIN_CAP; ++sp) {
    CLOBBER();
    bool ok[N], all = true;
#pragma unroll
    for (int i = 0; i < N; ++i) { ok[i] = TAGGED ? (u[i].y == tag && u[i].w == tag) : (u[i].y != 0u && u[i].w != 0u); all = all && ok[i]; }
    if (__all(all)) break;
    v4u nw[N];
#pragma unroll
    for (int i = 0; i < N; ++i) nw[i] = __builtin_amdgcn_raw_buffer_load_b128(rws, ok[i] ? OOB : off, i * 1024, AUX_SC1);
#pragma unroll
    for (int i = 0; i < N; ++i) u[i] = ok[i] ? u[i] : nw[i];
  }
}

__global__ void __launch_bounds__(NTHR, 2) fused_decode_kernel(Args a) {
  __shared__ __attribute__((aligned(16))) unsigned lds_raw[L_TOTAL];
  volatile LAS unsigned* L = (volatile LAS unsigned*)lds_raw;
  LAS float* LF = (LAS float*)lds_raw;
  const int tid = threadIdx.x, lane = tid & 63, w = __builtin_amdgcn_readfirstlane(tid >> 6);
  const int b = blockIdx.x, g = b >> 4, j = b & 15;
  const __amdgpu_buffer_rsrc_t rws = __builtin_amdgcn_make_buffer_rsrc((void*)a.ws, 0, WS_BYTES, RSRC3);
  const int r4 = lane >> 4, c16 = lane & 15;
  const int p1_off = P1_OFF + ((g * 16 + (c16 >> 1)) * 16 + j) * 128 + (8 * (c16 & 1) + 2 * r4) * 8;
  constexpr int P1_H1 = 8 * 16 * 128;
  const int slot = ((j & 7) - (g >> 1)) & 7, hi = j >> 3;
  const bool is_ffn = slot < 4 || (slot == 4 && hi == 0);
  const int ft = 18 * (g >> 1) + (slot < 4 ? (g & 1) * 8 + 2 * slot + hi : 16 + (g & 1));
  const int fh = b >= 18 ? 1 : 0, fl = b - 18 * fh;

  if (w >= 4) {
    const int sw = w - 4;
    Src sr;
    sr.rk = __builtin_amdgcn_make_buffer_rsrc((void*)(a.k + (size_t)g * HEAD_BYTES), 0, HEAD_BYTES, RSRC3);
    sr.rv = __builtin_amdgcn_make_buffer_rsrc((void*)(a.v + (size_t)g * HEAD_BYTES), 0, HEAD_BYTES, RSRC3);
    sr.voffk = (8 * (c16 >> 3) + r4) * 256 + (lane & 7) * 16;
    sr.voffv = lane * 16;
    const __amdgpu_buffer_rsrc_t rck = __builtin_amdgcn_make_buffer_rsrc((void*)(a.ws + C_OFF + g * 256), 0, 4, RSRC3);
    const __amdgpu_buffer_rsrc_t rcv = __builtin_amdgcn_make_buffer_rsrc((void*)(a.ws + C_OFF + g * 256 + 128), 0, 4, RSRC3);
    const __amdgpu_buffer_rsrc_t rsc = __builtin_amdgcn_make_buffer_rsrc((void*)(a.ws + SC_OFF + g * (NDYN * 256)), 0, NDYN * 256, RSRC3);
    const unsigned sb = (unsigned)(j * 64 + sw) * 16384u;
    auto pool_off = [](unsigned i) { return ((i / DFW) & 15u) * 1048576u + (4 * NSU + (i / (16 * DFW)) * DFW + i % DFW) * 16384u; };
    v4u t[16];
    WG_BARRIER();
    {
      const __amdgpu_buffer_rsrc_t rk = sr.rk;
      const int voffk = sr.voffk;
#pragma unroll
      for (int i = 0; i < 16; ++i) t[i] = LOAD_K(i, sb);
      __builtin_amdgcn_sched_barrier(0);
    }
    lds_wait<1>(L, L_QRDY, 1u);
    v4u qf[4];
#pragma unroll
    for (int i = 0; i < 4; ++i) qf[i] = *(LAS v4u*)(lds_raw + L_Q + 32 * i + 4 * (lane & 7));
    const unsigned T32 = lds_ld(L, L_TAG), c0k = lds_ld(L, L_TAG + 1), c0v = lds_ld(L, L_TAG + 6);
    volatile LAS unsigned* LS = L + L_SC + sw * (NSU * 64) + lane;
    {
      unsigned cur = 0, nxt = 0;
      unsigned offn = sb + 65536u;
#pragma nounroll
      for (int n = 2;; ++n) {
        if (n == NSU) lds_wait<1>(L, L_GO, 1u);
        const unsigned tk = __builtin_amdgcn_raw_ptr_buffer_atomic_add_i32(1, rck, lane * 4, n >= NSU ? 0 : 4, 0);
        const unsigned pk = k_unit<false>(t, qf, sr, offn, lane);
        if (n - 2 < NSU) LS[(n - 2) * 64] = pk;
        else __builtin_amdgcn_raw_buffer_store_b32(pk, rsc, lane * 4, cur * 256u, AUX_SC1);
        cur = nxt;
        if (n >= NSU) {
          nxt = __builtin_amdgcn_readfirstlane(tk) - c0k;
          if (nxt >= NDYN) {
            const unsigned pl = k_unit<true>(t, qf, sr, sb, lane);
            if (n - 1 < NSU) LS[(n - 1) * 64] = pl;
            else __builtin_amdgcn_raw_buffer_store_b32(pl, rsc, lane * 4, cur * 256u, AUX_SC1);
            break;
          }
          offn = pool_off(nxt);
        } else {
          offn = sb + (unsigned)n * 65536u;
        }
      }
    }
    SState s;
    s.acc = f32x4{0.f, 0.f, 0.f, 0.f};
    s.m0 = -INFINITY; s.m1 = -INFINITY; s.l0 = 0.f; s.l1 = 0.f;
    s.fa = v4u{0u, 0u, 0u, 0u}; s.fb = v4u{0u, 0u, 0u, 0u};
    {
      const unsigned psel = (lane & 3) == 0 ? 0x0c0c0100u : (lane & 3) == 1 ? 0x01000c0cu : (lane & 3) == 2 ? 0x0c0c0302u : 0x03020c0cu;
      unsigned offn = sb + 65536u, nxt = 0, scur = 0;
#pragma nounroll
      for (int n = 2;; ++n) {
        if (n == 3) {
          asm volatile("s_waitcnt vmcnt(16)" ::: "memory");
          lds_inc(L, L_CNTK, lane);
        }
        if (n == NSU + 1) lds_wait<1>(L, L_VGO, 1u);
        const unsigned tk = __builtin_amdgcn_raw_ptr_buffer_atomic_add_i32(1, rcv, lane * 4, (n >= NSU && offn < HEAD_BYTES) ? 0 : 4, 0);
        const unsigned snx = __builtin_amdgcn_raw_buffer_load_b32(rsc, lane * 4, (n - 1 >= NSU && offn < HEAD_BYTES) ? nxt * 256u : (unsigned)OOB, AUX_SC1);
        unsigned pk = scur;
        if (n - 2 < NSU) pk = LS[(n - 2) * 64];
        v_unit(t, s, pk, sr, offn, psel);
        if (offn >= HEAD_BYTES) break;
        scur = snx;
        if (n >= NSU) {
          nxt = __builtin_amdgcn_readfirstlane(tk) - c0v;
          offn = nxt < NDYN ? pool_off(nxt) : (unsigned)OOB;
        } else {
          offn = sb + (unsigned)n * 65536u;
        }
      }
    }
    {
      const float lw0 = wave_sum(s.l0), lw1 = wave_sum(s.l1);
      *(LAS f32x4*)(LF + L_ACC + (sw * 64 + lane) * 4) = s.acc;
      if (lane == 0) *(LAS f32x4*)(LF + L_ML + sw * 4) = f32x4{s.m0, lw0, s.m1, lw1};
      CLOBBER();
      const unsigned arr = __hip_atomic_fetch_add((LAS unsigned*)(lds_raw + (lane == 0 ? L_CNTS : L_DUMMY + lane)), 1u, __ATOMIC_RELAXED, __HIP_MEMORY_SCOPE_WORKGROUP);
      CLOBBER();
      f32x4 ml[4], ac[4];
#pragma unroll
      for (int i = 0; i < 4; ++i) { ml[i] = *(volatile LAS f32x4*)(LF + L_ML + i * 4); ac[i] = *(volatile LAS f32x4*)(LF + L_ACC + (i * 64 + lane) * 4); }
      if (__builtin_amdgcn_readfirstlane(arr) == 3u) {
        const float M0 = fmaxf(fmaxf(ml[0].x, ml[1].x), fmaxf(ml[2].x, ml[3].x)), M1 = fmaxf(fmaxf(ml[0].z, ml[1].z), fmaxf(ml[2].z, ml[3].z));
        float L0 = 0.f, L1 = 0.f;
        f32x4 o = {0.f, 0.f, 0.f, 0.f};
#pragma unroll
        for (int i = 0; i < 4; ++i) {
          const float w0 = __builtin_amdgcn_exp2f(ml[i].x - M0), w1 = __builtin_amdgcn_exp2f(ml[i].z - M1);
          L0 = fmaf(w0, ml[i].y, L0); L1 = fmaf(w1, ml[i].w, L1);
          o.x = fmaf(w0, ac[i].x, o.x); o.y = fmaf(w0, ac[i].y, o.y); o.z = fmaf(w1, ac[i].z, o.z); o.w = fmaf(w1, ac[i].w, o.w);
        }
        __builtin_amdgcn_raw_buffer_store_b128(v4u{asu(o.x), asu(L0), asu(o.y), ~asu(M0)}, rws, p1_off, 0, AUX_SC1);
        __builtin_amdgcn_raw_buffer_store_b128(v4u{asu(o.z), asu(L1), asu(o.w), ~asu(M1)}, rws, p1_off, P1_H1, AUX_SC1);
      }
    }
    if (b < NFC) {
      lds_wait<1>(L, L_FFR, 1u);
      const f32x4 gx = *(LAS f32x4*)(LF + L_G + 4 * (lane & 7));
      lds_wait<1>(L, L_YD, 3u);
      v4u u[8];
      poll_lines<8, true>(u, rws, P2_OFF + (b * NWG + 64 * sw) * 128 + lane * 16, T32);
      float y0 = 0.f, y1 = 0.f;
#pragma unroll
      for (int i = 0; i < 8; ++i) { y0 += asf(u[i].x); y1 += asf(u[i].z); }
      y0 += dpp<ROR8>(y0); y1 += dpp<ROR8>(y1);
      y0 = xrow_sum(y0); y1 = xrow_sum(y1);
      if (lane < 8) *(LAS f32x2*)(LF + L_FIN + sw * 16 + 2 * lane) = f32x2{y0, y1};
      CLOBBER();
      const unsigned arr = __hip_atomic_fetch_add((LAS unsigned*)(lds_raw + (lane == 0 ? L_CNTF : L_DUMMY + lane)), 1u, __ATOMIC_RELAXED, __HIP_MEMORY_SCOPE_WORKGROUP);
      CLOBBER();
      const int c = lane & 7;
      const f32x2 f0 = *(volatile LAS f32x2*)(LF + L_FIN + 2 * c), f1 = *(volatile LAS f32x2*)(LF + L_FIN + 16 + 2 * c),
                  f2 = *(volatile LAS f32x2*)(LF + L_FIN + 32 + 2 * c), f3 = *(volatile LAS f32x2*)(LF + L_FIN + 48 + 2 * c);
      if (__builtin_amdgcn_readfirstlane(arr) == 3u) {
        const float sv0 = rbf((f0.x + f1.x) + (f2.x + f3.x)), sv1 = rbf((f0.y + f1.y) + (f2.y + f3.y));
        const unsigned pk = cvtpk(gx.z + rbf(sv0 * gx.x), gx.w + rbf(sv1 * gx.y));
        if (lane < 8) *(unsigned*)(a.out + 32 * fl + 4 * lane + 2 * fh) = pk;
        if (b == 0 && lane == 0) {
          const u64 e = (((u64)L[L_TAG + 5] << 32) | L[L_TAG + 4]) * 6364136223846793005ull + 1442695040888963407ull;
          __builtin_amdgcn_raw_buffer_store_b64(v2u{(unsigned)e, (unsigned)(e >> 32)}, rws, E_OFF, 0, AUX_SC1);
        }
      }
    }
  } else if (w >= 1) {
    const int ta = w - 1, tt = tid - 64;
    __builtin_amdgcn_s_setprio(3);
    const int qs = ((b & 7) * 8 + (b >> 5)) * 4 + ((b >> 3) & 3);
    const __amdgpu_buffer_rsrc_t rx = __builtin_amdgcn_make_buffer_rsrc((void*)a.x, 0, D * 2, RSRC3);
    const __amdgpu_buffer_rsrc_t rq = __builtin_amdgcn_make_buffer_rsrc((void*)(a.wq + qs * 32), 0, (D - 1) * QN * 2 + 32, RSRC3);
    const int rr = lane >> 2, cc = lane & 3;
    v4u wqr[6];
    const v4u xc0 = __builtin_amdgcn_raw_buffer_load_b128(rx, lane * 16, 0, 0);
    const v4u xc1 = __builtin_amdgcn_raw_buffer_load_b128(rx, 1024 + lane * 16, 0, 0);
    unsigned xo[3];
#pragma unroll
    for (int i = 0; i < 3; ++i) xo[i] = __builtin_amdgcn_raw_buffer_load_b16(rx, (192 * ta + 64 * i + lane) * 2, 0, 0);
    {
      const int vo = (192 * ta + (lane >> 1)) * (QN * 2) + (lane & 1) * 16;
#pragma unroll
      for (int i = 0; i < 6; ++i) wqr[i] = __builtin_amdgcn_raw_buffer_load_b128(rq, vo, i * 32 * QN * 2, AUXQ);
    }
    WG_BARRIER();
    {
      const float x0 = bflo(__builtin_amdgcn_readfirstlane(xc0.x));
      float s0 = 0.f, s1 = 0.f, q0 = 0.f, q1 = 0.f;
#pragma unroll
      for (int i = 0; i < 4; ++i) {
        const float d0 = bflo(xc0[i]) - x0, d1 = bfhi(xc0[i]) - x0, d2 = bflo(xc1[i]) - x0, d3 = bfhi(xc1[i]) - x0;
        s0 += d0 + d1; s1 += d2 + d3;
        q0 = fmaf(d0, d0, q0); q0 = fmaf(d1, d1, q0); q1 = fmaf(d2, d2, q1); q1 = fmaf(d3, d3, q1);
      }
      const float dm = wave_sum(s0 + (lane < 8 ? s1 : 0.f)) * (1.0f / D);
      const float var = fmaxf(wave_sum(q0 + (lane < 8 ? q1 : 0.f)) * (1.0f / D) - dm * dm, 0.f);
      const float mb = rbf(x0 + dm), den = rbf(sqrtf(rbf(rbf(var) + 1e-5f)));
#pragma unroll
      for (int i = 0; i < 3; ++i) LF[L_XN + 192 * ta + 64 * i + lane] = rbf(rbf(bflo(xo[i]) - mb) / den);
      LGKM0();
    }
    {
      float acc[8];
#pragma unroll
      for (int e = 0; e < 8; ++e) acc[e] = 0.f;
#pragma unroll
      for (int i = 0; i < 6; ++i) {
        const float xn = LF[L_XN + 192 * ta + 32 * i + (lane >> 1)];
#pragma unroll
        for (int e = 0; e < 4; ++e) { acc[2 * e] = fmaf(xn, bflo(wqr[i][e]), acc[2 * e]); acc[2 * e + 1] = fmaf(xn, bfhi(wqr[i][e]), acc[2 * e + 1]); }
      }
      float u[4], v2[2];
#pragma unroll
      for (int e = 0; e < 4; ++e) u[e] = swap32_add(acc[e], acc[4 + e]);
#pragma unroll
      for (int e = 0; e < 2; ++e) v2[e] = swap16_add(u[e], u[2 + e]);
      const float t1 = v2[0] + dpp<ROR8>(v2[0]), t2 = v2[1] + dpp<ROR8>(v2[1]);
      float z = (lane & 8) ? t2 : t1;
      z += dpp<XOR2>(z);
      const float z1 = dpp<SHL4>(z), z2 = dpp<SHR4>(z);
      z += (lane & 4) ? z2 : z1;
      if ((lane & 6) == 0) LF[L_REDQ + ta * 16 + 8 * (lane & 1) + ((lane >> 3) & 7)] = z;
      LGKM0();
      if (lds_inc(L, L_CNTQ, lane) == 2u) {
        const float qv = (LF[L_REDQ + c16] + LF[L_REDQ + 16 + c16]) + LF[L_REDQ + 32 + c16];
        lds_wait<0>(L, L_TRDY, 1u);
        const unsigned tlo = lds_ld(L, L_TAG + 2), thi = lds_ld(L, L_TAG + 3);
        if (lane < 16) __builtin_amdgcn_raw_buffer_store_b64(v2u{(cvtpk(qv, qv) & 0xffffu) | (tlo << 16), thi}, rws, QG_OFF + (qs * 16 + lane) * 8, 0, AUX_SC1);
        CLOBBER();
        if (lane == 0) L[L_QPUB] = 1u;
      }
    }
    lds_wait<1>(L, L_QPUB, 1u);
    const int vo2 = tt < NCG ? tt * 8 : OOB;
    const int o2 = tt < NCG ? (tt & 7) * 16 : OOB;
    v2u wr[16];
    {
      const __amdgpu_buffer_rsrc_t ro = __builtin_amdgcn_make_buffer_rsrc((void*)(a.wo + (size_t)b * 16 * D * 2), 0, 16 * D * 2, RSRC3);
#pragma unroll
      for (int k = 0; k < 16; ++k) wr[k] = __builtin_amdgcn_raw_buffer_load_b64(ro, vo2, k * D * 2, AUXD);
      __builtin_amdgcn_sched_barrier(0);
    }
    if (is_ffn) {
      const __amdgpu_buffer_rsrc_t ra = __builtin_amdgcn_make_buffer_rsrc((void*)(a.wg + ft * 64), 0, D * FF * 2 - ft * 64, RSRC3);
      const __amdgpu_buffer_rsrc_t rb = __builtin_amdgcn_make_buffer_rsrc((void*)(a.wu + ft * 64), 0, D * FF * 2 - ft * 64, RSRC3);
      const __amdgpu_buffer_rsrc_t rd = __builtin_amdgcn_make_buffer_rsrc((void*)(a.wd + (size_t)ft * 32 * D * 2), 0, 32 * D * 2, RSRC3);
      const int vof = (192 * ta + rr) * (FF * 2) + cc * 16;
      constexpr int fstep = 16 * FF * 2;
      v4u wa[12], wb[12];
      v2u dn[32];
#pragma unroll
      for (int i = 0; i < 12; ++i) {
        wa[i] = __builtin_amdgcn_raw_buffer_load_b128(ra, vof, i * fstep, AUXG);
        wb[i] = __builtin_amdgcn_raw_buffer_load_b128(rb, vof, i * fstep, AUXG);
      }
#pragma unroll
      for (int k = 0; k < 32; ++k) dn[k] = __builtin_amdgcn_raw_buffer_load_b64(rd, vo2, k * D * 2, AUXD);
      __builtin_amdgcn_sched_barrier(0);
      float acc[16];
#pragma unroll
      for (int e = 0; e < 16; ++e) acc[e] = 0.f;
#pragma unroll
      for (int i = 0; i < 12; ++i) {
        const float xn = LF[L_XN + 192 * ta + 16 * i + rr];
#pragma unroll
        for (int e = 0; e < 4; ++e) {
          acc[2 * e] = fmaf(xn, bflo(wa[i][e]), acc[2 * e]); acc[2 * e + 1] = fmaf(xn, bfhi(wa[i][e]), acc[2 * e + 1]);
          acc[8 + 2 * e] = fmaf(xn, bflo(wb[i][e]), acc[8 + 2 * e]); acc[9 + 2 * e] = fmaf(xn, bfhi(wb[i][e]), acc[9 + 2 * e]);
        }
        asm volatile("" : "+v"(acc[0]), "+v"(acc[1]), "+v"(acc[2]), "+v"(acc[3]), "+v"(acc[4]), "+v"(acc[5]), "+v"(acc[6]), "+v"(acc[7]),
                          "+v"(acc[8]), "+v"(acc[9]), "+v"(acc[10]), "+v"(acc[11]), "+v"(acc[12]), "+v"(acc[13]), "+v"(acc[14]), "+v"(acc[15]));
      }
      float u[8], v4[4], z[2];
#pragma unroll
      for (int e = 0; e < 8; ++e) u[e] = swap32_add(acc[e], acc[8 + e]);
#pragma unroll
      for (int e = 0; e < 4; ++e) v4[e] = swap16_add(u[e], u[4 + e]);
#pragma unroll
      for (int e = 0; e < 2; ++e) { const float t1 = v4[e] + dpp<ROR8>(v4[e]), t2 = v4[2 + e] + dpp<ROR8>(v4[2 + e]); z[e] = (lane & 8) ? t2 : t1; }
      const float t1 = z[0] + dpp<SHL4>(z[0]), t2 = z[1] + dpp<SHR4>(z[1]);
      const float zz = (lane & 4) ? t2 : t1;
      LF[L_RED + ta * 64 + (lane & 32) + 8 * cc + ((lane >> 2) & 7)] = zz;
      LGKM0();
      lds_inc(L, L_CNTA, lane);
      lds_wait<0>(L, L_CNTA, 3u);
      const int u5 = lane & 31;
      const float gp = rbf((LF[L_RED + u5] + LF[L_RED + 64 + u5]) + LF[L_RED + 128 + u5]);
      const float up = rbf((LF[L_RED + 32 + u5] + LF[L_RED + 96 + u5]) + LF[L_RED + 160 + u5]);
      const float h = rbf(rbf(silu_f(gp)) * up);
      f32x4 y = {0.f, 0.f, 0.f, 0.f};
#pragma unroll
      for (int k = 0; k < 32; ++k) {
        const float hk = asf(__builtin_amdgcn_readlane(asu(h), k));
        y.x = fmaf(hk, bflo(dn[k].x), y.x); y.y = fmaf(hk, bfhi(dn[k].x), y.y); y.z = fmaf(hk, bflo(dn[k].y), y.z); y.w = fmaf(hk, bfhi(dn[k].y), y.w);
      }
      __builtin_amdgcn_raw_buffer_store_b128(__builtin_bit_cast(v4u, y), rws, tt < NCG ? FP_OFF + ft * (D * 4) + tt * 16 : OOB, 0, AUX_SC1);
    }
    DRAIN();
    lds_inc(L, L_CNTD, lane);
    float wf[64];
#pragma unroll
    for (int k = 0; k < 16; ++k) { wf[4 * k] = bflo(wr[k].x); wf[4 * k + 1] = bfhi(wr[k].x); wf[4 * k + 2] = bflo(wr[k].y); wf[4 * k + 3] = bfhi(wr[k].y); }
#pragma unroll
    for (int k = 0; k < 64; ++k) asm volatile("" : "+v"(wf[k]));
    lds_wait<8>(L, L_CNTS, 4u);
    const unsigned T32 = lds_ld(L, L_TAG);
    unsigned opk = 0;
#pragma nounroll
    for (unsigned sp = 0; sp < (SPIN_CAP << 4); ++sp) {
      const unsigned f = L[L_ORDY];
      opk = L[L_O + (lane & 7)];
      if (__builtin_amdgcn_readfirstlane(f)) break;
    }
    f32x4 y = {0.f, 0.f, 0.f, 0.f};
#pragma unroll
    for (int k = 0; k < 8; ++k) {
      const unsigned sp = __builtin_amdgcn_readlane(opk, k);
      const float e0 = bflo(sp), e1 = bfhi(sp);
      y.x = fmaf(e0, wf[8 * k], y.x); y.y = fmaf(e0, wf[8 * k + 1], y.y); y.z = fmaf(e0, wf[8 * k + 2], y.z); y.w = fmaf(e0, wf[8 * k + 3], y.w);
      y.x = fmaf(e1, wf[8 * k + 4], y.x); y.y = fmaf(e1, wf[8 * k + 5], y.y); y.z = fmaf(e1, wf[8 * k + 6], y.z); y.w = fmaf(e1, wf[8 * k + 7], y.w);
    }
    const int op = P2_OFF + ((tt >> 3) * NWG + b) * 128;
    __builtin_amdgcn_raw_buffer_store_b128(v4u{asu(y.x), T32, asu(y.y), T32}, rws, o2 + op, 0, AUX_SC1);
    __builtin_amdgcn_raw_buffer_store_b128(v4u{asu(y.z), T32, asu(y.w), T32}, rws, o2 + op, 18 * NWG * 128, AUX_SC1);
    lds_inc(L, L_YD, lane);
  } else {
    __builtin_amdgcn_s_setprio(3);
    const v2u ev = __builtin_amdgcn_raw_buffer_load_b64(rws, E_OFF, 0, AUX_SC1);
    const unsigned cv = __builtin_amdgcn_raw_buffer_load_b32(rws, C_OFF + g * 256, 0, AUX_SC1);
    const unsigned cv2 = __builtin_amdgcn_raw_buffer_load_b32(rws, C_OFF + g * 256 + 128, 0, AUX_SC1);
    if (lane < L_NCTL) L[lane] = 0u;
    LGKM0();
    WG_BARRIER();
    const unsigned elo = __builtin_amdgcn_readfirstlane(ev.x), ehi = __builtin_amdgcn_readfirstlane(ev.y);
    const unsigned qlo = (elo ^ 0x7F4A7C15u) & 0xffffu, qhi = ((elo ^ 0x7F4A7C15u) >> 16) | ((ehi ^ 0x9E3779B9u) << 16);
    const unsigned rlo = elo ^ 0xC2B2AE35u, rhi = ehi ^ 0x27D4EB2Fu;
    auto flags_ok = [&](const v4u& f) { return f.x == rlo && f.y == rhi && f.z == rlo && f.w == rhi; };
    if (lane == 0) { L[L_TAG] = (elo ^ 0x85EBCA6Bu) | 1u; L[L_TAG + 1] = cv; L[L_TAG + 6] = cv2; L[L_TAG + 2] = qlo; L[L_TAG + 3] = qhi; L[L_TAG + 4] = elo; L[L_TAG + 5] = ehi; }
    LGKM0();
    if (lane == 0) L[L_TRDY] = 1u;
    {
      const int oq = QG_OFF + g * 2048 + lane * 16;
      v4u q0 = {0u, ~qhi, 0u, 0u}, q1 = {0u, ~qhi, 0u, 0u};
#pragma nounroll
      for (unsigned sp = 0; sp < SPIN_CAP; ++sp) {
        CLOBBER();
        const bool ok0 = q0.y == qhi && q0.w == qhi && (q0.x >> 16) == qlo && (q0.z >> 16) == qlo;
        const bool ok1 = q1.y == qhi && q1.w == qhi && (q1.x >> 16) == qlo && (q1.z >> 16) == qlo;
        if (__all(ok0 && ok1)) break;
        const v4u n0 = __builtin_amdgcn_raw_buffer_load_b128(rws, ok0 ? OOB : oq, 0, AUX_SC1);
        const v4u n1 = __builtin_amdgcn_raw_buffer_load_b128(rws, ok1 ? OOB : oq + 1024, 0, AUX_SC1);
        q0 = ok0 ? q0 : n0; q1 = ok1 ? q1 : n1;
      }
      L[L_Q + lane] = (q0.x & 0xffffu) | (q0.z << 16);
      L[L_Q + 64 + lane] = (q1.x & 0xffffu) | (q1.z << 16);
      LGKM0();
      if (lane == 0) L[L_QRDY] = 1u;
    }
    {
      const v4u zero = {0u, 0u, 0u, 0u};
      __builtin_amdgcn_raw_buffer_store_b128(zero, rws, p1_off, 0, AUX_SC1);
      __builtin_amdgcn_raw_buffer_store_b128(zero, rws, p1_off, P1_H1, AUX_SC1);
#pragma unroll
      for (int i = 0; i < 5; ++i) {
        const int e = i * 8 + (lane >> 3);
        __builtin_amdgcn_raw_buffer_store_b128(zero, rws, e < NFC ? P2_OFF + (e * NWG + b) * 128 + (lane & 7) * 16 : OOB, 0, AUX_SC1);
      }
    }
    DRAIN();
    if (lane == 0) __builtin_amdgcn_raw_buffer_store_b64(v2u{rlo, rhi}, rws, RDY_OFF + b * 8, 0, AUX_SC1);
#pragma nounroll
    for (unsigned sp = 0; sp < SPIN_CAP; ++sp) {
      __builtin_amdgcn_s_sleep(30);
      CLOBBER();
      const v4u f0 = __builtin_amdgcn_raw_buffer_load_b128(rws, RDY_OFF + g * 128 + (lane & 7) * 16, 0, AUX_SC1);
      if (__all(flags_ok(f0))) break;
    }
    CLOBBER();
    if (lane == 0) L[L_GO] = 1u;
    if (is_ffn) {
      lds_wait<8>(L, L_CNTD, 3u);
      if (lane == 0) __builtin_amdgcn_raw_buffer_store_b64(v2u{rlo, rhi}, rws, RDF_OFF + ft * 8, 0, AUX_SC1);
    }
    if (b < NFC) {
#pragma nounroll
      for (unsigned sp = 0; sp < SPIN_CAP; ++sp) {
        __builtin_amdgcn_s_sleep(127);
        CLOBBER();
        const v4u f0 = __builtin_amdgcn_raw_buffer_load_b128(rws, RDF_OFF + lane * 16, 0, AUX_SC1);
        const v4u f1 = __builtin_amdgcn_raw_buffer_load_b128(rws, RDF_OFF + 1024 + (lane & 7) * 16, 0, AUX_SC1);
        const v4u f2 = __builtin_amdgcn_raw_buffer_load_b128(rws, RDY_OFF + lane * 16, 0, AUX_SC1);
        const v4u f3 = __builtin_amdgcn_raw_buffer_load_b128(rws, RDY_OFF + 1024 + lane * 16, 0, AUX_SC1);
        if (__all(flags_ok(f0) && flags_ok(f1) && flags_ok(f2) && flags_ok(f3))) break;
      }
      CLOBBER();
      const int of = FP_OFF + (lane >> 3) * (D * 4) + fl * 128 + (lane & 7) * 16 + fh * 8;
      v2u f[18];
#pragma unroll
      for (int i = 0; i < 18; ++i) f[i] = __builtin_amdgcn_raw_buffer_load_b64(rws, of, i * 8 * D * 4, AUX_SC1);
      const unsigned xv = *(const unsigned*)(a.x + 32 * fl + 4 * (lane & 7) + 2 * fh);
      float y0 = 0.f, y1 = 0.f;
#pragma unroll
      for (int i = 0; i < 18; ++i) { y0 += asf(f[i].x); y1 += asf(f[i].y); }
      y0 += dpp<ROR8>(y0); y1 += dpp<ROR8>(y1);
      y0 = xrow_sum(y0); y1 = xrow_sum(y1);
      if (lane < 8) *(LAS f32x4*)(LF + L_G + 4 * lane) = f32x4{rbf(silu_f(rbf(y0))), rbf(silu_f(rbf(y1))), bflo(xv), bfhi(xv)};
      LGKM0();
      if (lane == 0) L[L_FFR] = 1u;
    }
    lds_wait<16>(L, L_CNTK, 4u);
    if (lane == 0) __builtin_amdgcn_raw_buffer_store_b64(v2u{rlo, rhi}, rws, RDK_OFF + b * 8, 0, AUX_SC1);
#pragma nounroll
    for (unsigned sp = 0; sp < SPIN_CAP; ++sp) {
      __builtin_amdgcn_s_sleep(60);
      CLOBBER();
      const v4u f0 = __builtin_amdgcn_raw_buffer_load_b128(rws, RDK_OFF + g * 128 + (lane & 7) * 16, 0, AUX_SC1);
      if (__all(flags_ok(f0))) break;
    }
    CLOBBER();
    if (lane == 0) L[L_VGO] = 1u;
    lds_wait<4>(L, L_CNTS, 1u);
    lds_wait<2>(L, L_CNTS, 4u);
    {
      v4u p[2];
      poll_lines<2, false>(p, rws, P1_OFF + (g * 16 + j) * 2048 + lane * 16, 0u);
      float M = fmaxf(asf(~p[0].w), asf(~p[1].w));
      M = fmaxf(M, dpp<ROR8>(M)); M = xrow_max(M);
      float Ls = 0.f, o0 = 0.f, o1 = 0.f;
#pragma unroll
      for (int i = 0; i < 2; ++i) {
        const float wt = __builtin_amdgcn_exp2f(asf(~p[i].w) - M);
        Ls = fmaf(wt, asf(p[i].y), Ls); o0 = fmaf(wt, asf(p[i].x), o0); o1 = fmaf(wt, asf(p[i].z), o1);
      }
      Ls += dpp<ROR8>(Ls); o0 += dpp<ROR8>(o0); o1 += dpp<ROR8>(o1);
      Ls = xrow_sum(Ls); o0 = xrow_sum(o0); o1 = xrow_sum(o1);
      const unsigned opk = cvtpk(o0 / Ls, o1 / Ls);
      if (lane < 8) L[L_O + lane] = opk;
      CLOBBER();
      if (lane == 0) L[L_ORDY] = 1u;
    }
  }
}

}

extern "C" void kernel_launch(void* const* d_in, const int* in_sizes, int n_in, void* d_out, int out_size, void* d_ws, size_t ws_size,
                              hipStream_t stream) {
  Args a;
  a.x = (const unsigned short*)d_in[0];
  a.k = (const unsigned char*)d_in[1];
  a.v = (const unsigned char*)d_in[2];
  a.wq = (const unsigned char*)d_in[3];
  a.wo = (const unsigned char*)d_in[4];
  a.wg = (const unsigned char*)d_in[6];
  a.wu = (const unsigned char*)d_in[7];
  a.wd = (const unsigned char*)d_in[8];
  a.out = (unsigned short*)d_out;
  a.ws = (unsigned char*)d_ws;
  hipLaunchKernelGGL(fused_decode_kernel, dim3(NWG), dim3(NTHR), 0, stream, a);
}
```
